```python
import torch

def _softmax(x, axis=-1):
    return torch.softmax(x, dim=axis)
def _gelu(x):
    return torch.nn.functional.gelu(x, approximate="tanh")
def _silu(x):
    return torch.nn.functional.silu(x)
def _sigmoid(x):
    return torch.sigmoid(x)
def _layernorm(x, axis=-1, eps=1e-5):
    m = x.mean(dim=axis, keepdim=True)
    v = x.var(dim=axis, keepdim=True, unbiased=False)
    return (x - m) / torch.sqrt(v + eps)
def _scatter_add(tgt, idx, src, axis=0):
    return tgt.clone().index_add_(axis, idx, src)
def _segment_sum(src, seg, n):
    out = torch.zeros((n,) + src.shape[1:], dtype=src.dtype)
    return out.index_add_(0, seg, src)
def _pad(x, pw):
    flat = tuple(v for pair in reversed(pw) for v in pair)
    return torch.nn.functional.pad(x, flat)

kernel_name = 'gated_mix__spectral_fnet__geglu_ffn__prompt'

def setup_inputs():

    _w = (lambda a: (lambda t: t.to(torch.bfloat16) if t.is_floating_point() else t)(torch.as_tensor(a)))
    def _A_setup():
        B, S, D = 1, 2048, 1024
        ns = torch.arange(S)[:, None] * torch.arange(S)[None, :]
        nd = torch.arange(D)[:, None] * torch.arange(D)[None, :]
        Ws_re = torch.cos(-2.0 * torch.pi * ns / S).to(float)
        Ws_im = torch.sin(-2.0 * torch.pi * ns / S).to(float)
        Wd_re = torch.cos(-2.0 * torch.pi * nd / D).to(float)
        Wd_im = torch.sin(-2.0 * torch.pi * nd / D).to(float)
        return {
            "x": _w((lambda *s: torch.randn(*s, dtype=torch.bfloat16) * 0.05)(B, S, D)),
            "dft_seq_re": _w(Ws_re),
            "dft_seq_im": _w(Ws_im),
            "dft_hid_re": _w(Wd_re),
            "dft_hid_im": _w(Wd_im),
        }
    _A_in = _A_setup()

    def _B_setup():
        B, S, D, F = 1, 2048, 1024, 3584
        return {
            "x": _w((lambda *s: torch.randn(*s, dtype=torch.bfloat16) * 0.05)(B, S, D)),
            "w_gate": _w((lambda *s: torch.randn(*s, dtype=torch.bfloat16) * 0.4)(D, F)),
            "w_up": _w((lambda *s: torch.randn(*s, dtype=torch.bfloat16) * 0.4)(D, F)),
            "w_down": _w((lambda *s: torch.randn(*s, dtype=torch.bfloat16) * 0.4)(F, D)),
        }
    _B_in = _B_setup()

    return {
        "A_x": _A_in["x"],
        "A_dft_seq_re": _A_in["dft_seq_re"],
        "A_dft_seq_im": _A_in["dft_seq_im"],
        "A_dft_hid_re": _A_in["dft_hid_re"],
        "A_dft_hid_im": _A_in["dft_hid_im"],
        "B_x": _B_in["x"],
        "B_w_gate": _B_in["w_gate"],
        "B_w_up": _B_in["w_up"],
        "B_w_down": _B_in["w_down"],
        "gate_w": _w((lambda *s: torch.randn(*s, dtype=torch.bfloat16) * 0.05)(1024) * 16.0),
    }

def reference(A_x, A_dft_seq_re, A_dft_seq_im, A_dft_hid_re, A_dft_hid_im, B_x, B_w_gate, B_w_up, B_w_down, gate_w):
    def _A_ref(x, dft_seq_re, dft_seq_im, dft_hid_re, dft_hid_im):
        a = torch.einsum("bsd,ed->bse", x, dft_hid_re)
        b = torch.einsum("bsd,ed->bse", x, dft_hid_im)
        y = torch.einsum("ts,bse->bte", dft_seq_re, a) - torch.einsum("ts,bse->bte", dft_seq_im, b)
        return y

    def _B_ref(x, w_gate, w_up, w_down):
        return (_gelu(x @ w_gate) * (x @ w_up)) @ w_down

    x = A_x
    g = _sigmoid((x * gate_w).sum(-1, keepdims=True))
    a = _A_ref(x, A_dft_seq_re, A_dft_seq_im, A_dft_hid_re, A_dft_hid_im)
    b = _B_ref(x, B_w_gate, B_w_up, B_w_down)
    return g * a + (1 - g) * b
```

```cpp
#include <hip/hip_runtime.h>
__global__ __launch_bounds__(256, 1) __attribute__((aligned(4096))) void fnet_geglu(const void* x, const void* wsr, const void* wsi, const void* wdr, const void* wdi, const void* wg, const void* wu, const void* wd, const void* gw, void* out, void* ws) {
  __shared__ __attribute__((aligned(16))) char smem[147456];
  const unsigned wave = __builtin_amdgcn_readfirstlane(threadIdx.x >> 6);
  asm volatile(
    "s_load_dwordx16 s[32:47], %0, 0\n"
    "s_load_dwordx4 s[48:51], %0, 64\n"
    "s_load_dwordx2 s[52:53], %0, 80\n"
    "s_mov_b32 s54, %1\n"
    "s_mov_b32 s55, %2\n"
    "v_mbcnt_lo_u32_b32 v0, -1, 0\n"
    "v_mbcnt_hi_u32_b32 v0, -1, v0\n"
    "s_bfe_u32 s56, s54, 0x20001\n"
    "s_and_b32 s94, s54, 1\n"
    "s_lshl_b32 s94, s94, 2\n"
    "s_bfe_u32 s57, s54, 0x20003\n"
    "s_add_u32 s57, s57, s94\n"
    "s_lshr_b32 s58, s54, 5\n"
    "s_getpc_b64 s[12:13]\n"
    "PC:\n"
    "s_and_b32 s13, s13, 0xffff\n"
    "s_mov_b32 s14, K_end-PC\n"
    "s_mov_b32 s15, 0x20000\n"
    "s_bfe_u32 s94, s54, 0x10003\n"
    "s_lshl_b32 s94, s94, 2\n"
    "s_add_u32 s94, s94, s55\n"
    "s_lshl_b32 s94, s94, 13\n"
    "v_lshl_add_u32 v1, v0, 7, s94\n"
    "buffer_load_dword a232, v1, s[12:15], 0 offen\n"
    "v_lshrrev_b32 v1, 4, v0\n"
    "v_add_u32 v1, 0, v1\n"
    "v_and_b32 v2, 7, v0\n"
    "v_xor_b32 v1, v1, v2\n"
    "v_lshlrev_b32 v1, 4, v1\n"
    "v_lshrrev_b32 v2, 3, v0\n"
    "v_and_b32 v3, 4, v2\n"
    "v_add_u32 v2, v2, v3\n"
    "v_lshl_add_u32 v8, v2, 11, v1\n"
    "v_lshrrev_b32 v1, 4, v0\n"
    "v_add_u32 v1, 4, v1\n"
    "v_and_b32 v2, 7, v0\n"
    "v_xor_b32 v1, v1, v2\n"
    "v_lshlrev_b32 v1, 4, v1\n"
    "v_lshrrev_b32 v2, 3, v0\n"
    "v_and_b32 v3, 4, v2\n"
    "v_add_u32 v2, v2, v3\n"
    "v_lshl_add_u32 v9, v2, 11, v1\n"
    "s_cmp_eq_u32 s58, 7\n"
    "s_cselect_b32 s96, 0, s56\n"
    "s_lshl_b32 s94, s55, 1\n"
    "s_add_u32 s95, s94, s96\n"
    "s_add_u32 s95, s95, 0\n"
    "s_and_b32 s95, s95, 3\n"
    "s_and_b32 s94, s94, 4\n"
    "s_add_u32 s94, s94, s95\n"
    "s_lshl_b32 s94, s94, 16\n"
    "s_add_u32 s76, s94, 0\n"
    "s_add_u32 s77, s94, 31744\n"
    "s_add_u32 s78, s94, 6144\n"
    "s_add_u32 s79, s94, 37888\n"
    "s_lshl_b32 s94, s55, 1\n"
    "s_add_u32 s95, s94, s96\n"
    "s_add_u32 s95, s95, 1\n"
    "s_and_b32 s95, s95, 3\n"
    "s_and_b32 s94, s94, 4\n"
    "s_add_u32 s94, s94, s95\n"
    "s_lshl_b32 s94, s94, 16\n"
    "s_add_u32 s80, s94, 0\n"
    "s_add_u32 s81, s94, 31744\n"
    "s_add_u32 s82, s94, 6144\n"
    "s_add_u32 s83, s94, 37888\n"
    "s_lshl_b32 s92, s55, 13\n"
    "s_mov_b32 s93, 69632\n"
    "s_cmp_eq_u32 s58, 7\n"
    "s_cbranch_scc1 P1Z\n"
    "s_mov_b32 s84, 27648\n"
    "s_mov_b32 s85, 55296\n"
    "s_mov_b32 s86, 82944\n"
    "s_mul_i32 s94, s55, 114688\n"
    "v_lshrrev_b32 v1, 4, v0\n"
    "v_or_b32 v2, 0, v1\n"
    "v_lshlrev_b32 v2, 1, v2\n"
    "v_and_b32 v3, 15, v0\n"
    "v_xor_b32 v2, v2, v3\n"
    "v_lshlrev_b32 v2, 4, v2\n"
    "v_mul_u32_u24 v1, 7168, v1\n"
    "v_add3_u32 v10, v1, v2, s94\n"
    "v_lshrrev_b32 v1, 4, v0\n"
    "v_or_b32 v2, 4, v1\n"
    "v_lshlrev_b32 v2, 1, v2\n"
    "v_and_b32 v3, 15, v0\n"
    "v_xor_b32 v2, v2, v3\n"
    "v_lshlrev_b32 v2, 4, v2\n"
    "v_mul_u32_u24 v1, 7168, v1\n"
    "v_add3_u32 v11, v1, v2, s94\n"
    "s_and_b32 s94, s55, 1\n"
    "s_lshl_b32 s94, s94, 2\n"
    "v_lshrrev_b32 v1, 4, v0\n"
    "v_add_u32 v1, s94, v1\n"
    "v_and_b32 v2, 7, v0\n"
    "v_xor_b32 v1, v1, v2\n"
    "v_lshlrev_b32 v1, 4, v1\n"
    "v_lshrrev_b32 v2, 3, v0\n"
    "v_lshl_add_u32 v12, v2, 11, v1\n"
    "s_lshl_b32 s95, s55, 14\n"
    "v_add_u32 v12, s95, v12\n"
    "s_lshl_b32 s98, s55, 12\n"
    "s_lshl_b32 s99, s55, 10\n"
    "s_mul_i32 s94, s56, 7\n"
    "s_add_u32 s10, s58, s94\n"
    "s_lshl_b32 s94, s56, 3\n"
    "s_add_u32 s94, s94, s58\n"
    "s_lshl_b32 s94, s94, 2\n"
    "s_add_u32 s94, s94, s55\n"
    "s_lshl_b32 s95, s57, 7\n"
    "s_add_u32 s11, s94, s95\n"
    "s_lshl_b32 s95, s11, 12\n"
    "v_lshlrev_b32 v1, 5, v0\n"
    "v_add_u32 v2, s95, v1\n"
    "v_mov_b32 v3, 37781504\n"
    "s_lshl_b32 s94, s57, 19\n"
    "s_waitcnt lgkmcnt(0)\n"
    "global_load_dwordx2 v[6:7], v3, s[52:53] sc1\n"
    "global_load_dwordx4 v[22:25], v1, s[48:49]\n"
    "global_load_dwordx4 v[26:29], v1, s[48:49] offset:16\n"
    "global_load_dwordx4 v[32:35], v2, s[32:33]\n"
    "global_load_dwordx4 v[36:39], v2, s[32:33] offset:16\n"
    "global_load_dwordx4 v[40:43], v2, s[32:33] offset:2048\n"
    "global_load_dwordx4 v[44:47], v2, s[32:33] offset:2064\n"
    "s_add_u32 s60, s32, s94\n"
    "s_addc_u32 s61, s33, 0\n"
    "s_and_b32 s61, s61, 0xffff\n"
    "s_mov_b32 s62, 522368\n"
    "s_mov_b32 s63, 0x20000\n"
    "s_cmp_lt_u32 s56, 2\n"
    "s_cselect_b64 s[38:39], s[38:39], s[40:41]\n"
    "s_cselect_b64 s[34:35], s[34:35], s[36:37]\n"
    "s_lshl_b32 s94, s10, 8\n"
    "s_add_u32 s64, s42, s94\n"
    "s_addc_u32 s65, s43, 0\n"
    "s_and_b32 s65, s65, 0xffff\n"
    "s_mov_b32 s66, 451840\n"
    "s_mov_b32 s67, 0x20000\n"
    "s_add_u32 s68, s44, s94\n"
    "s_addc_u32 s69, s45, 0\n"
    "s_and_b32 s69, s69, 0xffff\n"
    "s_mov_b32 s70, 451840\n"
    "s_mov_b32 s71, 0x20000\n"
    "s_and_b32 s94, s56, 1\n"
    "s_mul_i32 s94, s94, 7\n"
    "s_add_u32 s91, s94, s58\n"
    "s_lshl_b32 s94, s91, 16\n"
    "s_add_u32 s72, s38, s94\n"
    "s_addc_u32 s73, s39, 0\n"
    "s_and_b32 s73, s73, 0xffff\n"
    "s_mov_b32 s74, 63616\n"
    "s_mov_b32 s75, 0x20000\n"
    "s_add_u32 m0, s92, 0\n"
    "s_nop 0\n"
    "buffer_load_dwordx4 v8, s[60:63], s76 offen offset:0  lds\n"
    "buffer_load_dwordx4 v9, s[60:63], s77 offen offset:1024  lds\n"
    "buffer_load_dwordx4 v8, s[60:63], s78 offen offset:2048  lds\n"
    "buffer_load_dwordx4 v9, s[60:63], s79 offen offset:3072  lds\n"
    "v_lshlrev_b32 v21, 4, v0\n"
    "s_lshr_b32 s94, s55, 1\n"
    "s_lshl_b32 s94, s94, 14\n"
    "v_and_b32 v1, 15, v0\n"
    "v_lshrrev_b32 v2, 4, v0\n"
    "s_add_u32 m0, s92, 4096\n"
    "s_nop 0\n"
    "buffer_load_dwordx4 v8, s[60:63], s80 offen offset:0  lds\n"
    "v_lshrrev_b32 v3, 1, v1\n"
    "v_xor_b32 v2, v2, v3\n"
    "v_lshlrev_b32 v2, 4, v2\n"
    "v_lshl_add_u32 v2, v1, 7, v2\n"
    "v_add_u32 v13, s94, v2\n"
    "v_xor_b32 v2, 64, v2\n"
    "buffer_load_dwordx4 v9, s[60:63], s81 offen offset:1024  lds\n"
    "v_add_u32 v14, s94, v2\n"
    "s_and_b32 s94, s55, 1\n"
    "s_lshl_b32 s94, s94, 2\n"
    "v_lshrrev_b32 v1, 4, v0\n"
    "v_bfe_u32 v2, v0, 2, 2\n"
    "buffer_load_dwordx4 v8, s[60:63], s82 offen offset:2048  lds\n"
    "v_lshl_add_u32 v3, v1, 3, v2\n"
    "v_and_b32 v1, 1, v1\n"
    "v_lshl_add_u32 v1, v1, 2, v2\n"
    "v_xor_b32 v1, s94, v1\n"
    "v_and_b32 v2, 3, v0\n"
    "v_lshlrev_b32 v2, 3, v2\n"
    "buffer_load_dwordx4 v9, s[60:63], s83 offen offset:3072  lds\n"
    "v_lshl_add_u32 v3, v3, 8, v2\n"
    "v_add_u32 v3, 32768, v3\n"
    "v_xor_b32 v2, 0, v1\n"
    "v_lshl_add_u32 v15, v2, 5, v3\n"
    "v_xor_b32 v2, 1, v1\n"
    "s_add_u32 m0, s98, 32768\n"
    "s_nop 0\n"
    "buffer_load_dwordx4 v10, s[64:67], 0 offen offset:0  lds\n"
    "v_lshl_add_u32 v16, v2, 5, v3\n"
    "v_xor_b32 v2, 2, v1\n"
    "v_lshl_add_u32 v17, v2, 5, v3\n"
    "v_xor_b32 v2, 3, v1\n"
    "v_lshl_add_u32 v18, v2, 5, v3\n"
    "s_lshl_b32 s94, s94, 9\n"
    "buffer_load_dwordx4 v10, s[64:67], s84 offen offset:1024  lds\n"
    "s_add_u32 s94, s94, 65536\n"
    "v_and_b32 v1, 15, v0\n"
    "v_lshrrev_b32 v2, 4, v0\n"
    "v_lshrrev_b32 v3, 1, v1\n"
    "v_xor_b32 v2, v2, v3\n"
    "v_lshlrev_b32 v2, 4, v2\n"
    "buffer_load_dwordx4 v11, s[64:67], s85 offen offset:2048  lds\n"
    "v_lshl_add_u32 v2, v1, 7, v2\n"
    "v_add_u32 v19, s94, v2\n"
    "v_xor_b32 v2, 64, v2\n"
    "v_add_u32 v20, s94, v2\n"
    "v_and_b32 v1, 15, v0\n"
    "buffer_load_dwordx4 v11, s[64:67], s86 offen offset:3072  lds\n"
    "v_and_b32 v2, 12, v0\n"
    "s_lshl_b32 s94, s57, 1\n"
    "s_lshr_b32 s95, s55, 1\n"
    "s_add_u32 s94, s94, s95\n"
    "s_lshl_b32 s94, s94, 7\n"
    "v_add3_u32 v31, v1, v2, s94\n"
    "s_add_u32 m0, s98, 49152\n"
    "s_nop 0\n"
    "buffer_load_dwordx4 v10, s[68:71], 0 offen offset:0  lds\n"
    "v_lshrrev_b32 v2, 4, v0\n"
    "v_lshlrev_b32 v1, 7, v1\n"
    "v_lshl_add_u32 v30, v2, 24, v1\n"
    "s_lshr_b32 s94, s54, 3\n"
    "s_lshl_b32 s95, s56, 5\n"
    "buffer_load_dwordx4 v10, s[68:71], s84 offen offset:1024  lds\n"
    "s_add_u32 s94, s94, s95\n"
    "s_lshl_b32 s94, s94, 1\n"
    "s_lshr_b32 s95, s55, 1\n"
    "s_add_u32 s94, s94, s95\n"
    "s_mul_i32 s94, s94, 14\n"
    "s_and_b32 s95, s55, 1\n"
    "buffer_load_dwordx4 v11, s[68:71], s85 offen offset:2048  lds\n"
    "s_add_u32 s94, s94, s95\n"
    "s_lshl_b32 s94, s94, 11\n"
    "s_add_u32 s12, s46, s94\n"
    "s_addc_u32 s13, s47, 0\n"
    "s_and_b32 s13, s13, 0xffff\n"
    "s_mov_b32 s14, 65536\n"
    "buffer_load_dwordx4 v11, s[68:71], s86 offen offset:3072  lds\n"
    "s_mov_b32 s15, 0x20000\n"
    "s_lshl_b32 s94, s57, 3\n"
    "s_add_u32 s94, s94, s58\n"
    "s_lshl_b32 s94, s94, 2\n"
    "s_add_u32 s94, s94, s55\n"
    "s_lshl_b32 s94, s94, 4\n"
    "s_add_u32 m0, s99, 65536\n"
    "s_nop 0\n"
    "buffer_load_dwordx4 v12, s[72:75], 0 offen offset:0  lds\n"
    "s_and_b32 s95, s56, 1\n"
    "s_add_u32 s94, s94, s95\n"
    "s_lshl_b32 s94, s94, 11\n"
    "s_add_u32 s4, s34, s94\n"
    "s_addc_u32 s5, s35, 0\n"
    "s_sub_u32 s4, s4, 28672\n"
    "s_subb_u32 s5, s5, 0\n"
    "s_and_b32 s5, s5, 0xffff\n"
    "s_add_u32 s60, s60, 128\n"
    "s_addc_u32 s61, s61, 0\n"
    "s_add_u32 s64, s64, 458752\n"
    "s_addc_u32 s65, s65, 0\n"
    "s_add_u32 s68, s68, 458752\n"
    "s_addc_u32 s69, s69, 0\n"
    "s_add_u32 s72, s72, 128\n"
    "s_addc_u32 s73, s73, 0\n"
    "s_add_u32 s92, s92, s93\n"
    "s_add_u32 s98, s98, s93\n"
    "s_add_u32 s99, s99, s93\n"
    "s_sub_u32 s93, 0, s93\n"
    "s_mov_b64 exec, 0\n"
    "s_cmp_eq_u32 s55, 0\n"
    "s_cbranch_scc1 TC0\n"
    "s_cmp_eq_u32 s55, 1\n"
    "s_cbranch_scc1 TC1\n"
    "s_cmp_eq_u32 s55, 2\n"
    "s_cbranch_scc1 TC2\n"
    "s_branch TC3\n"
    "TC13:\n"
    "TC14:\n"
    "TC15:\n"
    "TC16:\n"
    "TC_ret:\n"
    "s_cmp_eq_u32 s58, 7\n"
    "s_cbranch_scc1 TC_retZ\n"
    "s_mov_b64 exec, -1\n"
    "s_waitcnt vmcnt(17)\n"
    "v_readfirstlane_b32 s94, v6\n"
    "v_readfirstlane_b32 s95, v7\n"
    "v_mov_b32 v4, 0\n"
    "v_mov_b32 v5, 0\n"
    "s_mul_i32 s9, s95, 0x4c957f2d\n"
    "s_mul_hi_u32 s95, s94, 0x4c957f2d\n"
    "s_add_u32 s9, s9, s95\n"
    "s_mul_i32 s95, s94, 0x5851f42d\n"
    "s_add_u32 s9, s9, s95\n"
    "s_mul_i32 s8, s94, 0x4c957f2d\n"
    "s_add_u32 s8, s8, 0xf767814f\n"
    "s_addc_u32 s9, s9, 0x14057b7e\n"
    "v_dot2_f32_bf16 v4, v32, v22, v4\n"
    "v_dot2_f32_bf16 v5, v40, v22, v5\n"
    "v_dot2_f32_bf16 v4, v33, v23, v4\n"
    "v_dot2_f32_bf16 v5, v41, v23, v5\n"
    "v_dot2_f32_bf16 v4, v34, v24, v4\n"
    "v_dot2_f32_bf16 v5, v42, v24, v5\n"
    "v_dot2_f32_bf16 v4, v35, v25, v4\n"
    "v_dot2_f32_bf16 v5, v43, v25, v5\n"
    "v_dot2_f32_bf16 v4, v36, v26, v4\n"
    "v_dot2_f32_bf16 v5, v44, v26, v5\n"
    "v_dot2_f32_bf16 v4, v37, v27, v4\n"
    "v_dot2_f32_bf16 v5, v45, v27, v5\n"
    "v_dot2_f32_bf16 v4, v38, v28, v4\n"
    "v_dot2_f32_bf16 v5, v46, v28, v5\n"
    "v_dot2_f32_bf16 v4, v39, v29, v4\n"
    "v_dot2_f32_bf16 v5, v47, v29, v5\n"
    "s_nop 2\n"
    "v_add_f32_dpp v4, v4, v4 row_ror:8 row_mask:0xf bank_mask:0xf\n"
    "v_add_f32_dpp v5, v5, v5 row_ror:8 row_mask:0xf bank_mask:0xf\n"
    "s_nop 0\n"
    "v_add_f32_dpp v4, v4, v4 row_ror:4 row_mask:0xf bank_mask:0xf\n"
    "v_add_f32_dpp v5, v5, v5 row_ror:4 row_mask:0xf bank_mask:0xf\n"
    "s_nop 0\n"
    "v_add_f32_dpp v4, v4, v4 row_ror:2 row_mask:0xf bank_mask:0xf\n"
    "v_add_f32_dpp v5, v5, v5 row_ror:2 row_mask:0xf bank_mask:0xf\n"
    "s_nop 0\n"
    "v_add_f32_dpp v4, v4, v4 row_ror:1 row_mask:0xf bank_mask:0xf\n"
    "v_add_f32_dpp v5, v5, v5 row_ror:1 row_mask:0xf bank_mask:0xf\n"
    "s_nop 0\n"
    "v_mov_b32 v6, v4\n"
    "v_mov_b32 v7, v5\n"
    "s_nop 1\n"
    "v_permlane16_swap_b32 v4, v6\n"
    "v_permlane16_swap_b32 v5, v7\n"
    "s_nop 1\n"
    "v_add_f32 v4, v4, v6\n"
    "v_add_f32 v5, v5, v7\n"
    "v_mov_b32 v6, v4\n"
    "v_mov_b32 v7, v5\n"
    "s_nop 1\n"
    "v_permlane32_swap_b32 v4, v6\n"
    "v_permlane32_swap_b32 v5, v7\n"
    "s_nop 1\n"
    "v_add_f32 v4, v4, v6\n"
    "v_add_f32 v5, v5, v7\n"
    "v_cmp_eq_u32 vcc, 0, v0\n"
    "s_lshl_b32 s95, s11, 4\n"
    "s_add_u32 s95, s95, 37748736\n"
    "v_cndmask_b32 v4, v5, v4, vcc\n"
    "v_mul_f32 v4, 0x3fb8aa3b, v4\n"
    "v_exp_f32 v4, v4\n"
    "v_lshl_add_u32 v1, v0, 3, s95\n"
    "v_mov_b32 v5, s8\n"
    "v_cmp_lt_f32 vcc, 0x7149f2ca, v4\n"
    "v_mov_b32 v2, 0x7149f2ca\n"
    "s_nop 0\n"
    "v_cndmask_b32 v4, v4, v2, vcc\n"
    "s_mov_b64 exec, 3\n"
    "global_store_dwordx2 v1, v[4:5], s[52:53] sc1\n"
    "s_mov_b64 exec, -1\n"
    "s_waitcnt vmcnt(1)\n"
    "s_barrier\n"
    "ds_read_b128 a[64:67], v13 offset:0\n"
    "ds_read_b64_tr_b16 a[96:97], v15 offset:0\n"
    "ds_read_b64_tr_b16 a[98:99], v15 offset:1024\n"
    "ds_read_b64_tr_b16 a[100:101], v16 offset:0\n"
    "ds_read_b64_tr_b16 a[102:103], v16 offset:1024\n"
    "ds_read_b64_tr_b16 a[104:105], v17 offset:0\n"
    "ds_read_b64_tr_b16 a[106:107], v17 offset:1024\n"
    "ds_read_b64_tr_b16 a[108:109], v18 offset:0\n"
    "ds_read_b64_tr_b16 a[110:111], v18 offset:1024\n"
    "ds_read_b64_tr_b16 a[112:113], v15 offset:16384\n"
    "ds_read_b64_tr_b16 a[114:115], v15 offset:17408\n"
    "ds_read_b64_tr_b16 a[116:117], v16 offset:16384\n"
    "ds_read_b64_tr_b16 a[118:119], v16 offset:17408\n"
    "ds_read_b64_tr_b16 a[120:121], v17 offset:16384\n"
    "ds_read_b64_tr_b16 a[122:123], v17 offset:17408\n"
    "ds_read_b64_tr_b16 a[124:125], v18 offset:16384\n"
    "ds_read_b64_tr_b16 a[126:127], v18 offset:17408\n"
    "ds_read_b128 a[128:131], v19\n"
    "ds_read_b128 a[68:71], v13 offset:2048\n"
    "ds_read_b128 a[72:75], v13 offset:4096\n"
    "ds_read_b128 a[76:79], v13 offset:6144\n"
    "ds_read_b128 a[80:83], v13 offset:8192\n"
    "ds_read_b128 a[84:87], v13 offset:10240\n"
    "ds_read_b128 a[88:91], v13 offset:12288\n"
    "ds_read_b128 a[92:95], v13 offset:14336\n"
    "s_waitcnt lgkmcnt(7)\n"
    "s_add_u32 m0, s92, 0\n"
    "v_mfma_f32_16x16x32_bf16 v[32:35], a[96:99], a[64:67], 0\n"
    "buffer_load_dwordx4 v8, s[60:63], s76 offen offset:0  lds\n"
    "v_mfma_f32_16x16x32_bf16 v[36:39], a[100:103], a[64:67], 0\n"
    "v_mfma_f32_16x16x32_bf16 v[40:43], a[104:107], a[64:67], 0\n"
    "v_mfma_f32_16x16x32_bf16 v[44:47], a[108:111], a[64:67], 0\n"
    "buffer_load_dwordx4 v9, s[60:63], s77 offen offset:1024  lds\n"
    "v_mfma_f32_16x16x32_bf16 v[160:163], a[112:115], a[64:67], 0\n"
    "v_mfma_f32_16x16x32_bf16 v[164:167], a[116:119], a[64:67], 0\n"
    "v_mfma_f32_16x16x32_bf16 v[168:171], a[120:123], a[64:67], 0\n"
    "buffer_load_dwordx4 v8, s[60:63], s78 offen offset:2048  lds\n"
    "v_mfma_f32_16x16x32_bf16 v[172:175], a[124:127], a[64:67], 0\n"
    "v_mfma_f32_16x16x32_bf16 a[32:35], a[64:67], a[128:131], 0\n"
    "s_waitcnt lgkmcnt(0)\n"
    "ds_read_b128 a[132:135], v14 offset:0\n"
    "v_mfma_f32_16x16x32_bf16 a[36:39], a[68:71], a[128:131], 0\n"
    "ds_read_b64_tr_b16 a[164:165], v15 offset:8192\n"
    "buffer_load_dwordx4 v9, s[60:63], s79 offen offset:3072  lds\n"
    "v_mfma_f32_16x16x32_bf16 v[188:191], a[124:127], a[68:71], 0\n"
    "ds_read_b64_tr_b16 a[166:167], v15 offset:9216\n"
    "v_mfma_f32_16x16x32_bf16 v[184:187], a[120:123], a[68:71], 0\n"
    "ds_read_b64_tr_b16 a[168:169], v16 offset:8192\n"
    "s_add_u32 m0, s92, 4096\n"
    "v_mfma_f32_16x16x32_bf16 v[180:183], a[116:119], a[68:71], 0\n"
    "ds_read_b64_tr_b16 a[170:171], v16 offset:9216\n"
    "buffer_load_dwordx4 v8, s[60:63], s80 offen offset:0  lds\n"
    "v_mfma_f32_16x16x32_bf16 v[176:179], a[112:115], a[68:71], 0\n"
    "ds_read_b64_tr_b16 a[172:173], v17 offset:8192\n"
    "v_mfma_f32_16x16x32_bf16 v[60:63], a[108:111], a[68:71], 0\n"
    "ds_read_b64_tr_b16 a[174:175], v17 offset:9216\n"
    "v_mfma_f32_16x16x32_bf16 v[56:59], a[104:107], a[68:71], 0\n"
    "ds_read_b64_tr_b16 a[176:177], v18 offset:8192\n"
    "buffer_load_dwordx4 v9, s[60:63], s81 offen offset:1024  lds\n"
    "v_mfma_f32_16x16x32_bf16 v[52:55], a[100:103], a[68:71], 0\n"
    "ds_read_b64_tr_b16 a[178:179], v18 offset:9216\n"
    "v_mfma_f32_16x16x32_bf16 v[48:51], a[96:99], a[68:71], 0\n"
    "ds_read_b64_tr_b16 a[180:181], v15 offset:24576\n"
    "v_mfma_f32_16x16x32_bf16 v[64:67], a[96:99], a[72:75], 0\n"
    "ds_read_b64_tr_b16 a[182:183], v15 offset:25600\n"
    "buffer_load_dwordx4 v8, s[60:63], s82 offen offset:2048  lds\n"
    "v_mfma_f32_16x16x32_bf16 v[68:71], a[100:103], a[72:75], 0\n"
    "ds_read_b64_tr_b16 a[184:185], v16 offset:24576\n"
    "v_mfma_f32_16x16x32_bf16 v[72:75], a[104:107], a[72:75], 0\n"
    "ds_read_b64_tr_b16 a[186:187], v16 offset:25600\n"
    "v_mfma_f32_16x16x32_bf16 v[76:79], a[108:111], a[72:75], 0\n"
    "ds_read_b64_tr_b16 a[188:189], v17 offset:24576\n"
    "buffer_load_dwordx4 v9, s[60:63], s83 offen offset:3072  lds\n"
    "v_mfma_f32_16x16x32_bf16 v[192:195], a[112:115], a[72:75], 0\n"
    "ds_read_b64_tr_b16 a[190:191], v17 offset:25600\n"
    "v_mfma_f32_16x16x32_bf16 v[196:199], a[116:119], a[72:75], 0\n"
    "ds_read_b64_tr_b16 a[192:193], v18 offset:24576\n"
    "s_add_u32 m0, s98, 32768\n"
    "v_mfma_f32_16x16x32_bf16 v[200:203], a[120:123], a[72:75], 0\n"
    "ds_read_b64_tr_b16 a[194:195], v18 offset:25600\n"
    "buffer_load_dwordx4 v10, s[64:67], 0 offen offset:0  lds\n"
    "v_mfma_f32_16x16x32_bf16 v[204:207], a[124:127], a[72:75], 0\n"
    "ds_read_b128 a[196:199], v20\n"
    "v_mfma_f32_16x16x32_bf16 a[40:43], a[72:75], a[128:131], 0\n"
    "ds_read_b128 a[136:139], v14 offset:2048\n"
    "v_mfma_f32_16x16x32_bf16 a[44:47], a[76:79], a[128:131], 0\n"
    "ds_read_b128 a[140:143], v14 offset:4096\n"
    "buffer_load_dwordx4 v10, s[64:67], s84 offen offset:1024  lds\n"
    "v_mfma_f32_16x16x32_bf16 v[220:223], a[124:127], a[76:79], 0\n"
    "ds_read_b128 a[144:147], v14 offset:6144\n"
    "v_mfma_f32_16x16x32_bf16 v[216:219], a[120:123], a[76:79], 0\n"
    "ds_read_b128 a[148:151], v14 offset:8192\n"
    "v_mfma_f32_16x16x32_bf16 v[212:215], a[116:119], a[76:79], 0\n"
    "ds_read_b128 a[152:155], v14 offset:10240\n"
    "buffer_load_dwordx4 v11, s[64:67], s85 offen offset:2048  lds\n"
    "v_mfma_f32_16x16x32_bf16 v[208:211], a[112:115], a[76:79], 0\n"
    "ds_read_b128 a[156:159], v14 offset:12288\n"
    "v_mfma_f32_16x16x32_bf16 v[92:95], a[108:111], a[76:79], 0\n"
    "ds_read_b128 a[160:163], v14 offset:14336\n"
    "v_mfma_f32_16x16x32_bf16 v[88:91], a[104:107], a[76:79], 0\n"
    "buffer_load_dwordx4 v11, s[64:67], s86 offen offset:3072  lds\n"
    "v_mfma_f32_16x16x32_bf16 v[84:87], a[100:103], a[76:79], 0\n"
    "v_mfma_f32_16x16x32_bf16 v[80:83], a[96:99], a[76:79], 0\n"
    "s_add_u32 m0, s98, 49152\n"
    "v_mfma_f32_16x16x32_bf16 v[96:99], a[96:99], a[80:83], 0\n"
    "buffer_load_dwordx4 v10, s[68:71], 0 offen offset:0  lds\n"
    "v_mfma_f32_16x16x32_bf16 v[100:103], a[100:103], a[80:83], 0\n"
    "v_mfma_f32_16x16x32_bf16 v[104:107], a[104:107], a[80:83], 0\n"
    "v_mfma_f32_16x16x32_bf16 v[108:111], a[108:111], a[80:83], 0\n"
    "buffer_load_dwordx4 v10, s[68:71], s84 offen offset:1024  lds\n"
    "v_mfma_f32_16x16x32_bf16 v[224:227], a[112:115], a[80:83], 0\n"
    "v_mfma_f32_16x16x32_bf16 v[228:231], a[116:119], a[80:83], 0\n"
    "s_waitcnt lgkmcnt(0)\n"
    "s_barrier\n"
    "v_mfma_f32_16x16x32_bf16 v[232:235], a[120:123], a[80:83], 0\n"
    "buffer_load_dwordx4 v11, s[68:71], s85 offen offset:2048  lds\n"
    "v_mfma_f32_16x16x32_bf16 v[236:239], a[124:127], a[80:83], 0\n"
    "v_mfma_f32_16x16x32_bf16 a[48:51], a[80:83], a[128:131], 0\n"
    "v_mfma_f32_16x16x32_bf16 a[52:55], a[84:87], a[128:131], 0\n"
    "buffer_load_dwordx4 v11, s[68:71], s86 offen offset:3072  lds\n"
    "v_mfma_f32_16x16x32_bf16 v[252:255], a[124:127], a[84:87], 0\n"
    "v_mfma_f32_16x16x32_bf16 v[248:251], a[120:123], a[84:87], 0\n"
    "s_add_u32 m0, s99, 65536\n"
    "v_mfma_f32_16x16x32_bf16 v[244:247], a[116:119], a[84:87], 0\n"
    "buffer_load_dwordx4 v12, s[72:75], 0 offen offset:0  lds\n"
    "v_mfma_f32_16x16x32_bf16 v[240:243], a[112:115], a[84:87], 0\n"
    "s_add_u32 s60, s60, 128\n"
    "s_addc_u32 s61, s61, 0\n"
    "s_add_u32 s64, s64, 458752\n"
    "s_addc_u32 s65, s65, 0\n"
    "v_mfma_f32_16x16x32_bf16 v[124:127], a[108:111], a[84:87], 0\n"
    "s_add_u32 s68, s68, 458752\n"
    "s_addc_u32 s69, s69, 0\n"
    "v_mfma_f32_16x16x32_bf16 v[120:123], a[104:107], a[84:87], 0\n"
    "s_add_u32 s72, s72, 128\n"
    "s_addc_u32 s73, s73, 0\n"
    "v_mfma_f32_16x16x32_bf16 v[116:119], a[100:103], a[84:87], 0\n"
    "s_add_u32 s92, s92, s93\n"
    "s_add_u32 s98, s98, s93\n"
    "v_mfma_f32_16x16x32_bf16 v[112:115], a[96:99], a[84:87], 0\n"
    "s_add_u32 s99, s99, s93\n"
    "s_sub_u32 s93, 0, s93\n"
    "v_mfma_f32_16x16x32_bf16 v[128:131], a[96:99], a[88:91], 0\n"
    "s_add_u32 m0, s92, 0\n"
    "v_mfma_f32_16x16x32_bf16 v[132:135], a[100:103], a[88:91], 0\n"
    "buffer_load_dwordx4 v8, s[60:63], s76 offen offset:0  lds\n"
    "v_mfma_f32_16x16x32_bf16 v[136:139], a[104:107], a[88:91], 0\n"
    "v_add_u32 v13, s93, v13\n"
    "v_add_u32 v14, s93, v14\n"
    "v_mfma_f32_16x16x32_bf16 v[140:143], a[108:111], a[88:91], 0\n"
    "v_add_u32 v15, s93, v15\n"
    "v_add_u32 v16, s93, v16\n"
    "v_mfma_f32_16x16x32_bf16 a[0:3], a[112:115], a[88:91], 0\n"
    "v_add_u32 v17, s93, v17\n"
    "v_add_u32 v18, s93, v18\n"
    "v_mfma_f32_16x16x32_bf16 a[4:7], a[116:119], a[88:91], 0\n"
    "v_add_u32 v19, s93, v19\n"
    "v_add_u32 v20, s93, v20\n"
    "v_mfma_f32_16x16x32_bf16 a[8:11], a[120:123], a[88:91], 0\n"
    "buffer_load_dwordx4 v9, s[60:63], s77 offen offset:1024  lds\n"
    "v_mfma_f32_16x16x32_bf16 a[12:15], a[124:127], a[88:91], 0\n"
    "v_mfma_f32_16x16x32_bf16 a[56:59], a[88:91], a[128:131], 0\n"
    "v_mfma_f32_16x16x32_bf16 a[60:63], a[92:95], a[128:131], 0\n"
    "v_mfma_f32_16x16x32_bf16 a[28:31], a[124:127], a[92:95], 0\n"
    "v_mfma_f32_16x16x32_bf16 a[24:27], a[120:123], a[92:95], 0\n"
    "buffer_load_dwordx4 v8, s[60:63], s78 offen offset:2048  lds\n"
    "v_mfma_f32_16x16x32_bf16 a[20:23], a[116:119], a[92:95], 0\n"
    "v_mfma_f32_16x16x32_bf16 a[16:19], a[112:115], a[92:95], 0\n"
    "v_mfma_f32_16x16x32_bf16 v[156:159], a[108:111], a[92:95], 0\n"
    "v_mfma_f32_16x16x32_bf16 v[152:155], a[104:107], a[92:95], 0\n"
    "v_mfma_f32_16x16x32_bf16 v[148:151], a[100:103], a[92:95], 0\n"
    "buffer_load_dwordx4 v9, s[60:63], s79 offen offset:3072  lds\n"
    "v_mfma_f32_16x16x32_bf16 v[144:147], a[96:99], a[92:95], 0\n"
    "v_mfma_f32_16x16x32_bf16 v[32:35], a[164:167], a[132:135], v[32:35]\n"
    "v_mfma_f32_16x16x32_bf16 v[36:39], a[168:171], a[132:135], v[36:39]\n"
    "v_mfma_f32_16x16x32_bf16 v[40:43], a[172:175], a[132:135], v[40:43]\n"
    "s_add_u32 m0, s92, 4096\n"
    "v_mfma_f32_16x16x32_bf16 v[44:47], a[176:179], a[132:135], v[44:47]\n"
    "buffer_load_dwordx4 v8, s[60:63], s80 offen offset:0  lds\n"
    "v_mfma_f32_16x16x32_bf16 v[160:163], a[180:183], a[132:135], v[160:163]\n"
    "v_mfma_f32_16x16x32_bf16 v[164:167], a[184:187], a[132:135], v[164:167]\n"
    "v_mfma_f32_16x16x32_bf16 v[168:171], a[188:191], a[132:135], v[168:171]\n"
    "v_mfma_f32_16x16x32_bf16 v[172:175], a[192:195], a[132:135], v[172:175]\n"
    "v_mfma_f32_16x16x32_bf16 a[32:35], a[132:135], a[196:199], a[32:35]\n"
    "buffer_load_dwordx4 v9, s[60:63], s81 offen offset:1024  lds\n"
    "v_mfma_f32_16x16x32_bf16 a[36:39], a[136:139], a[196:199], a[36:39]\n"
    "v_mfma_f32_16x16x32_bf16 v[188:191], a[192:195], a[136:139], v[188:191]\n"
    "v_mfma_f32_16x16x32_bf16 v[184:187], a[188:191], a[136:139], v[184:187]\n"
    "v_mfma_f32_16x16x32_bf16 v[180:183], a[184:187], a[136:139], v[180:183]\n"
    "v_mfma_f32_16x16x32_bf16 v[176:179], a[180:183], a[136:139], v[176:179]\n"
    "buffer_load_dwordx4 v8, s[60:63], s82 offen offset:2048  lds\n"
    "v_mfma_f32_16x16x32_bf16 v[60:63], a[176:179], a[136:139], v[60:63]\n"
    "v_mfma_f32_16x16x32_bf16 v[56:59], a[172:175], a[136:139], v[56:59]\n"
    "v_mfma_f32_16x16x32_bf16 v[52:55], a[168:171], a[136:139], v[52:55]\n"
    "v_mfma_f32_16x16x32_bf16 v[48:51], a[164:167], a[136:139], v[48:51]\n"
    "v_mfma_f32_16x16x32_bf16 v[64:67], a[164:167], a[140:143], v[64:67]\n"
    "buffer_load_dwordx4 v9, s[60:63], s83 offen offset:3072  lds\n"
    "v_mfma_f32_16x16x32_bf16 v[68:71], a[168:171], a[140:143], v[68:71]\n"
    "v_mfma_f32_16x16x32_bf16 v[72:75], a[172:175], a[140:143], v[72:75]\n"
    "v_mfma_f32_16x16x32_bf16 v[76:79], a[176:179], a[140:143], v[76:79]\n"
    "v_mfma_f32_16x16x32_bf16 v[192:195], a[180:183], a[140:143], v[192:195]\n"
    "s_add_u32 m0, s98, 32768\n"
    "v_mfma_f32_16x16x32_bf16 v[196:199], a[184:187], a[140:143], v[196:199]\n"
    "buffer_load_dwordx4 v10, s[64:67], 0 offen offset:0  lds\n"
    "v_mfma_f32_16x16x32_bf16 v[200:203], a[188:191], a[140:143], v[200:203]\n"
    "v_mfma_f32_16x16x32_bf16 v[204:207], a[192:195], a[140:143], v[204:207]\n"
    "v_mfma_f32_16x16x32_bf16 a[40:43], a[140:143], a[196:199], a[40:43]\n"
    "v_mfma_f32_16x16x32_bf16 a[44:47], a[144:147], a[196:199], a[44:47]\n"
    "v_mfma_f32_16x16x32_bf16 v[220:223], a[192:195], a[144:147], v[220:223]\n"
    "buffer_load_dwordx4 v10, s[64:67], s84 offen offset:1024  lds\n"
    "v_mfma_f32_16x16x32_bf16 v[216:219], a[188:191], a[144:147], v[216:219]\n"
    "v_mfma_f32_16x16x32_bf16 v[212:215], a[184:187], a[144:147], v[212:215]\n"
    "v_mfma_f32_16x16x32_bf16 v[208:211], a[180:183], a[144:147], v[208:211]\n"
    "v_mfma_f32_16x16x32_bf16 v[92:95], a[176:179], a[144:147], v[92:95]\n"
    "v_mfma_f32_16x16x32_bf16 v[88:91], a[172:175], a[144:147], v[88:91]\n"
    "s_waitcnt vmcnt(10)\n"
    "s_barrier\n"
    "buffer_load_dwordx4 v11, s[64:67], s85 offen offset:2048  lds\n"
    "v_mfma_f32_16x16x32_bf16 v[84:87], a[168:171], a[144:147], v[84:87]\n"
    "v_mfma_f32_16x16x32_bf16 v[80:83], a[164:167], a[144:147], v[80:83]\n"
    "ds_read_b128 a[64:67], v13 offset:0\n"
    "buffer_load_dword a232, v30, s[12:15], 0 offen\n"
    "v_mfma_f32_16x16x32_bf16 v[96:99], a[164:167], a[148:151], v[96:99]\n"
    "ds_read_b64_tr_b16 a[96:97], v15 offset:0\n"
    "v_add_u32 v30, 4096, v30\n"
    "v_mfma_f32_16x16x32_bf16 v[100:103], a[168:171], a[148:151], v[100:103]\n"
    "ds_read_b64_tr_b16 a[98:99], v15 offset:1024\n"
    "v_mfma_f32_16x16x32_bf16 v[104:107], a[172:175], a[148:151], v[104:107]\n"
    "ds_read_b64_tr_b16 a[100:101], v16 offset:0\n"
    "buffer_load_dwordx4 v11, s[64:67], s86 offen offset:3072  lds\n"
    "v_mfma_f32_16x16x32_bf16 v[108:111], a[176:179], a[148:151], v[108:111]\n"
    "ds_read_b64_tr_b16 a[102:103], v16 offset:1024\n"
    "v_mfma_f32_16x16x32_bf16 v[224:227], a[180:183], a[148:151], v[224:227]\n"
    "ds_read_b64_tr_b16 a[104:105], v17 offset:0\n"
    "v_mfma_f32_16x16x32_bf16 v[228:231], a[184:187], a[148:151], v[228:231]\n"
    "ds_read_b64_tr_b16 a[106:107], v17 offset:1024\n"
    "v_mfma_f32_16x16x32_bf16 v[232:235], a[188:191], a[148:151], v[232:235]\n"
    "ds_read_b64_tr_b16 a[108:109], v18 offset:0\n"
    "s_add_u32 m0, s98, 49152\n"
    "v_mfma_f32_16x16x32_bf16 v[236:239], a[192:195], a[148:151], v[236:239]\n"
    "ds_read_b64_tr_b16 a[110:111], v18 offset:1024\n"
    "buffer_load_dwordx4 v10, s[68:71], 0 offen offset:0  lds\n"
    "v_mfma_f32_16x16x32_bf16 a[48:51], a[148:151], a[196:199], a[48:51]\n"
    "ds_read_b64_tr_b16 a[112:113], v15 offset:16384\n"
    "v_mfma_f32_16x16x32_bf16 a[52:55], a[152:155], a[196:199], a[52:55]\n"
    "ds_read_b64_tr_b16 a[114:115], v15 offset:17408\n"
    "v_mfma_f32_16x16x32_bf16 v[252:255], a[192:195], a[152:155], v[252:255]\n"
    "ds_read_b64_tr_b16 a[116:117], v16 offset:16384\n"
    "v_mfma_f32_16x16x32_bf16 v[248:251], a[188:191], a[152:155], v[248:251]\n"
    "ds_read_b64_tr_b16 a[118:119], v16 offset:17408\n"
    "v_mfma_f32_16x16x32_bf16 v[244:247], a[184:187], a[152:155], v[244:247]\n"
    "ds_read_b64_tr_b16 a[120:121], v17 offset:16384\n"
    "buffer_load_dwordx4 v10, s[68:71], s84 offen offset:1024  lds\n"
    "v_mfma_f32_16x16x32_bf16 v[240:243], a[180:183], a[152:155], v[240:243]\n"
    "ds_read_b64_tr_b16 a[122:123], v17 offset:17408\n"
    "v_mfma_f32_16x16x32_bf16 v[124:127], a[176:179], a[152:155], v[124:127]\n"
    "ds_read_b64_tr_b16 a[124:125], v18 offset:16384\n"
    "v_mfma_f32_16x16x32_bf16 v[120:123], a[172:175], a[152:155], v[120:123]\n"
    "ds_read_b64_tr_b16 a[126:127], v18 offset:17408\n"
    "v_mfma_f32_16x16x32_bf16 v[116:119], a[168:171], a[152:155], v[116:119]\n"
    "ds_read_b128 a[128:131], v19\n"
    "v_mfma_f32_16x16x32_bf16 v[112:115], a[164:167], a[152:155], v[112:115]\n"
    "ds_read_b128 a[68:71], v13 offset:2048\n"
    "buffer_load_dwordx4 v11, s[68:71], s85 offen offset:2048  lds\n"
    "v_mfma_f32_16x16x32_bf16 v[128:131], a[164:167], a[156:159], v[128:131]\n"
    "ds_read_b128 a[72:75], v13 offset:4096\n"
    "v_mfma_f32_16x16x32_bf16 v[132:135], a[168:171], a[156:159], v[132:135]\n"
    "ds_read_b128 a[76:79], v13 offset:6144\n"
    "v_mfma_f32_16x16x32_bf16 v[136:139], a[172:175], a[156:159], v[136:139]\n"
    "ds_read_b128 a[80:83], v13 offset:8192\n"
    "v_mfma_f32_16x16x32_bf16 v[140:143], a[176:179], a[156:159], v[140:143]\n"
    "ds_read_b128 a[84:87], v13 offset:10240\n"
    "v_mfma_f32_16x16x32_bf16 a[0:3], a[180:183], a[156:159], a[0:3]\n"
    "ds_read_b128 a[88:91], v13 offset:12288\n"
    "buffer_load_dwordx4 v11, s[68:71], s86 offen offset:3072  lds\n"
    "v_mfma_f32_16x16x32_bf16 a[4:7], a[184:187], a[156:159], a[4:7]\n"
    "ds_read_b128 a[92:95], v13 offset:14336\n"
    "v_mfma_f32_16x16x32_bf16 a[8:11], a[188:191], a[156:159], a[8:11]\n"
    "v_mfma_f32_16x16x32_bf16 a[12:15], a[192:195], a[156:159], a[12:15]\n"
    "TC0:\n"
    "s_cbranch_execz TC4\n"
    "v_mfma_f32_16x16x32_bf16 a[56:59], a[156:159], a[196:199], a[56:59]\n"
    "s_add_u32 m0, s99, 65536\n"
    "v_mfma_f32_16x16x32_bf16 a[60:63], a[160:163], a[196:199], a[60:63]\n"
    "buffer_load_dwordx4 v12, s[72:75], 0 offen offset:0  lds\n"
    "v_mfma_f32_16x16x32_bf16 a[28:31], a[192:195], a[160:163], a[28:31]\n"
    "s_add_u32 s60, s60, 128\n"
    "s_addc_u32 s61, s61, 0\n"
    "s_add_u32 s64, s64, 458752\n"
    "s_addc_u32 s65, s65, 0\n"
    "v_mfma_f32_16x16x32_bf16 a[24:27], a[188:191], a[160:163], a[24:27]\n"
    "s_add_u32 s68, s68, 458752\n"
    "s_addc_u32 s69, s69, 0\n"
    "v_mfma_f32_16x16x32_bf16 a[20:23], a[184:187], a[160:163], a[20:23]\n"
    "s_add_u32 s72, s72, 128\n"
    "s_addc_u32 s73, s73, 0\n"
    "v_mfma_f32_16x16x32_bf16 a[16:19], a[180:183], a[160:163], a[16:19]\n"
    "s_add_u32 s92, s92, s93\n"
    "s_add_u32 s98, s98, s93\n"
    "v_mfma_f32_16x16x32_bf16 v[156:159], a[176:179], a[160:163], v[156:159]\n"
    "s_add_u32 s99, s99, s93\n"
    "s_sub_u32 s93, 0, s93\n"
    "v_mfma_f32_16x16x32_bf16 v[152:155], a[172:175], a[160:163], v[152:155]\n"
    "v_mfma_f32_16x16x32_bf16 v[148:151], a[168:171], a[160:163], v[148:151]\n"
    "s_mov_b32 s59, 1\n"
    "v_mfma_f32_16x16x32_bf16 v[144:147], a[164:167], a[160:163], v[144:147]\n"
    "s_waitcnt lgkmcnt(0)\n"
    ".p2align 6\n"
    "P1X_loop:\n"
    "v_mfma_f32_16x16x32_bf16 v[32:35], a[96:99], a[64:67], v[32:35]\n"
    "ds_read_b128 a[132:135], v14 offset:0\n"
    "v_mfma_f32_16x16x32_bf16 v[36:39], a[100:103], a[64:67], v[36:39]\n"
    "ds_read_b64_tr_b16 a[164:165], v15 offset:8192\n"
    "v_mfma_f32_16x16x32_bf16 v[40:43], a[104:107], a[64:67], v[40:43]\n"
    "ds_read_b64_tr_b16 a[166:167], v15 offset:9216\n"
    "v_mfma_f32_16x16x32_bf16 v[44:47], a[108:111], a[64:67], v[44:47]\n"
    "ds_read_b64_tr_b16 a[168:169], v16 offset:8192\n"
    "v_mfma_f32_16x16x32_bf16 v[160:163], a[112:115], a[64:67], v[160:163]\n"
    "ds_read_b64_tr_b16 a[170:171], v16 offset:9216\n"
    "v_mfma_f32_16x16x32_bf16 v[164:167], a[116:119], a[64:67], v[164:167]\n"
    "ds_read_b64_tr_b16 a[172:173], v17 offset:8192\n"
    "v_mfma_f32_16x16x32_bf16 v[168:171], a[120:123], a[64:67], v[168:171]\n"
    "ds_read_b64_tr_b16 a[174:175], v17 offset:9216\n"
    "v_mfma_f32_16x16x32_bf16 v[172:175], a[124:127], a[64:67], v[172:175]\n"
    "ds_read_b64_tr_b16 a[176:177], v18 offset:8192\n"
    "v_mfma_f32_16x16x32_bf16 a[32:35], a[64:67], a[128:131], a[32:35]\n"
    "ds_read_b64_tr_b16 a[178:179], v18 offset:9216\n"
    "v_mfma_f32_16x16x32_bf16 a[36:39], a[68:71], a[128:131], a[36:39]\n"
    "ds_read_b64_tr_b16 a[180:181], v15 offset:24576\n"
    "v_mfma_f32_16x16x32_bf16 v[188:191], a[124:127], a[68:71], v[188:191]\n"
    "ds_read_b64_tr_b16 a[182:183], v15 offset:25600\n"
    "v_mfma_f32_16x16x32_bf16 v[184:187], a[120:123], a[68:71], v[184:187]\n"
    "ds_read_b64_tr_b16 a[184:185], v16 offset:24576\n"
    "v_mfma_f32_16x16x32_bf16 v[180:183], a[116:119], a[68:71], v[180:183]\n"
    "ds_read_b64_tr_b16 a[186:187], v16 offset:25600\n"
    "v_mfma_f32_16x16x32_bf16 v[176:179], a[112:115], a[68:71], v[176:179]\n"
    "ds_read_b64_tr_b16 a[188:189], v17 offset:24576\n"
    "v_mfma_f32_16x16x32_bf16 v[60:63], a[108:111], a[68:71], v[60:63]\n"
    "ds_read_b64_tr_b16 a[190:191], v17 offset:25600\n"
    "v_mfma_f32_16x16x32_bf16 v[56:59], a[104:107], a[68:71], v[56:59]\n"
    "ds_read_b64_tr_b16 a[192:193], v18 offset:24576\n"
    "v_mfma_f32_16x16x32_bf16 v[52:55], a[100:103], a[68:71], v[52:55]\n"
    "ds_read_b64_tr_b16 a[194:195], v18 offset:25600\n"
    "v_mfma_f32_16x16x32_bf16 v[48:51], a[96:99], a[68:71], v[48:51]\n"
    "ds_read_b128 a[196:199], v20\n"
    "v_mfma_f32_16x16x32_bf16 v[64:67], a[96:99], a[72:75], v[64:67]\n"
    "ds_read_b128 a[136:139], v14 offset:2048\n"
    "v_mfma_f32_16x16x32_bf16 v[68:71], a[100:103], a[72:75], v[68:71]\n"
    "ds_read_b128 a[140:143], v14 offset:4096\n"
    "v_mfma_f32_16x16x32_bf16 v[72:75], a[104:107], a[72:75], v[72:75]\n"
    "ds_read_b128 a[144:147], v14 offset:6144\n"
    "v_mfma_f32_16x16x32_bf16 v[76:79], a[108:111], a[72:75], v[76:79]\n"
    "ds_read_b128 a[148:151], v14 offset:8192\n"
    "v_mfma_f32_16x16x32_bf16 v[192:195], a[112:115], a[72:75], v[192:195]\n"
    "ds_read_b128 a[152:155], v14 offset:10240\n"
    "v_mfma_f32_16x16x32_bf16 v[196:199], a[116:119], a[72:75], v[196:199]\n"
    "ds_read_b128 a[156:159], v14 offset:12288\n"
    "v_mfma_f32_16x16x32_bf16 v[200:203], a[120:123], a[72:75], v[200:203]\n"
    "ds_read_b128 a[160:163], v14 offset:14336\n"
    "v_mfma_f32_16x16x32_bf16 v[204:207], a[124:127], a[72:75], v[204:207]\n"
    "v_mfma_f32_16x16x32_bf16 a[40:43], a[72:75], a[128:131], a[40:43]\n"
    "v_mfma_f32_16x16x32_bf16 a[44:47], a[76:79], a[128:131], a[44:47]\n"
    "v_mfma_f32_16x16x32_bf16 v[220:223], a[124:127], a[76:79], v[220:223]\n"
    "v_mfma_f32_16x16x32_bf16 v[216:219], a[120:123], a[76:79], v[216:219]\n"
    "v_mfma_f32_16x16x32_bf16 v[212:215], a[116:119], a[76:79], v[212:215]\n"
    "v_mfma_f32_16x16x32_bf16 v[208:211], a[112:115], a[76:79], v[208:211]\n"
    "v_mfma_f32_16x16x32_bf16 v[92:95], a[108:111], a[76:79], v[92:95]\n"
    "v_mfma_f32_16x16x32_bf16 v[88:91], a[104:107], a[76:79], v[88:91]\n"
    "s_waitcnt lgkmcnt(0)\n"
    "s_barrier\n"
    "v_mfma_f32_16x16x32_bf16 v[84:87], a[100:103], a[76:79], v[84:87]\n"
    "s_add_u32 m0, s92, 0\n"
    "v_mfma_f32_16x16x32_bf16 v[80:83], a[96:99], a[76:79], v[80:83]\n"
    "buffer_load_dwordx4 v8, s[60:63], s76 offen offset:0  lds\n"
    "v_mfma_f32_16x16x32_bf16 v[96:99], a[96:99], a[80:83], v[96:99]\n"
    "v_add_u32 v13, s93, v13\n"
    "v_mfma_f32_16x16x32_bf16 v[100:103], a[100:103], a[80:83], v[100:103]\n"
    "v_add_u32 v14, s93, v14\n"
    "v_mfma_f32_16x16x32_bf16 v[104:107], a[104:107], a[80:83], v[104:107]\n"
    "v_mfma_f32_16x16x32_bf16 v[108:111], a[108:111], a[80:83], v[108:111]\n"
    "buffer_load_dwordx4 v9, s[60:63], s77 offen offset:1024  lds\n"
    "v_mfma_f32_16x16x32_bf16 v[224:227], a[112:115], a[80:83], v[224:227]\n"
    "v_add_u32 v15, s93, v15\n"
    "v_mfma_f32_16x16x32_bf16 v[228:231], a[116:119], a[80:83], v[228:231]\n"
    "v_add_u32 v16, s93, v16\n"
    "v_mfma_f32_16x16x32_bf16 v[232:235], a[120:123], a[80:83], v[232:235]\n"
    "v_mfma_f32_16x16x32_bf16 v[236:239], a[124:127], a[80:83], v[236:239]\n"
    "buffer_load_dwordx4 v8, s[60:63], s78 offen offset:2048  lds\n"
    "v_mfma_f32_16x16x32_bf16 a[48:51], a[80:83], a[128:131], a[48:51]\n"
    "v_add_u32 v17, s93, v17\n"
    "v_mfma_f32_16x16x32_bf16 a[52:55], a[84:87], a[128:131], a[52:55]\n"
    "v_add_u32 v18, s93, v18\n"
    "v_mfma_f32_16x16x32_bf16 v[252:255], a[124:127], a[84:87], v[252:255]\n"
    "v_mfma_f32_16x16x32_bf16 v[248:251], a[120:123], a[84:87], v[248:251]\n"
    "buffer_load_dwordx4 v9, s[60:63], s79 offen offset:3072  lds\n"
    "v_mfma_f32_16x16x32_bf16 v[244:247], a[116:119], a[84:87], v[244:247]\n"
    "v_add_u32 v19, s93, v19\n"
    "v_mfma_f32_16x16x32_bf16 v[240:243], a[112:115], a[84:87], v[240:243]\n"
    "v_add_u32 v20, s93, v20\n"
    "v_mfma_f32_16x16x32_bf16 v[124:127], a[108:111], a[84:87], v[124:127]\n"
    "s_add_u32 m0, s92, 4096\n"
    "v_mfma_f32_16x16x32_bf16 v[120:123], a[104:107], a[84:87], v[120:123]\n"
    "buffer_load_dwordx4 v8, s[60:63], s80 offen offset:0  lds\n"
    "v_mfma_f32_16x16x32_bf16 v[116:119], a[100:103], a[84:87], v[116:119]\n"
    "v_mfma_f32_16x16x32_bf16 v[112:115], a[96:99], a[84:87], v[112:115]\n"
    "v_mfma_f32_16x16x32_bf16 v[128:131], a[96:99], a[88:91], v[128:131]\n"
    "v_mfma_f32_16x16x32_bf16 v[132:135], a[100:103], a[88:91], v[132:135]\n"
    "buffer_load_dwordx4 v9, s[60:63], s81 offen offset:1024  lds\n"
    "v_mfma_f32_16x16x32_bf16 v[136:139], a[104:107], a[88:91], v[136:139]\n"
    "v_mfma_f32_16x16x32_bf16 v[140:143], a[108:111], a[88:91], v[140:143]\n"
    "v_mfma_f32_16x16x32_bf16 a[0:3], a[112:115], a[88:91], a[0:3]\n"
    "v_mfma_f32_16x16x32_bf16 a[4:7], a[116:119], a[88:91], a[4:7]\n"
    "buffer_load_dwordx4 v8, s[60:63], s82 offen offset:2048  lds\n"
    "v_mfma_f32_16x16x32_bf16 a[8:11], a[120:123], a[88:91], a[8:11]\n"
    "v_mfma_f32_16x16x32_bf16 a[12:15], a[124:127], a[88:91], a[12:15]\n"
    "v_mfma_f32_16x16x32_bf16 a[56:59], a[88:91], a[128:131], a[56:59]\n"
    "v_mfma_f32_16x16x32_bf16 a[60:63], a[92:95], a[128:131], a[60:63]\n"
    "buffer_load_dwordx4 v9, s[60:63], s83 offen offset:3072  lds\n"
    "v_mfma_f32_16x16x32_bf16 a[28:31], a[124:127], a[92:95], a[28:31]\n"
    "v_mfma_f32_16x16x32_bf16 a[24:27], a[120:123], a[92:95], a[24:27]\n"
    "v_mfma_f32_16x16x32_bf16 a[20:23], a[116:119], a[92:95], a[20:23]\n"
    "s_add_u32 m0, s98, 32768\n"
    "v_mfma_f32_16x16x32_bf16 a[16:19], a[112:115], a[92:95], a[16:19]\n"
    "buffer_load_dwordx4 v10, s[64:67], 0 offen offset:0  lds\n"
    "v_mfma_f32_16x16x32_bf16 v[156:159], a[108:111], a[92:95], v[156:159]\n"
    "v_mfma_f32_16x16x32_bf16 v[152:155], a[104:107], a[92:95], v[152:155]\n"
    "v_mfma_f32_16x16x32_bf16 v[148:151], a[100:103], a[92:95], v[148:151]\n"
    "v_mfma_f32_16x16x32_bf16 v[144:147], a[96:99], a[92:95], v[144:147]\n"
    "buffer_load_dwordx4 v10, s[64:67], s84 offen offset:1024  lds\n"
    "v_mfma_f32_16x16x32_bf16 v[32:35], a[164:167], a[132:135], v[32:35]\n"
    "v_mfma_f32_16x16x32_bf16 v[36:39], a[168:171], a[132:135], v[36:39]\n"
    "v_mfma_f32_16x16x32_bf16 v[40:43], a[172:175], a[132:135], v[40:43]\n"
    "v_mfma_f32_16x16x32_bf16 v[44:47], a[176:179], a[132:135], v[44:47]\n"
    "buffer_load_dwordx4 v11, s[64:67], s85 offen offset:2048  lds\n"
    "v_mfma_f32_16x16x32_bf16 v[160:163], a[180:183], a[132:135], v[160:163]\n"
    "v_mfma_f32_16x16x32_bf16 v[164:167], a[184:187], a[132:135], v[164:167]\n"
    "v_mfma_f32_16x16x32_bf16 v[168:171], a[188:191], a[132:135], v[168:171]\n"
    "v_mfma_f32_16x16x32_bf16 v[172:175], a[192:195], a[132:135], v[172:175]\n"
    "buffer_load_dwordx4 v11, s[64:67], s86 offen offset:3072  lds\n"
    "v_mfma_f32_16x16x32_bf16 a[32:35], a[132:135], a[196:199], a[32:35]\n"
    "v_mfma_f32_16x16x32_bf16 a[36:39], a[136:139], a[196:199], a[36:39]\n"
    "v_mfma_f32_16x16x32_bf16 v[188:191], a[192:195], a[136:139], v[188:191]\n"
    "s_add_u32 m0, s98, 49152\n"
    "v_mfma_f32_16x16x32_bf16 v[184:187], a[188:191], a[136:139], v[184:187]\n"
    "buffer_load_dwordx4 v10, s[68:71], 0 offen offset:0  lds\n"
    "v_mfma_f32_16x16x32_bf16 v[180:183], a[184:187], a[136:139], v[180:183]\n"
    "v_mfma_f32_16x16x32_bf16 v[176:179], a[180:183], a[136:139], v[176:179]\n"
    "v_mfma_f32_16x16x32_bf16 v[60:63], a[176:179], a[136:139], v[60:63]\n"
    "v_mfma_f32_16x16x32_bf16 v[56:59], a[172:175], a[136:139], v[56:59]\n"
    "buffer_load_dwordx4 v10, s[68:71], s84 offen offset:1024  lds\n"
    "v_mfma_f32_16x16x32_bf16 v[52:55], a[168:171], a[136:139], v[52:55]\n"
    "v_mfma_f32_16x16x32_bf16 v[48:51], a[164:167], a[136:139], v[48:51]\n"
    "v_mfma_f32_16x16x32_bf16 v[64:67], a[164:167], a[140:143], v[64:67]\n"
    "v_mfma_f32_16x16x32_bf16 v[68:71], a[168:171], a[140:143], v[68:71]\n"
    "buffer_load_dwordx4 v11, s[68:71], s85 offen offset:2048  lds\n"
    "v_mfma_f32_16x16x32_bf16 v[72:75], a[172:175], a[140:143], v[72:75]\n"
    "v_mfma_f32_16x16x32_bf16 v[76:79], a[176:179], a[140:143], v[76:79]\n"
    "v_mfma_f32_16x16x32_bf16 v[192:195], a[180:183], a[140:143], v[192:195]\n"
    "v_mfma_f32_16x16x32_bf16 v[196:199], a[184:187], a[140:143], v[196:199]\n"
    "buffer_load_dwordx4 v11, s[68:71], s86 offen offset:3072  lds\n"
    "v_mfma_f32_16x16x32_bf16 v[200:203], a[188:191], a[140:143], v[200:203]\n"
    "v_mfma_f32_16x16x32_bf16 v[204:207], a[192:195], a[140:143], v[204:207]\n"
    "v_mfma_f32_16x16x32_bf16 a[40:43], a[140:143], a[196:199], a[40:43]\n"
    "s_add_u32 m0, s99, 65536\n"
    "v_mfma_f32_16x16x32_bf16 a[44:47], a[144:147], a[196:199], a[44:47]\n"
    "buffer_load_dwordx4 v12, s[72:75], 0 offen offset:0  lds\n"
    "v_mfma_f32_16x16x32_bf16 v[220:223], a[192:195], a[144:147], v[220:223]\n"
    "v_mfma_f32_16x16x32_bf16 v[216:219], a[188:191], a[144:147], v[216:219]\n"
    "v_mfma_f32_16x16x32_bf16 v[212:215], a[184:187], a[144:147], v[212:215]\n"
    "v_mfma_f32_16x16x32_bf16 v[208:211], a[180:183], a[144:147], v[208:211]\n"
    "v_mfma_f32_16x16x32_bf16 v[92:95], a[176:179], a[144:147], v[92:95]\n"
    "v_mfma_f32_16x16x32_bf16 v[88:91], a[172:175], a[144:147], v[88:91]\n"
    "s_waitcnt vmcnt(17)\n"
    "s_barrier\n"
    "v_mfma_f32_16x16x32_bf16 v[84:87], a[168:171], a[144:147], v[84:87]\n"
    "buffer_load_dword a232, v30, s[12:15], 0 offen\n"
    "v_mfma_f32_16x16x32_bf16 v[80:83], a[164:167], a[144:147], v[80:83]\n"
    "ds_read_b128 a[64:67], v13 offset:0\n"
    "v_add_u32 v30, 4096, v30\n"
    "v_mfma_f32_16x16x32_bf16 v[96:99], a[164:167], a[148:151], v[96:99]\n"
    "ds_read_b64_tr_b16 a[96:97], v15 offset:0\n"
    "v_mfma_f32_16x16x32_bf16 v[100:103], a[168:171], a[148:151], v[100:103]\n"
    "ds_read_b64_tr_b16 a[98:99], v15 offset:1024\n"
    "v_mfma_f32_16x16x32_bf16 v[104:107], a[172:175], a[148:151], v[104:107]\n"
    "ds_read_b64_tr_b16 a[100:101], v16 offset:0\n"
    "v_mfma_f32_16x16x32_bf16 v[108:111], a[176:179], a[148:151], v[108:111]\n"
    "ds_read_b64_tr_b16 a[102:103], v16 offset:1024\n"
    "v_mfma_f32_16x16x32_bf16 v[224:227], a[180:183], a[148:151], v[224:227]\n"
    "ds_read_b64_tr_b16 a[104:105], v17 offset:0\n"
    "v_mfma_f32_16x16x32_bf16 v[228:231], a[184:187], a[148:151], v[228:231]\n"
    "ds_read_b64_tr_b16 a[106:107], v17 offset:1024\n"
    "v_mfma_f32_16x16x32_bf16 v[232:235], a[188:191], a[148:151], v[232:235]\n"
    "ds_read_b64_tr_b16 a[108:109], v18 offset:0\n"
    "v_mfma_f32_16x16x32_bf16 v[236:239], a[192:195], a[148:151], v[236:239]\n"
    "ds_read_b64_tr_b16 a[110:111], v18 offset:1024\n"
    "v_mfma_f32_16x16x32_bf16 a[48:51], a[148:151], a[196:199], a[48:51]\n"
    "ds_read_b64_tr_b16 a[112:113], v15 offset:16384\n"
    "v_mfma_f32_16x16x32_bf16 a[52:55], a[152:155], a[196:199], a[52:55]\n"
    "ds_read_b64_tr_b16 a[114:115], v15 offset:17408\n"
    "v_mfma_f32_16x16x32_bf16 v[252:255], a[192:195], a[152:155], v[252:255]\n"
    "ds_read_b64_tr_b16 a[116:117], v16 offset:16384\n"
    "v_mfma_f32_16x16x32_bf16 v[248:251], a[188:191], a[152:155], v[248:251]\n"
    "ds_read_b64_tr_b16 a[118:119], v16 offset:17408\n"
    "v_mfma_f32_16x16x32_bf16 v[244:247], a[184:187], a[152:155], v[244:247]\n"
    "ds_read_b64_tr_b16 a[120:121], v17 offset:16384\n"
    "v_mfma_f32_16x16x32_bf16 v[240:243], a[180:183], a[152:155], v[240:243]\n"
    "ds_read_b64_tr_b16 a[122:123], v17 offset:17408\n"
    "v_mfma_f32_16x16x32_bf16 v[124:127], a[176:179], a[152:155], v[124:127]\n"
    "ds_read_b64_tr_b16 a[124:125], v18 offset:16384\n"
    "v_mfma_f32_16x16x32_bf16 v[120:123], a[172:175], a[152:155], v[120:123]\n"
    "ds_read_b64_tr_b16 a[126:127], v18 offset:17408\n"
    "v_mfma_f32_16x16x32_bf16 v[116:119], a[168:171], a[152:155], v[116:119]\n"
    "ds_read_b128 a[128:131], v19\n"
    "v_mfma_f32_16x16x32_bf16 v[112:115], a[164:167], a[152:155], v[112:115]\n"
    "ds_read_b128 a[68:71], v13 offset:2048\n"
    "v_mfma_f32_16x16x32_bf16 v[128:131], a[164:167], a[156:159], v[128:131]\n"
    "ds_read_b128 a[72:75], v13 offset:4096\n"
    "v_mfma_f32_16x16x32_bf16 v[132:135], a[168:171], a[156:159], v[132:135]\n"
    "ds_read_b128 a[76:79], v13 offset:6144\n"
    "v_mfma_f32_16x16x32_bf16 v[136:139], a[172:175], a[156:159], v[136:139]\n"
    "ds_read_b128 a[80:83], v13 offset:8192\n"
    "v_mfma_f32_16x16x32_bf16 v[140:143], a[176:179], a[156:159], v[140:143]\n"
    "ds_read_b128 a[84:87], v13 offset:10240\n"
    "v_mfma_f32_16x16x32_bf16 a[0:3], a[180:183], a[156:159], a[0:3]\n"
    "ds_read_b128 a[88:91], v13 offset:12288\n"
    "v_mfma_f32_16x16x32_bf16 a[4:7], a[184:187], a[156:159], a[4:7]\n"
    "ds_read_b128 a[92:95], v13 offset:14336\n"
    "v_mfma_f32_16x16x32_bf16 a[8:11], a[188:191], a[156:159], a[8:11]\n"
    "s_add_u32 s60, s60, 128\n"
    "s_addc_u32 s61, s61, 0\n"
    "v_mfma_f32_16x16x32_bf16 a[12:15], a[192:195], a[156:159], a[12:15]\n"
    "s_add_u32 s64, s64, 458752\n"
    "s_addc_u32 s65, s65, 0\n"
    "v_mfma_f32_16x16x32_bf16 a[56:59], a[156:159], a[196:199], a[56:59]\n"
    "s_add_u32 s68, s68, 458752\n"
    "s_addc_u32 s69, s69, 0\n"
    "v_mfma_f32_16x16x32_bf16 a[60:63], a[160:163], a[196:199], a[60:63]\n"
    "s_add_u32 s72, s72, 128\n"
    "s_addc_u32 s73, s73, 0\n"
    "v_mfma_f32_16x16x32_bf16 a[28:31], a[192:195], a[160:163], a[28:31]\n"
    "s_add_u32 s92, s92, s93\n"
    "s_add_u32 s98, s98, s93\n"
    "v_mfma_f32_16x16x32_bf16 a[24:27], a[188:191], a[160:163], a[24:27]\n"
    "s_add_u32 s99, s99, s93\n"
    "s_sub_u32 s93, 0, s93\n"
    "v_mfma_f32_16x16x32_bf16 a[20:23], a[184:187], a[160:163], a[20:23]\n"
    "s_add_u32 s59, s59, 1\n"
    "s_cmp_eq_u32 s59, 7\n"
    "v_mfma_f32_16x16x32_bf16 a[16:19], a[180:183], a[160:163], a[16:19]\n"
    "s_cselect_b64 s[12:13], s[4:5], s[12:13]\n"
    "s_cmp_lt_u32 s59, 14\n"
    "v_mfma_f32_16x16x32_bf16 v[156:159], a[176:179], a[160:163], v[156:159]\n"
    "s_cselect_b32 s62, s62, 0\n"
    "s_cselect_b32 s66, s66, 0\n"
    "v_mfma_f32_16x16x32_bf16 v[152:155], a[172:175], a[160:163], v[152:155]\n"
    "s_cselect_b32 s70, s70, 0\n"
    "s_cselect_b32 s74, s74, 0\n"
    "v_mfma_f32_16x16x32_bf16 v[148:151], a[168:171], a[160:163], v[148:151]\n"
    "s_cmp_lt_u32 s59, 15\n"
    "v_mfma_f32_16x16x32_bf16 v[144:147], a[164:167], a[160:163], v[144:147]\n"
    "s_waitcnt lgkmcnt(0)\n"
    "s_cbranch_scc1 P1X_loop\n"
    "s_lshl_b32 s94, s56, 8\n"
    "s_add_u32 s94, s94, 37748736\n"
    "s_movk_i32 s95, 0x300\n"
    "v_mfma_f32_16x16x32_bf16 a[32:35], a[64:67], a[128:131], a[32:35]\n"
    "ds_read_b128 a[132:135], v14 offset:0\n"
    "v_lshl_add_u32 v4, v31, 3, s94\n"
    "v_mfma_f32_16x16x32_bf16 a[36:39], a[68:71], a[128:131], a[36:39]\n"
    "ds_read_b64_tr_b16 a[164:165], v15 offset:8192\n"
    "global_load_dwordx2 a[200:201], v4, s[52:53] offset:0 sc1\n"
    "v_mfma_f32_16x16x32_bf16 a[40:43], a[72:75], a[128:131], a[40:43]\n"
    "ds_read_b64_tr_b16 a[166:167], v15 offset:9216\n"
    "v_mfma_f32_16x16x32_bf16 a[44:47], a[76:79], a[128:131], a[44:47]\n"
    "ds_read_b64_tr_b16 a[168:169], v16 offset:8192\n"
    "global_load_dwordx2 a[202:203], v4, s[52:53] offset:32 sc1\n"
    "v_mfma_f32_16x16x32_bf16 a[48:51], a[80:83], a[128:131], a[48:51]\n"
    "ds_read_b64_tr_b16 a[170:171], v16 offset:9216\n"
    "v_add_u32 v5, 256, v4\n"
    "v_bfi_b32 v4, s95, v5, v4\n"
    "v_mfma_f32_16x16x32_bf16 a[52:55], a[84:87], a[128:131], a[52:55]\n"
    "ds_read_b64_tr_b16 a[172:173], v17 offset:8192\n"
    "global_load_dwordx2 a[204:205], v4, s[52:53] offset:0 sc1\n"
    "v_mfma_f32_16x16x32_bf16 a[56:59], a[88:91], a[128:131], a[56:59]\n"
    "ds_read_b64_tr_b16 a[174:175], v17 offset:9216\n"
    "v_mfma_f32_16x16x32_bf16 a[60:63], a[92:95], a[128:131], a[60:63]\n"
    "ds_read_b64_tr_b16 a[176:177], v18 offset:8192\n"
    "global_load_dwordx2 a[206:207], v4, s[52:53] offset:32 sc1\n"
    "v_mfma_f32_16x16x32_bf16 v[32:35], a[96:99], a[64:67], v[32:35]\n"
    "ds_read_b64_tr_b16 a[178:179], v18 offset:9216\n"
    "v_add_u32 v5, 256, v4\n"
    "v_bfi_b32 v4, s95, v5, v4\n"
    "v_mfma_f32_16x16x32_bf16 v[36:39], a[100:103], a[64:67], v[36:39]\n"
    "ds_read_b64_tr_b16 a[180:181], v15 offset:24576\n"
    "global_load_dwordx2 a[208:209], v4, s[52:53] offset:0 sc1\n"
    "v_mfma_f32_16x16x32_bf16 v[40:43], a[104:107], a[64:67], v[40:43]\n"
    "ds_read_b64_tr_b16 a[182:183], v15 offset:25600\n"
    "v_mfma_f32_16x16x32_bf16 v[44:47], a[108:111], a[64:67], v[44:47]\n"
    "ds_read_b64_tr_b16 a[184:185], v16 offset:24576\n"
    "global_load_dwordx2 a[210:211], v4, s[52:53] offset:32 sc1\n"
    "v_mfma_f32_16x16x32_bf16 v[160:163], a[112:115], a[64:67], v[160:163]\n"
    "ds_read_b64_tr_b16 a[186:187], v16 offset:25600\n"
    "v_add_u32 v5, 256, v4\n"
    "v_bfi_b32 v4, s95, v5, v4\n"
    "v_mfma_f32_16x16x32_bf16 v[164:167], a[116:119], a[64:67], v[164:167]\n"
    "ds_read_b64_tr_b16 a[188:189], v17 offset:24576\n"
    "global_load_dwordx2 a[212:213], v4, s[52:53] offset:0 sc1\n"
    "v_mfma_f32_16x16x32_bf16 v[168:171], a[120:123], a[64:67], v[168:171]\n"
    "ds_read_b64_tr_b16 a[190:191], v17 offset:25600\n"
    "v_mfma_f32_16x16x32_bf16 v[172:175], a[124:127], a[64:67], v[172:175]\n"
    "ds_read_b64_tr_b16 a[192:193], v18 offset:24576\n"
    "global_load_dwordx2 a[214:215], v4, s[52:53] offset:32 sc1\n"
    "s_lshr_b32 s94, s56, 1\n"
    "s_lshl_b32 s94, s94, 6\n"
    "v_mfma_f32_16x16x32_bf16 v[188:191], a[124:127], a[68:71], v[188:191]\n"
    "ds_read_b64_tr_b16 a[194:195], v18 offset:25600\n"
    "s_add_u32 s94, s94, s57\n"
    "s_lshl_b32 s94, s94, 1\n"
    "v_mfma_f32_16x16x32_bf16 v[184:187], a[120:123], a[68:71], v[184:187]\n"
    "ds_read_b128 a[196:199], v20\n"
    "s_lshr_b32 s95, s55, 1\n"
    "s_add_u32 s94, s94, s95\n"
    "v_mfma_f32_16x16x32_bf16 v[180:183], a[116:119], a[68:71], v[180:183]\n"
    "ds_read_b128 a[136:139], v14 offset:2048\n"
    "s_lshl_b32 s94, s94, 15\n"
    "s_add_u32 s94, s94, 16777216\n"
    "v_mfma_f32_16x16x32_bf16 v[176:179], a[112:115], a[68:71], v[176:179]\n"
    "ds_read_b128 a[140:143], v14 offset:4096\n"
    "s_add_u32 s12, s52, s94\n"
    "s_addc_u32 s13, s53, 0\n"
    "v_mfma_f32_16x16x32_bf16 v[60:63], a[108:111], a[68:71], v[60:63]\n"
    "ds_read_b128 a[144:147], v14 offset:6144\n"
    "s_and_b32 s13, s13, 0xffff\n"
    "s_mov_b32 s14, 4194304\n"
    "v_mfma_f32_16x16x32_bf16 v[56:59], a[104:107], a[68:71], v[56:59]\n"
    "ds_read_b128 a[148:151], v14 offset:8192\n"
    "s_mov_b32 s15, 0x20000\n"
    "s_add_u32 s94, s96, 0\n"
    "v_mfma_f32_16x16x32_bf16 v[52:55], a[100:103], a[68:71], v[52:55]\n"
    "ds_read_b128 a[152:155], v14 offset:10240\n"
    "s_and_b32 s95, s94, 2\n"
    "s_lshl_b32 s95, s95, 13\n"
    "v_mfma_f32_16x16x32_bf16 v[48:51], a[96:99], a[68:71], v[48:51]\n"
    "ds_read_b128 a[156:159], v14 offset:12288\n"
    "s_and_b32 s94, s94, 1\n"
    "s_lshl_b32 s94, s94, 10\n"
    "v_mfma_f32_16x16x32_bf16 v[64:67], a[96:99], a[72:75], v[64:67]\n"
    "ds_read_b128 a[160:163], v14 offset:14336\n"
    "s_add_u32 s84, s94, s95\n"
    "s_add_u32 s94, s96, 1\n"
    "v_mfma_f32_16x16x32_bf16 v[68:71], a[100:103], a[72:75], v[68:71]\n"
    "s_and_b32 s95, s94, 2\n"
    "s_lshl_b32 s95, s95, 13\n"
    "s_and_b32 s94, s94, 1\n"
    "v_mfma_f32_16x16x32_bf16 v[72:75], a[104:107], a[72:75], v[72:75]\n"
    "s_lshl_b32 s94, s94, 10\n"
    "s_add_u32 s85, s94, s95\n"
    "s_add_u32 s94, s96, 2\n"
    "v_mfma_f32_16x16x32_bf16 v[76:79], a[108:111], a[72:75], v[76:79]\n"
    "s_and_b32 s95, s94, 2\n"
    "s_lshl_b32 s95, s95, 13\n"
    "s_and_b32 s94, s94, 1\n"
    "v_mfma_f32_16x16x32_bf16 v[192:195], a[112:115], a[72:75], v[192:195]\n"
    "s_lshl_b32 s94, s94, 10\n"
    "s_add_u32 s86, s94, s95\n"
    "s_add_u32 s94, s96, 3\n"
    "v_mfma_f32_16x16x32_bf16 v[196:199], a[116:119], a[72:75], v[196:199]\n"
    "s_and_b32 s95, s94, 2\n"
    "s_lshl_b32 s95, s95, 13\n"
    "s_and_b32 s94, s94, 1\n"
    "v_mfma_f32_16x16x32_bf16 v[200:203], a[120:123], a[72:75], v[200:203]\n"
    "s_lshl_b32 s94, s94, 10\n"
    "s_add_u32 s87, s94, s95\n"
    "s_and_b32 s94, s55, 1\n"
    "v_mfma_f32_16x16x32_bf16 v[204:207], a[124:127], a[72:75], v[204:207]\n"
    "s_lshl_b32 s11, s91, 1\n"
    "s_add_u32 s11, s11, s94\n"
    "s_lshr_b32 s94, s11, 3\n"
    "v_mfma_f32_16x16x32_bf16 v[220:223], a[124:127], a[76:79], v[220:223]\n"
    "s_lshl_b32 s94, s94, 19\n"
    "s_and_b32 s95, s11, 7\n"
    "s_lshl_b32 s95, s95, 11\n"
    "v_mfma_f32_16x16x32_bf16 v[216:219], a[120:123], a[76:79], v[216:219]\n"
    "s_waitcnt lgkmcnt(0) vmcnt(9)\n"
    "s_barrier\n"
    "s_add_u32 s94, s94, s95\n"
    "v_mfma_f32_16x16x32_bf16 a[32:35], a[132:135], a[196:199], a[32:35]\n"
    "v_add_u32 v7, s94, v21\n"
    "v_lshrrev_b32 v1, 4, v0\n"
    "v_mfma_f32_16x16x32_bf16 a[36:39], a[136:139], a[196:199], a[36:39]\n"
    "v_add_u32 v1, 0, v1\n"
    "v_and_b32 v2, 7, v0\n"
    "v_mfma_f32_16x16x32_bf16 a[40:43], a[140:143], a[196:199], a[40:43]\n"
    "v_xor_b32 v1, v1, v2\n"
    "v_lshlrev_b32 v1, 4, v1\n"
    "v_mfma_f32_16x16x32_bf16 a[44:47], a[144:147], a[196:199], a[44:47]\n"
    "v_lshrrev_b32 v2, 3, v0\n"
    "v_and_b32 v3, 4, v2\n"
    "v_mfma_f32_16x16x32_bf16 a[48:51], a[148:151], a[196:199], a[48:51]\n"
    "v_add_u32 v2, v2, v3\n"
    "v_lshl_add_u32 v8, v2, 12, v1\n"
    "v_mfma_f32_16x16x32_bf16 a[52:55], a[152:155], a[196:199], a[52:55]\n"
    "v_lshrrev_b32 v1, 4, v0\n"
    "v_add_u32 v1, 4, v1\n"
    "v_mfma_f32_16x16x32_bf16 a[56:59], a[156:159], a[196:199], a[56:59]\n"
    "v_and_b32 v2, 7, v0\n"
    "v_xor_b32 v1, v1, v2\n"
    "v_mfma_f32_16x16x32_bf16 a[60:63], a[160:163], a[196:199], a[60:63]\n"
    "v_lshlrev_b32 v1, 4, v1\n"
    "v_lshrrev_b32 v2, 3, v0\n"
    "v_mfma_f32_16x16x32_bf16 v[212:215], a[116:119], a[76:79], v[212:215]\n"
    "v_and_b32 v3, 4, v2\n"
    "v_add_u32 v2, v2, v3\n"
    "v_mfma_f32_16x16x32_bf16 v[208:211], a[112:115], a[76:79], v[208:211]\n"
    "v_lshl_add_u32 v9, v2, 12, v1\n"
    "s_lshl_b32 s94, s55, 1\n"
    "v_mfma_f32_16x16x32_bf16 v[92:95], a[108:111], a[76:79], v[92:95]\n"
    "s_add_u32 s95, s94, s56\n"
    "s_add_u32 s95, s95, 0\n"
    "s_and_b32 s95, s95, 3\n"
    "v_mfma_f32_16x16x32_bf16 v[88:91], a[104:107], a[76:79], v[88:91]\n"
    "s_and_b32 s94, s94, 4\n"
    "s_add_u32 s94, s94, s95\n"
    "s_lshl_b32 s94, s94, 17\n"
    "v_mfma_f32_16x16x32_bf16 v[84:87], a[100:103], a[76:79], v[84:87]\n"
    "s_add_u32 s76, s94, 0\n"
    "s_add_u32 s77, s94, 64512\n"
    "s_add_u32 s78, s94, 14336\n"
    "v_mfma_f32_16x16x32_bf16 v[80:83], a[96:99], a[76:79], v[80:83]\n"
    "s_add_u32 s79, s94, 78848\n"
    "s_lshl_b32 s94, s55, 1\n"
    "s_add_u32 s95, s94, s56\n"
    "v_mfma_f32_16x16x32_bf16 v[96:99], a[96:99], a[80:83], v[96:99]\n"
    "s_add_u32 s95, s95, 1\n"
    "s_and_b32 s95, s95, 3\n"
    "s_and_b32 s94, s94, 4\n"
    "v_mfma_f32_16x16x32_bf16 v[100:103], a[100:103], a[80:83], v[100:103]\n"
    "s_add_u32 s94, s94, s95\n"
    "s_lshl_b32 s94, s94, 17\n"
    "s_add_u32 s80, s94, 0\n"
    "v_mfma_f32_16x16x32_bf16 v[104:107], a[104:107], a[80:83], v[104:107]\n"
    "s_add_u32 s81, s94, 64512\n"
    "s_add_u32 s82, s94, 14336\n"
    "s_add_u32 s83, s94, 78848\n"
    "v_mfma_f32_16x16x32_bf16 v[108:111], a[108:111], a[80:83], v[108:111]\n"
    "s_lshl_b32 s94, s57, 9\n"
    "s_and_b32 s95, s56, 1\n"
    "s_add_u32 s94, s94, s95\n"
    "v_mfma_f32_16x16x32_bf16 v[224:227], a[112:115], a[80:83], v[224:227]\n"
    "s_lshl_b32 s94, s94, 11\n"
    "s_add_u32 s60, s34, s94\n"
    "s_addc_u32 s61, s35, 0\n"
    "v_mfma_f32_16x16x32_bf16 v[228:231], a[116:119], a[80:83], v[228:231]\n"
    "s_and_b32 s61, s61, 0xffff\n"
    "s_mov_b32 s62, 1044608\n"
    "s_mov_b32 s63, 0x20000\n"
    "v_mfma_f32_16x16x32_bf16 v[232:235], a[120:123], a[80:83], v[232:235]\n"
    "s_lshl_b32 s92, s55, 13\n"
    "s_lshl_b32 s98, s55, 12\n"
    "v_mfma_f32_16x16x32_bf16 v[236:239], a[124:127], a[80:83], v[236:239]\n"
    "v_accvgpr_read_b32 v12, a32\n"
    "v_accvgpr_read_b32 v13, a33\n"
    "v_mfma_f32_16x16x32_bf16 v[252:255], a[124:127], a[84:87], v[252:255]\n"
    "v_accvgpr_read_b32 v14, a34\n"
    "v_accvgpr_read_b32 v15, a35\n"
    "v_mfma_f32_16x16x32_bf16 v[248:251], a[120:123], a[84:87], v[248:251]\n"
    "v_accvgpr_read_b32 v16, a36\n"
    "v_accvgpr_read_b32 v17, a37\n"
    "v_mfma_f32_16x16x32_bf16 v[244:247], a[116:119], a[84:87], v[244:247]\n"
    "v_accvgpr_read_b32 v18, a38\n"
    "v_accvgpr_read_b32 v19, a39\n"
    "v_mfma_f32_16x16x32_bf16 v[240:243], a[112:115], a[84:87], v[240:243]\n"
    "v_cvt_pk_bf16_f32 v12, v12, v13\n"
    "v_cvt_pk_bf16_f32 v13, v14, v15\n"
    "v_mfma_f32_16x16x32_bf16 v[124:127], a[108:111], a[84:87], v[124:127]\n"
    "v_cvt_pk_bf16_f32 v14, v16, v17\n"
    "v_cvt_pk_bf16_f32 v15, v18, v19\n"
    "v_mfma_f32_16x16x32_bf16 v[120:123], a[104:107], a[84:87], v[120:123]\n"
    "buffer_store_dwordx4 v[12:15], v7, s[12:15], s84 offen sc1\n"
    "v_mfma_f32_16x16x32_bf16 v[116:119], a[100:103], a[84:87], v[116:119]\n"
    "s_nop 1\n"
    "v_accvgpr_read_b32 v12, a40\n"
    "v_mfma_f32_16x16x32_bf16 v[112:115], a[96:99], a[84:87], v[112:115]\n"
    "v_accvgpr_read_b32 v13, a41\n"
    "v_accvgpr_read_b32 v14, a42\n"
    "v_mfma_f32_16x16x32_bf16 v[128:131], a[96:99], a[88:91], v[128:131]\n"
    "v_accvgpr_read_b32 v15, a43\n"
    "v_accvgpr_read_b32 v16, a44\n"
    "v_mfma_f32_16x16x32_bf16 v[132:135], a[100:103], a[88:91], v[132:135]\n"
    "v_accvgpr_read_b32 v17, a45\n"
    "v_accvgpr_read_b32 v18, a46\n"
    "v_mfma_f32_16x16x32_bf16 v[136:139], a[104:107], a[88:91], v[136:139]\n"
    "v_accvgpr_read_b32 v19, a47\n"
    "v_cvt_pk_bf16_f32 v12, v12, v13\n"
    "v_mfma_f32_16x16x32_bf16 v[140:143], a[108:111], a[88:91], v[140:143]\n"
    "v_cvt_pk_bf16_f32 v13, v14, v15\n"
    "v_cvt_pk_bf16_f32 v14, v16, v17\n"
    "v_mfma_f32_16x16x32_bf16 a[0:3], a[112:115], a[88:91], a[0:3]\n"
    "v_cvt_pk_bf16_f32 v15, v18, v19\n"
    "v_mfma_f32_16x16x32_bf16 a[4:7], a[116:119], a[88:91], a[4:7]\n"
    "buffer_store_dwordx4 v[12:15], v7, s[12:15], s85 offen sc1\n"
    "v_mfma_f32_16x16x32_bf16 a[8:11], a[120:123], a[88:91], a[8:11]\n"
    "s_nop 1\n"
    "v_accvgpr_read_b32 v12, a48\n"
    "v_mfma_f32_16x16x32_bf16 a[12:15], a[124:127], a[88:91], a[12:15]\n"
    "v_accvgpr_read_b32 v13, a49\n"
    "v_accvgpr_read_b32 v14, a50\n"
    "v_mfma_f32_16x16x32_bf16 a[28:31], a[124:127], a[92:95], a[28:31]\n"
    "v_accvgpr_read_b32 v15, a51\n"
    "v_accvgpr_read_b32 v16, a52\n"
    "v_mfma_f32_16x16x32_bf16 a[24:27], a[120:123], a[92:95], a[24:27]\n"
    "v_accvgpr_read_b32 v17, a53\n"
    "v_accvgpr_read_b32 v18, a54\n"
    "v_mfma_f32_16x16x32_bf16 a[20:23], a[116:119], a[92:95], a[20:23]\n"
    "v_accvgpr_read_b32 v19, a55\n"
    "v_cvt_pk_bf16_f32 v12, v12, v13\n"
    "v_mfma_f32_16x16x32_bf16 a[16:19], a[112:115], a[92:95], a[16:19]\n"
    "v_cvt_pk_bf16_f32 v13, v14, v15\n"
    "v_cvt_pk_bf16_f32 v14, v16, v17\n"
    "v_mfma_f32_16x16x32_bf16 v[156:159], a[108:111], a[92:95], v[156:159]\n"
    "v_cvt_pk_bf16_f32 v15, v18, v19\n"
    "v_mfma_f32_16x16x32_bf16 v[152:155], a[104:107], a[92:95], v[152:155]\n"
    "buffer_store_dwordx4 v[12:15], v7, s[12:15], s86 offen sc1\n"
    "v_mfma_f32_16x16x32_bf16 v[148:151], a[100:103], a[92:95], v[148:151]\n"
    "s_nop 1\n"
    "v_accvgpr_read_b32 v12, a56\n"
    "v_mfma_f32_16x16x32_bf16 v[144:147], a[96:99], a[92:95], v[144:147]\n"
    "v_accvgpr_read_b32 v13, a57\n"
    "v_accvgpr_read_b32 v14, a58\n"
    "v_mfma_f32_16x16x32_bf16 v[32:35], a[164:167], a[132:135], v[32:35]\n"
    "v_accvgpr_read_b32 v15, a59\n"
    "v_accvgpr_read_b32 v16, a60\n"
    "v_mfma_f32_16x16x32_bf16 v[36:39], a[168:171], a[132:135], v[36:39]\n"
    "v_accvgpr_read_b32 v17, a61\n"
    "v_accvgpr_read_b32 v18, a62\n"
    "v_mfma_f32_16x16x32_bf16 v[40:43], a[172:175], a[132:135], v[40:43]\n"
    "v_accvgpr_read_b32 v19, a63\n"
    "v_cvt_pk_bf16_f32 v12, v12, v13\n"
    "v_mfma_f32_16x16x32_bf16 v[44:47], a[176:179], a[132:135], v[44:47]\n"
    "v_cvt_pk_bf16_f32 v13, v14, v15\n"
    "v_cvt_pk_bf16_f32 v14, v16, v17\n"
    "v_mfma_f32_16x16x32_bf16 v[160:163], a[180:183], a[132:135], v[160:163]\n"
    "v_cvt_pk_bf16_f32 v15, v18, v19\n"
    "v_mfma_f32_16x16x32_bf16 v[164:167], a[184:187], a[132:135], v[164:167]\n"
    "buffer_store_dwordx4 v[12:15], v7, s[12:15], s87 offen sc1\n"
    "v_mfma_f32_16x16x32_bf16 v[168:171], a[188:191], a[132:135], v[168:171]\n"
    "s_nop 1\n"
    "s_add_u32 m0, s92, 0\n"
    "s_nop 0\n"
    "v_mfma_f32_16x16x32_bf16 v[172:175], a[192:195], a[132:135], v[172:175]\n"
    "v_mfma_f32_16x16x32_bf16 v[188:191], a[192:195], a[136:139], v[188:191]\n"
    "v_mfma_f32_16x16x32_bf16 v[184:187], a[188:191], a[136:139], v[184:187]\n"
    "buffer_load_dwordx4 v8, s[60:63], s76 offen offset:0 sc1 lds\n"
    "v_mfma_f32_16x16x32_bf16 v[180:183], a[184:187], a[136:139], v[180:183]\n"
    "s_lshl_b32 s94, s54, 9\n"
    "s_add_u32 s94, s94, 37879808\n"
    "v_mfma_f32_16x16x32_bf16 v[176:179], a[180:183], a[136:139], v[176:179]\n"
    "v_lshl_add_u32 v20, v0, 3, s94\n"
    "s_and_b32 s95, s56, 1\n"
    "v_mfma_f32_16x16x32_bf16 v[60:63], a[176:179], a[136:139], v[60:63]\n"
    "TC1:\n"
    "s_cbranch_execz TC5\n"
    "s_lshr_b32 s94, s56, 1\n"
    "s_lshl_b32 s94, s94, 3\n"
    "s_add_u32 s94, s94, s58\n"
    "v_mfma_f32_16x16x32_bf16 v[56:59], a[172:175], a[136:139], v[56:59]\n"
    "buffer_load_dwordx4 v9, s[60:63], s77 offen offset:1024 sc1 lds\n"
    "v_mfma_f32_16x16x32_bf16 v[52:55], a[168:171], a[136:139], v[52:55]\n"
    "s_lshl_b32 s94, s94, 1\n"
    "s_add_u32 s94, s94, s95\n"
    "s_lshl_b32 s94, s94, 18\n"
    "v_mfma_f32_16x16x32_bf16 v[48:51], a[164:167], a[136:139], v[48:51]\n"
    "v_add_u32 v10, s98, v21\n"
    "s_add_u32 s94, s94, 16777216\n"
    "v_mfma_f32_16x16x32_bf16 v[64:67], a[164:167], a[140:143], v[64:67]\n"
    "s_add_u32 s64, s52, s94\n"
    "s_addc_u32 s65, s53, 0\n"
    "s_and_b32 s65, s65, 0xffff\n"
    "v_mfma_f32_16x16x32_bf16 v[68:71], a[168:171], a[140:143], v[68:71]\n"
    "buffer_load_dwordx4 v8, s[60:63], s78 offen offset:2048 sc1 lds\n"
    "v_mfma_f32_16x16x32_bf16 v[72:75], a[172:175], a[140:143], v[72:75]\n"
    "s_mov_b32 s66, 16384\n"
    "s_mov_b32 s67, 0x20000\n"
    "s_and_b32 s94, s56, 2\n"
    "v_mfma_f32_16x16x32_bf16 v[76:79], a[176:179], a[140:143], v[76:79]\n"
    "s_and_b32 s95, s54, 1\n"
    "s_add_u32 s94, s94, s95\n"
    "s_lshl_b32 s94, s94, 3\n"
    "v_mfma_f32_16x16x32_bf16 v[192:195], a[180:183], a[140:143], v[192:195]\n"
    "s_and_b32 s95, s57, 3\n"
    "s_add_u32 s94, s94, s95\n"
    "v_mfma_f32_16x16x32_bf16 v[196:199], a[184:187], a[140:143], v[196:199]\n"
    "buffer_load_dwordx4 v9, s[60:63], s79 offen offset:3072 sc1 lds\n"
    "v_mfma_f32_16x16x32_bf16 v[200:203], a[188:191], a[140:143], v[200:203]\n"
    "s_lshl_b32 s94, s94, 1\n"
    "s_lshr_b32 s95, s55, 1\n"
    "s_add_u32 s94, s94, s95\n"
    "v_mfma_f32_16x16x32_bf16 v[204:207], a[192:195], a[140:143], v[204:207]\n"
    "s_lshl_b32 s94, s94, 6\n"
    "s_add_u32 s94, s94, 37879808\n"
    "v_mfma_f32_16x16x32_bf16 v[220:223], a[192:195], a[144:147], v[220:223]\n"
    "v_bfe_u32 v1, v0, 3, 2\n"
    "v_lshl_add_u32 v1, v1, 12, s94\n"
    "v_mfma_f32_16x16x32_bf16 v[216:219], a[188:191], a[144:147], v[216:219]\n"
    "v_lshrrev_b32 v2, 5, v0\n"
    "s_add_u32 m0, s92, 4096\n"
    "v_mfma_f32_16x16x32_bf16 v[212:215], a[184:187], a[144:147], v[212:215]\n"
    "s_nop 0\n"
    "v_mfma_f32_16x16x32_bf16 v[208:211], a[180:183], a[144:147], v[208:211]\n"
    "buffer_load_dwordx4 v8, s[60:63], s80 offen offset:0 sc1 lds\n"
    "v_mfma_f32_16x16x32_bf16 v[92:95], a[176:179], a[144:147], v[92:95]\n"
    "v_lshl_add_u32 v1, v2, 9, v1\n"
    "v_and_b32 v2, 1, v0\n"
    "v_mfma_f32_16x16x32_bf16 v[88:91], a[172:175], a[144:147], v[88:91]\n"
    "v_lshl_add_u32 v1, v2, 17, v1\n"
    "v_mov_b32 v2, s11\n"
    "v_mfma_f32_16x16x32_bf16 v[84:87], a[168:171], a[144:147], v[84:87]\n"
    "v_lshrrev_b32 v3, 3, v2\n"
    "v_lshl_add_u32 v3, v3, 14, v1\n"
    "v_mfma_f32_16x16x32_bf16 v[80:83], a[164:167], a[144:147], v[80:83]\n"
    "v_and_b32 v2, 7, v2\n"
    "v_mfma_f32_16x16x32_bf16 v[96:99], a[164:167], a[148:151], v[96:99]\n"
    "buffer_load_dwordx4 v9, s[60:63], s81 offen offset:1024 sc1 lds\n"
    "v_mfma_f32_16x16x32_bf16 v[100:103], a[168:171], a[148:151], v[100:103]\n"
    "v_lshl_add_u32 v6, v2, 3, v3\n"
    "s_waitcnt vmcnt(10)\n"
    "v_mfma_f32_16x16x32_bf16 v[104:107], a[172:175], a[148:151], v[104:107]\n"
    "v_accvgpr_read_b32 v2, a201\n"
    "v_accvgpr_read_b32 v22, a200\n"
    "v_mfma_f32_16x16x32_bf16 v[108:111], a[176:179], a[148:151], v[108:111]\n"
    "v_xor_b32 v2, s8, v2\n"
    "v_add_f32 v22, 1.0, v22\n"
    "v_mfma_f32_16x16x32_bf16 v[224:227], a[180:183], a[148:151], v[224:227]\n"
    "v_accvgpr_read_b32 v3, a203\n"
    "v_accvgpr_read_b32 v23, a202\n"
    "v_mfma_f32_16x16x32_bf16 v[228:231], a[184:187], a[148:151], v[228:231]\n"
    "buffer_load_dwordx4 v8, s[60:63], s82 offen offset:2048 sc1 lds\n"
    "v_mfma_f32_16x16x32_bf16 v[232:235], a[188:191], a[148:151], v[232:235]\n"
    "v_xor_b32 v3, s8, v3\n"
    "v_add_f32 v23, 1.0, v23\n"
    "v_mfma_f32_16x16x32_bf16 v[236:239], a[192:195], a[148:151], v[236:239]\n"
    "v_or_b32 v5, v2, v3\n"
    "v_accvgpr_read_b32 v2, a205\n"
    "v_mfma_f32_16x16x32_bf16 v[252:255], a[192:195], a[152:155], v[252:255]\n"
    "v_accvgpr_read_b32 v24, a204\n"
    "v_xor_b32 v2, s8, v2\n"
    "v_mfma_f32_16x16x32_bf16 v[248:251], a[188:191], a[152:155], v[248:251]\n"
    "v_add_f32 v24, 1.0, v24\n"
    "v_accvgpr_read_b32 v3, a207\n"
    "v_mfma_f32_16x16x32_bf16 v[244:247], a[184:187], a[152:155], v[244:247]\n"
    "buffer_load_dwordx4 v9, s[60:63], s83 offen offset:3072 sc1 lds\n"
    "v_mfma_f32_16x16x32_bf16 v[240:243], a[180:183], a[152:155], v[240:243]\n"
    "s_add_u32 s60, s60, 256\n"
    "s_addc_u32 s61, s61, 0\n"
    "v_mfma_f32_16x16x32_bf16 v[124:127], a[176:179], a[152:155], v[124:127]\n"
    "v_accvgpr_read_b32 v25, a206\n"
    "v_xor_b32 v3, s8, v3\n"
    "v_mfma_f32_16x16x32_bf16 v[120:123], a[172:175], a[152:155], v[120:123]\n"
    "v_add_f32 v25, 1.0, v25\n"
    "v_or3_b32 v5, v2, v3, v5\n"
    "v_mfma_f32_16x16x32_bf16 v[116:119], a[168:171], a[152:155], v[116:119]\n"
    "v_accvgpr_read_b32 v2, a209\n"
    "v_accvgpr_read_b32 v26, a208\n"
    "v_mfma_f32_16x16x32_bf16 v[112:115], a[164:167], a[152:155], v[112:115]\n"
    "v_xor_b32 v2, s8, v2\n"
    "v_add_f32 v26, 1.0, v26\n"
    "v_mfma_f32_16x16x32_bf16 v[128:131], a[164:167], a[156:159], v[128:131]\n"
    "s_add_u32 m0, s92, 49152\n"
    "s_nop 0\n"
    "v_mfma_f32_16x16x32_bf16 v[132:135], a[168:171], a[156:159], v[132:135]\n"
    "buffer_load_dwordx4 v8, s[60:63], s76 offen offset:0 sc1 lds\n"
    "v_mfma_f32_16x16x32_bf16 v[136:139], a[172:175], a[156:159], v[136:139]\n"
    "v_accvgpr_read_b32 v3, a211\n"
    "v_accvgpr_read_b32 v27, a210\n"
    "v_mfma_f32_16x16x32_bf16 v[140:143], a[176:179], a[156:159], v[140:143]\n"
    "v_xor_b32 v3, s8, v3\n"
    "v_add_f32 v27, 1.0, v27\n"
    "v_mfma_f32_16x16x32_bf16 a[0:3], a[180:183], a[156:159], a[0:3]\n"
    "v_or3_b32 v5, v2, v3, v5\n"
    "v_accvgpr_read_b32 v2, a213\n"
    "v_mfma_f32_16x16x32_bf16 a[4:7], a[184:187], a[156:159], a[4:7]\n"
    "v_accvgpr_read_b32 v28, a212\n"
    "v_mfma_f32_16x16x32_bf16 a[8:11], a[188:191], a[156:159], a[8:11]\n"
    "buffer_load_dwordx4 v9, s[60:63], s77 offen offset:1024 sc1 lds\n"
    "v_mfma_f32_16x16x32_bf16 a[12:15], a[192:195], a[156:159], a[12:15]\n"
    "v_xor_b32 v2, s8, v2\n"
    "v_add_f32 v28, 1.0, v28\n"
    "v_mfma_f32_16x16x32_bf16 a[28:31], a[192:195], a[160:163], a[28:31]\n"
    "v_accvgpr_read_b32 v3, a215\n"
    "v_accvgpr_read_b32 v29, a214\n"
    "v_mfma_f32_16x16x32_bf16 a[24:27], a[188:191], a[160:163], a[24:27]\n"
    "v_xor_b32 v3, s8, v3\n"
    "v_add_f32 v29, 1.0, v29\n"
    "v_mfma_f32_16x16x32_bf16 a[20:23], a[184:187], a[160:163], a[20:23]\n"
    "v_or3_b32 v5, v2, v3, v5\n"
    "s_mul_i32 s94, s57, 28\n"
    "v_mfma_f32_16x16x32_bf16 a[16:19], a[180:183], a[160:163], a[16:19]\n"
    "buffer_load_dwordx4 v8, s[60:63], s78 offen offset:2048 sc1 lds\n"
    "v_mfma_f32_16x16x32_bf16 v[156:159], a[176:179], a[160:163], v[156:159]\n"
    "s_add_u32 s94, s94, s10\n"
    "s_lshl_b32 s94, s94, 1\n"
    "s_and_b32 s95, s55, 1\n"
    "v_mfma_f32_16x16x32_bf16 v[152:155], a[172:175], a[160:163], v[152:155]\n"
    "s_add_u32 s94, s94, s95\n"
    "s_lshl_b32 s94, s94, 1\n"
    "s_lshr_b32 s95, s55, 1\n"
    "v_mfma_f32_16x16x32_bf16 v[148:151], a[168:171], a[160:163], v[148:151]\n"
    "s_add_u32 s94, s94, s95\n"
    "s_lshl_b32 s94, s94, 14\n"
    "v_mfma_f32_16x16x32_bf16 v[144:147], a[164:167], a[160:163], v[144:147]\n"
    "buffer_load_dwordx4 v9, s[60:63], s79 offen offset:3072 sc1 lds\n"
    "s_add_u32 s72, s52, s94\n"
    "s_addc_u32 s73, s53, 0\n"
    "s_and_b32 s73, s73, 0xffff\n"
    "s_mov_b32 s74, 16384\n"
    "s_mov_b32 s75, 0x20000\n"
    "s_mov_b32 s10, 0xbdd2d3e8\n"
    "s_mov_b32 s11, 0xc0135761\n"
    "v_mov_b32 v7, v21\n"
    "v_cmp_ne_u32 vcc, 0, v5\n"
    "s_cbranch_vccnz GRX_slow\n"
    "GRX_back:\n"
    "s_waitcnt vmcnt(12)\n"
    "v_mov_b32 v2, s8\n"
    "v_mov_b32 v3, s9\n"
    "global_store_dwordx2 v6, v[2:3], s[52:53] sc1\n"
    "v_pk_mul_f32 v[12:13], v[32:33], v[32:33]\n"
    "v_pk_mul_f32 v[14:15], v[34:35], v[34:35]\n"
    "s_add_u32 m0, s92, 53248\n"
    "s_nop 0\n"
    "buffer_load_dwordx4 v8, s[60:63], s80 offen offset:0 sc1 lds\n"
    "v_pk_mul_f32 v[16:17], v[36:37], v[36:37]\n"
    "v_pk_mul_f32 v[18:19], v[38:39], v[38:39]\n"
    "v_pk_fma_f32 v[12:13], v[12:13], s[10:11], s[10:11] op_sel:[0,0,1] op_sel_hi:[1,0,1]\n"
    "v_pk_fma_f32 v[14:15], v[14:15], s[10:11], s[10:11] op_sel:[0,0,1] op_sel_hi:[1,0,1]\n"
    "v_pk_fma_f32 v[16:17], v[16:17], s[10:11], s[10:11] op_sel:[0,0,1] op_sel_hi:[1,0,1]\n"
    "v_pk_fma_f32 v[18:19], v[18:19], s[10:11], s[10:11] op_sel:[0,0,1] op_sel_hi:[1,0,1]\n"
    "v_pk_mul_f32 v[12:13], v[12:13], v[32:33]\n"
    "v_pk_mul_f32 v[14:15], v[14:15], v[34:35]\n"
    "v_pk_mul_f32 v[16:17], v[16:17], v[36:37]\n"
    "v_pk_mul_f32 v[18:19], v[18:19], v[38:39]\n"
    "v_exp_f32 v12, v12\n"
    "v_exp_f32 v13, v13\n"
    "v_exp_f32 v14, v14\n"
    "v_exp_f32 v15, v15\n"
    "v_exp_f32 v16, v16\n"
    "v_exp_f32 v17, v17\n"
    "v_exp_f32 v18, v18\n"
    "v_exp_f32 v19, v19\n"
    "v_pk_mul_f32 v[32:33], v[32:33], v[160:161]\n"
    "v_pk_mul_f32 v[34:35], v[34:35], v[162:163]\n"
    "v_pk_mul_f32 v[36:37], v[36:37], v[164:165]\n"
    "buffer_load_dwordx4 v9, s[60:63], s81 offen offset:1024 sc1 lds\n"
    "v_pk_mul_f32 v[38:39], v[38:39], v[166:167]\n"
    "v_pk_fma_f32 v[12:13], v[12:13], v[22:23], v[22:23] op_sel:[0,0,0] op_sel_hi:[1,0,0]\n"
    "v_pk_fma_f32 v[14:15], v[14:15], v[22:23], v[22:23] op_sel:[0,0,0] op_sel_hi:[1,0,0]\n"
    "v_pk_fma_f32 v[16:17], v[16:17], v[22:23], v[22:23] op_sel:[0,0,0] op_sel_hi:[1,0,0]\n"
    "v_pk_fma_f32 v[18:19], v[18:19], v[22:23], v[22:23] op_sel:[0,0,0] op_sel_hi:[1,0,0]\n"
    "v_rcp_f32 v12, v12\n"
    "v_rcp_f32 v13, v13\n"
    "v_rcp_f32 v14, v14\n"
    "v_rcp_f32 v15, v15\n"
    "v_rcp_f32 v16, v16\n"
    "v_rcp_f32 v17, v17\n"
    "v_rcp_f32 v18, v18\n"
    "v_rcp_f32 v19, v19\n"
    "v_pk_mul_f32 v[32:33], v[32:33], v[12:13]\n"
    "v_pk_mul_f32 v[34:35], v[34:35], v[14:15]\n"
    "v_pk_mul_f32 v[36:37], v[36:37], v[16:17]\n"
    "v_pk_mul_f32 v[38:39], v[38:39], v[18:19]\n"
    "v_cvt_pk_bf16_f32 v32, v32, v33\n"
    "v_cvt_pk_bf16_f32 v33, v34, v35\n"
    "v_cvt_pk_bf16_f32 v34, v36, v37\n"
    "v_cvt_pk_bf16_f32 v35, v38, v39\n"
    "v_pk_mul_f32 v[12:13], v[40:41], v[40:41]\n"
    "v_pk_mul_f32 v[14:15], v[42:43], v[42:43]\n"
    "buffer_load_dwordx4 v8, s[60:63], s82 offen offset:2048 sc1 lds\n"
    "v_pk_mul_f32 v[16:17], v[44:45], v[44:45]\n"
    "v_pk_mul_f32 v[18:19], v[46:47], v[46:47]\n"
    "v_pk_fma_f32 v[12:13], v[12:13], s[10:11], s[10:11] op_sel:[0,0,1] op_sel_hi:[1,0,1]\n"
    "v_pk_fma_f32 v[14:15], v[14:15], s[10:11], s[10:11] op_sel:[0,0,1] op_sel_hi:[1,0,1]\n"
    "v_pk_fma_f32 v[16:17], v[16:17], s[10:11], s[10:11] op_sel:[0,0,1] op_sel_hi:[1,0,1]\n"
    "v_pk_fma_f32 v[18:19], v[18:19], s[10:11], s[10:11] op_sel:[0,0,1] op_sel_hi:[1,0,1]\n"
    "v_pk_mul_f32 v[12:13], v[12:13], v[40:41]\n"
    "v_pk_mul_f32 v[14:15], v[14:15], v[42:43]\n"
    "v_pk_mul_f32 v[16:17], v[16:17], v[44:45]\n"
    "v_pk_mul_f32 v[18:19], v[18:19], v[46:47]\n"
    "v_exp_f32 v12, v12\n"
    "v_exp_f32 v13, v13\n"
    "v_exp_f32 v14, v14\n"
    "v_exp_f32 v15, v15\n"
    "v_exp_f32 v16, v16\n"
    "v_exp_f32 v17, v17\n"
    "v_exp_f32 v18, v18\n"
    "v_exp_f32 v19, v19\n"
    "v_pk_mul_f32 v[40:41], v[40:41], v[168:169]\n"
    "v_pk_mul_f32 v[42:43], v[42:43], v[170:171]\n"
    "v_pk_mul_f32 v[44:45], v[44:45], v[172:173]\n"
    "buffer_load_dwordx4 v9, s[60:63], s83 offen offset:3072 sc1 lds\n"
    "s_add_u32 s60, s60, 256\n"
    "s_addc_u32 s61, s61, 0\n"
    "v_pk_mul_f32 v[46:47], v[46:47], v[174:175]\n"
    "v_pk_fma_f32 v[12:13], v[12:13], v[22:23], v[22:23] op_sel:[0,0,0] op_sel_hi:[1,0,0]\n"
    "v_pk_fma_f32 v[14:15], v[14:15], v[22:23], v[22:23] op_sel:[0,0,0] op_sel_hi:[1,0,0]\n"
    "v_pk_fma_f32 v[16:17], v[16:17], v[22:23], v[22:23] op_sel:[0,0,0] op_sel_hi:[1,0,0]\n"
    "v_pk_fma_f32 v[18:19], v[18:19], v[22:23], v[22:23] op_sel:[0,0,0] op_sel_hi:[1,0,0]\n"
    "v_rcp_f32 v12, v12\n"
    "v_rcp_f32 v13, v13\n"
    "v_rcp_f32 v14, v14\n"
    "v_rcp_f32 v15, v15\n"
    "v_rcp_f32 v16, v16\n"
    "v_rcp_f32 v17, v17\n"
    "v_rcp_f32 v18, v18\n"
    "v_rcp_f32 v19, v19\n"
    "v_pk_mul_f32 v[40:41], v[40:41], v[12:13]\n"
    "v_pk_mul_f32 v[42:43], v[42:43], v[14:15]\n"
    "v_pk_mul_f32 v[44:45], v[44:45], v[16:17]\n"
    "v_pk_mul_f32 v[46:47], v[46:47], v[18:19]\n"
    "v_cvt_pk_bf16_f32 v36, v40, v41\n"
    "v_cvt_pk_bf16_f32 v37, v42, v43\n"
    "v_cvt_pk_bf16_f32 v38, v44, v45\n"
    "v_cvt_pk_bf16_f32 v39, v46, v47\n"
    "v_pk_mul_f32 v[12:13], v[48:49], v[48:49]\n"
    "v_pk_mul_f32 v[14:15], v[50:51], v[50:51]\n"
    "s_add_u32 m0, s92, 98304\n"
    "s_nop 0\n"
    "buffer_load_dwordx4 v8, s[60:63], s76 offen offset:0 sc1 lds\n"
    "v_pk_mul_f32 v[16:17], v[52:53], v[52:53]\n"
    "v_pk_mul_f32 v[18:19], v[54:55], v[54:55]\n"
    "v_pk_fma_f32 v[12:13], v[12:13], s[10:11], s[10:11] op_sel:[0,0,1] op_sel_hi:[1,0,1]\n"
    "v_pk_fma_f32 v[14:15], v[14:15], s[10:11], s[10:11] op_sel:[0,0,1] op_sel_hi:[1,0,1]\n"
    "v_pk_fma_f32 v[16:17], v[16:17], s[10:11], s[10:11] op_sel:[0,0,1] op_sel_hi:[1,0,1]\n"
    "v_pk_fma_f32 v[18:19], v[18:19], s[10:11], s[10:11] op_sel:[0,0,1] op_sel_hi:[1,0,1]\n"
    "v_pk_mul_f32 v[12:13], v[12:13], v[48:49]\n"
    "v_pk_mul_f32 v[14:15], v[14:15], v[50:51]\n"
    "v_pk_mul_f32 v[16:17], v[16:17], v[52:53]\n"
    "v_pk_mul_f32 v[18:19], v[18:19], v[54:55]\n"
    "v_exp_f32 v12, v12\n"
    "v_exp_f32 v13, v13\n"
    "v_exp_f32 v14, v14\n"
    "v_exp_f32 v15, v15\n"
    "v_exp_f32 v16, v16\n"
    "v_exp_f32 v17, v17\n"
    "v_exp_f32 v18, v18\n"
    "v_exp_f32 v19, v19\n"
    "v_pk_mul_f32 v[48:49], v[48:49], v[176:177]\n"
    "v_pk_mul_f32 v[50:51], v[50:51], v[178:179]\n"
    "v_pk_mul_f32 v[52:53], v[52:53], v[180:181]\n"
    "buffer_load_dwordx4 v9, s[60:63], s77 offen offset:1024 sc1 lds\n"
    "v_pk_mul_f32 v[54:55], v[54:55], v[182:183]\n"
    "v_pk_fma_f32 v[12:13], v[12:13], v[22:23], v[22:23] op_sel:[0,1,1] op_sel_hi:[1,1,1]\n"
    "v_pk_fma_f32 v[14:15], v[14:15], v[22:23], v[22:23] op_sel:[0,1,1] op_sel_hi:[1,1,1]\n"
    "v_pk_fma_f32 v[16:17], v[16:17], v[22:23], v[22:23] op_sel:[0,1,1] op_sel_hi:[1,1,1]\n"
    "v_pk_fma_f32 v[18:19], v[18:19], v[22:23], v[22:23] op_sel:[0,1,1] op_sel_hi:[1,1,1]\n"
    "v_rcp_f32 v12, v12\n"
    "v_rcp_f32 v13, v13\n"
    "v_rcp_f32 v14, v14\n"
    "v_rcp_f32 v15, v15\n"
    "v_rcp_f32 v16, v16\n"
    "v_rcp_f32 v17, v17\n"
    "v_rcp_f32 v18, v18\n"
    "v_rcp_f32 v19, v19\n"
    "v_pk_mul_f32 v[48:49], v[48:49], v[12:13]\n"
    "v_pk_mul_f32 v[50:51], v[50:51], v[14:15]\n"
    "v_pk_mul_f32 v[52:53], v[52:53], v[16:17]\n"
    "v_pk_mul_f32 v[54:55], v[54:55], v[18:19]\n"
    "v_cvt_pk_bf16_f32 v40, v48, v49\n"
    "v_cvt_pk_bf16_f32 v41, v50, v51\n"
    "v_cvt_pk_bf16_f32 v42, v52, v53\n"
    "v_cvt_pk_bf16_f32 v43, v54, v55\n"
    "v_pk_mul_f32 v[12:13], v[56:57], v[56:57]\n"
    "v_pk_mul_f32 v[14:15], v[58:59], v[58:59]\n"
    "buffer_load_dwordx4 v8, s[60:63], s78 offen offset:2048 sc1 lds\n"
    "v_pk_mul_f32 v[16:17], v[60:61], v[60:61]\n"
    "v_pk_mul_f32 v[18:19], v[62:63], v[62:63]\n"
    "v_pk_fma_f32 v[12:13], v[12:13], s[10:11], s[10:11] op_sel:[0,0,1] op_sel_hi:[1,0,1]\n"
    "v_pk_fma_f32 v[14:15], v[14:15], s[10:11], s[10:11] op_sel:[0,0,1] op_sel_hi:[1,0,1]\n"
    "v_pk_fma_f32 v[16:17], v[16:17], s[10:11], s[10:11] op_sel:[0,0,1] op_sel_hi:[1,0,1]\n"
    "v_pk_fma_f32 v[18:19], v[18:19], s[10:11], s[10:11] op_sel:[0,0,1] op_sel_hi:[1,0,1]\n"
    "v_pk_mul_f32 v[12:13], v[12:13], v[56:57]\n"
    "v_pk_mul_f32 v[14:15], v[14:15], v[58:59]\n"
    "v_pk_mul_f32 v[16:17], v[16:17], v[60:61]\n"
    "v_pk_mul_f32 v[18:19], v[18:19], v[62:63]\n"
    "v_exp_f32 v12, v12\n"
    "v_exp_f32 v13, v13\n"
    "v_exp_f32 v14, v14\n"
    "v_exp_f32 v15, v15\n"
    "v_exp_f32 v16, v16\n"
    "v_exp_f32 v17, v17\n"
    "v_exp_f32 v18, v18\n"
    "v_exp_f32 v19, v19\n"
    "v_pk_mul_f32 v[56:57], v[56:57], v[184:185]\n"
    "v_pk_mul_f32 v[58:59], v[58:59], v[186:187]\n"
    "v_pk_mul_f32 v[60:61], v[60:61], v[188:189]\n"
    "buffer_load_dwordx4 v9, s[60:63], s79 offen offset:3072 sc1 lds\n"
    "v_pk_mul_f32 v[62:63], v[62:63], v[190:191]\n"
    "v_pk_fma_f32 v[12:13], v[12:13], v[22:23], v[22:23] op_sel:[0,1,1] op_sel_hi:[1,1,1]\n"
    "v_pk_fma_f32 v[14:15], v[14:15], v[22:23], v[22:23] op_sel:[0,1,1] op_sel_hi:[1,1,1]\n"
    "v_pk_fma_f32 v[16:17], v[16:17], v[22:23], v[22:23] op_sel:[0,1,1] op_sel_hi:[1,1,1]\n"
    "v_pk_fma_f32 v[18:19], v[18:19], v[22:23], v[22:23] op_sel:[0,1,1] op_sel_hi:[1,1,1]\n"
    "v_rcp_f32 v12, v12\n"
    "v_rcp_f32 v13, v13\n"
    "v_rcp_f32 v14, v14\n"
    "v_rcp_f32 v15, v15\n"
    "v_rcp_f32 v16, v16\n"
    "v_rcp_f32 v17, v17\n"
    "v_rcp_f32 v18, v18\n"
    "v_rcp_f32 v19, v19\n"
    "v_pk_mul_f32 v[56:57], v[56:57], v[12:13]\n"
    "v_pk_mul_f32 v[58:59], v[58:59], v[14:15]\n"
    "v_pk_mul_f32 v[60:61], v[60:61], v[16:17]\n"
    "v_pk_mul_f32 v[62:63], v[62:63], v[18:19]\n"
    "v_cvt_pk_bf16_f32 v44, v56, v57\n"
    "v_cvt_pk_bf16_f32 v45, v58, v59\n"
    "v_cvt_pk_bf16_f32 v46, v60, v61\n"
    "v_cvt_pk_bf16_f32 v47, v62, v63\n"
    "v_pk_mul_f32 v[12:13], v[64:65], v[64:65]\n"
    "v_pk_mul_f32 v[14:15], v[66:67], v[66:67]\n"
    "s_add_u32 m0, s92, 102400\n"
    "s_nop 0\n"
    "buffer_load_dwordx4 v8, s[60:63], s80 offen offset:0 sc1 lds\n"
    "v_pk_mul_f32 v[16:17], v[68:69], v[68:69]\n"
    "v_pk_mul_f32 v[18:19], v[70:71], v[70:71]\n"
    "v_pk_fma_f32 v[12:13], v[12:13], s[10:11], s[10:11] op_sel:[0,0,1] op_sel_hi:[1,0,1]\n"
    "v_pk_fma_f32 v[14:15], v[14:15], s[10:11], s[10:11] op_sel:[0,0,1] op_sel_hi:[1,0,1]\n"
    "v_pk_fma_f32 v[16:17], v[16:17], s[10:11], s[10:11] op_sel:[0,0,1] op_sel_hi:[1,0,1]\n"
    "v_pk_fma_f32 v[18:19], v[18:19], s[10:11], s[10:11] op_sel:[0,0,1] op_sel_hi:[1,0,1]\n"
    "v_pk_mul_f32 v[12:13], v[12:13], v[64:65]\n"
    "v_pk_mul_f32 v[14:15], v[14:15], v[66:67]\n"
    "v_pk_mul_f32 v[16:17], v[16:17], v[68:69]\n"
    "v_pk_mul_f32 v[18:19], v[18:19], v[70:71]\n"
    "v_exp_f32 v12, v12\n"
    "v_exp_f32 v13, v13\n"
    "v_exp_f32 v14, v14\n"
    "v_exp_f32 v15, v15\n"
    "v_exp_f32 v16, v16\n"
    "v_exp_f32 v17, v17\n"
    "v_exp_f32 v18, v18\n"
    "v_exp_f32 v19, v19\n"
    "v_pk_mul_f32 v[64:65], v[64:65], v[192:193]\n"
    "v_pk_mul_f32 v[66:67], v[66:67], v[194:195]\n"
    "v_pk_mul_f32 v[68:69], v[68:69], v[196:197]\n"
    "buffer_load_dwordx4 v9, s[60:63], s81 offen offset:1024 sc1 lds\n"
    "v_pk_mul_f32 v[70:71], v[70:71], v[198:199]\n"
    "v_pk_fma_f32 v[12:13], v[12:13], v[24:25], v[24:25] op_sel:[0,0,0] op_sel_hi:[1,0,0]\n"
    "v_pk_fma_f32 v[14:15], v[14:15], v[24:25], v[24:25] op_sel:[0,0,0] op_sel_hi:[1,0,0]\n"
    "v_pk_fma_f32 v[16:17], v[16:17], v[24:25], v[24:25] op_sel:[0,0,0] op_sel_hi:[1,0,0]\n"
    "v_pk_fma_f32 v[18:19], v[18:19], v[24:25], v[24:25] op_sel:[0,0,0] op_sel_hi:[1,0,0]\n"
    "v_rcp_f32 v12, v12\n"
    "v_rcp_f32 v13, v13\n"
    "v_rcp_f32 v14, v14\n"
    "v_rcp_f32 v15, v15\n"
    "v_rcp_f32 v16, v16\n"
    "v_rcp_f32 v17, v17\n"
    "v_rcp_f32 v18, v18\n"
    "v_rcp_f32 v19, v19\n"
    "v_pk_mul_f32 v[64:65], v[64:65], v[12:13]\n"
    "v_pk_mul_f32 v[66:67], v[66:67], v[14:15]\n"
    "v_pk_mul_f32 v[68:69], v[68:69], v[16:17]\n"
    "v_pk_mul_f32 v[70:71], v[70:71], v[18:19]\n"
    "v_cvt_pk_bf16_f32 v48, v64, v65\n"
    "v_cvt_pk_bf16_f32 v49, v66, v67\n"
    "v_cvt_pk_bf16_f32 v50, v68, v69\n"
    "v_cvt_pk_bf16_f32 v51, v70, v71\n"
    "v_pk_mul_f32 v[12:13], v[72:73], v[72:73]\n"
    "v_pk_mul_f32 v[14:15], v[74:75], v[74:75]\n"
    "buffer_load_dwordx4 v8, s[60:63], s82 offen offset:2048 sc1 lds\n"
    "v_pk_mul_f32 v[16:17], v[76:77], v[76:77]\n"
    "v_pk_mul_f32 v[18:19], v[78:79], v[78:79]\n"
    "v_pk_fma_f32 v[12:13], v[12:13], s[10:11], s[10:11] op_sel:[0,0,1] op_sel_hi:[1,0,1]\n"
    "v_pk_fma_f32 v[14:15], v[14:15], s[10:11], s[10:11] op_sel:[0,0,1] op_sel_hi:[1,0,1]\n"
    "v_pk_fma_f32 v[16:17], v[16:17], s[10:11], s[10:11] op_sel:[0,0,1] op_sel_hi:[1,0,1]\n"
    "v_pk_fma_f32 v[18:19], v[18:19], s[10:11], s[10:11] op_sel:[0,0,1] op_sel_hi:[1,0,1]\n"
    "v_pk_mul_f32 v[12:13], v[12:13], v[72:73]\n"
    "v_pk_mul_f32 v[14:15], v[14:15], v[74:75]\n"
    "v_pk_mul_f32 v[16:17], v[16:17], v[76:77]\n"
    "v_pk_mul_f32 v[18:19], v[18:19], v[78:79]\n"
    "v_exp_f32 v12, v12\n"
    "v_exp_f32 v13, v13\n"
    "v_exp_f32 v14, v14\n"
    "v_exp_f32 v15, v15\n"
    "v_exp_f32 v16, v16\n"
    "v_exp_f32 v17, v17\n"
    "v_exp_f32 v18, v18\n"
    "v_exp_f32 v19, v19\n"
    "v_pk_mul_f32 v[72:73], v[72:73], v[200:201]\n"
    "v_pk_mul_f32 v[74:75], v[74:75], v[202:203]\n"
    "v_pk_mul_f32 v[76:77], v[76:77], v[204:205]\n"
    "buffer_load_dwordx4 v9, s[60:63], s83 offen offset:3072 sc1 lds\n"
    "s_add_u32 s60, s60, 256\n"
    "s_addc_u32 s61, s61, 0\n"
    "v_pk_mul_f32 v[78:79], v[78:79], v[206:207]\n"
    "v_pk_fma_f32 v[12:13], v[12:13], v[24:25], v[24:25] op_sel:[0,0,0] op_sel_hi:[1,0,0]\n"
    "v_pk_fma_f32 v[14:15], v[14:15], v[24:25], v[24:25] op_sel:[0,0,0] op_sel_hi:[1,0,0]\n"
    "v_pk_fma_f32 v[16:17], v[16:17], v[24:25], v[24:25] op_sel:[0,0,0] op_sel_hi:[1,0,0]\n"
    "v_pk_fma_f32 v[18:19], v[18:19], v[24:25], v[24:25] op_sel:[0,0,0] op_sel_hi:[1,0,0]\n"
    "v_rcp_f32 v12, v12\n"
    "v_rcp_f32 v13, v13\n"
    "v_rcp_f32 v14, v14\n"
    "v_rcp_f32 v15, v15\n"
    "v_rcp_f32 v16, v16\n"
    "v_rcp_f32 v17, v17\n"
    "v_rcp_f32 v18, v18\n"
    "v_rcp_f32 v19, v19\n"
    "v_pk_mul_f32 v[72:73], v[72:73], v[12:13]\n"
    "v_pk_mul_f32 v[74:75], v[74:75], v[14:15]\n"
    "v_pk_mul_f32 v[76:77], v[76:77], v[16:17]\n"
    "v_pk_mul_f32 v[78:79], v[78:79], v[18:19]\n"
    "v_cvt_pk_bf16_f32 v52, v72, v73\n"
    "v_cvt_pk_bf16_f32 v53, v74, v75\n"
    "v_cvt_pk_bf16_f32 v54, v76, v77\n"
    "v_cvt_pk_bf16_f32 v55, v78, v79\n"
    "v_pk_mul_f32 v[12:13], v[80:81], v[80:81]\n"
    "v_pk_mul_f32 v[14:15], v[82:83], v[82:83]\n"
    "v_pk_mul_f32 v[16:17], v[84:85], v[84:85]\n"
    "v_pk_mul_f32 v[18:19], v[86:87], v[86:87]\n"
    "v_pk_fma_f32 v[12:13], v[12:13], s[10:11], s[10:11] op_sel:[0,0,1] op_sel_hi:[1,0,1]\n"
    "v_pk_fma_f32 v[14:15], v[14:15], s[10:11], s[10:11] op_sel:[0,0,1] op_sel_hi:[1,0,1]\n"
    "v_pk_fma_f32 v[16:17], v[16:17], s[10:11], s[10:11] op_sel:[0,0,1] op_sel_hi:[1,0,1]\n"
    "v_pk_fma_f32 v[18:19], v[18:19], s[10:11], s[10:11] op_sel:[0,0,1] op_sel_hi:[1,0,1]\n"
    "v_pk_mul_f32 v[12:13], v[12:13], v[80:81]\n"
    "v_pk_mul_f32 v[14:15], v[14:15], v[82:83]\n"
    "v_pk_mul_f32 v[16:17], v[16:17], v[84:85]\n"
    "v_pk_mul_f32 v[18:19], v[18:19], v[86:87]\n"
    "v_exp_f32 v12, v12\n"
    "v_exp_f32 v13, v13\n"
    "v_exp_f32 v14, v14\n"
    "v_exp_f32 v15, v15\n"
    "v_exp_f32 v16, v16\n"
    "v_exp_f32 v17, v17\n"
    "v_exp_f32 v18, v18\n"
    "v_exp_f32 v19, v19\n"
    "v_pk_mul_f32 v[80:81], v[80:81], v[208:209]\n"
    "v_pk_mul_f32 v[82:83], v[82:83], v[210:211]\n"
    "v_pk_mul_f32 v[84:85], v[84:85], v[212:213]\n"
    "v_pk_mul_f32 v[86:87], v[86:87], v[214:215]\n"
    "v_pk_fma_f32 v[12:13], v[12:13], v[24:25], v[24:25] op_sel:[0,1,1] op_sel_hi:[1,1,1]\n"
    "v_pk_fma_f32 v[14:15], v[14:15], v[24:25], v[24:25] op_sel:[0,1,1] op_sel_hi:[1,1,1]\n"
    "v_pk_fma_f32 v[16:17], v[16:17], v[24:25], v[24:25] op_sel:[0,1,1] op_sel_hi:[1,1,1]\n"
    "v_pk_fma_f32 v[18:19], v[18:19], v[24:25], v[24:25] op_sel:[0,1,1] op_sel_hi:[1,1,1]\n"
    "v_rcp_f32 v12, v12\n"
    "v_rcp_f32 v13, v13\n"
    "v_rcp_f32 v14, v14\n"
    "v_rcp_f32 v15, v15\n"
    "v_rcp_f32 v16, v16\n"
    "v_rcp_f32 v17, v17\n"
    "v_rcp_f32 v18, v18\n"
    "v_rcp_f32 v19, v19\n"
    "v_pk_mul_f32 v[80:81], v[80:81], v[12:13]\n"
    "v_pk_mul_f32 v[82:83], v[82:83], v[14:15]\n"
    "v_pk_mul_f32 v[84:85], v[84:85], v[16:17]\n"
    "v_pk_mul_f32 v[86:87], v[86:87], v[18:19]\n"
    "v_cvt_pk_bf16_f32 v56, v80, v81\n"
    "v_cvt_pk_bf16_f32 v57, v82, v83\n"
    "v_cvt_pk_bf16_f32 v58, v84, v85\n"
    "v_cvt_pk_bf16_f32 v59, v86, v87\n"
    "v_pk_mul_f32 v[12:13], v[88:89], v[88:89]\n"
    "v_pk_mul_f32 v[14:15], v[90:91], v[90:91]\n"
    "v_pk_mul_f32 v[16:17], v[92:93], v[92:93]\n"
    "v_pk_mul_f32 v[18:19], v[94:95], v[94:95]\n"
    "v_pk_fma_f32 v[12:13], v[12:13], s[10:11], s[10:11] op_sel:[0,0,1] op_sel_hi:[1,0,1]\n"
    "v_pk_fma_f32 v[14:15], v[14:15], s[10:11], s[10:11] op_sel:[0,0,1] op_sel_hi:[1,0,1]\n"
    "v_pk_fma_f32 v[16:17], v[16:17], s[10:11], s[10:11] op_sel:[0,0,1] op_sel_hi:[1,0,1]\n"
    "v_pk_fma_f32 v[18:19], v[18:19], s[10:11], s[10:11] op_sel:[0,0,1] op_sel_hi:[1,0,1]\n"
    "v_pk_mul_f32 v[12:13], v[12:13], v[88:89]\n"
    "v_pk_mul_f32 v[14:15], v[14:15], v[90:91]\n"
    "v_pk_mul_f32 v[16:17], v[16:17], v[92:93]\n"
    "v_pk_mul_f32 v[18:19], v[18:19], v[94:95]\n"
    "v_exp_f32 v12, v12\n"
    "v_exp_f32 v13, v13\n"
    "v_exp_f32 v14, v14\n"
    "v_exp_f32 v15, v15\n"
    "v_exp_f32 v16, v16\n"
    "v_exp_f32 v17, v17\n"
    "v_exp_f32 v18, v18\n"
    "v_exp_f32 v19, v19\n"
    "v_pk_mul_f32 v[88:89], v[88:89], v[216:217]\n"
    "v_pk_mul_f32 v[90:91], v[90:91], v[218:219]\n"
    "v_pk_mul_f32 v[92:93], v[92:93], v[220:221]\n"
    "v_pk_mul_f32 v[94:95], v[94:95], v[222:223]\n"
    "v_pk_fma_f32 v[12:13], v[12:13], v[24:25], v[24:25] op_sel:[0,1,1] op_sel_hi:[1,1,1]\n"
    "v_pk_fma_f32 v[14:15], v[14:15], v[24:25], v[24:25] op_sel:[0,1,1] op_sel_hi:[1,1,1]\n"
    "v_pk_fma_f32 v[16:17], v[16:17], v[24:25], v[24:25] op_sel:[0,1,1] op_sel_hi:[1,1,1]\n"
    "v_pk_fma_f32 v[18:19], v[18:19], v[24:25], v[24:25] op_sel:[0,1,1] op_sel_hi:[1,1,1]\n"
    "v_rcp_f32 v12, v12\n"
    "v_rcp_f32 v13, v13\n"
    "v_rcp_f32 v14, v14\n"
    "v_rcp_f32 v15, v15\n"
    "v_rcp_f32 v16, v16\n"
    "v_rcp_f32 v17, v17\n"
    "v_rcp_f32 v18, v18\n"
    "v_rcp_f32 v19, v19\n"
    "v_pk_mul_f32 v[88:89], v[88:89], v[12:13]\n"
    "v_pk_mul_f32 v[90:91], v[90:91], v[14:15]\n"
    "v_pk_mul_f32 v[92:93], v[92:93], v[16:17]\n"
    "v_pk_mul_f32 v[94:95], v[94:95], v[18:19]\n"
    "v_cvt_pk_bf16_f32 v60, v88, v89\n"
    "v_cvt_pk_bf16_f32 v61, v90, v91\n"
    "v_cvt_pk_bf16_f32 v62, v92, v93\n"
    "v_cvt_pk_bf16_f32 v63, v94, v95\n"
    "v_pk_mul_f32 v[12:13], v[96:97], v[96:97]\n"
    "v_pk_mul_f32 v[14:15], v[98:99], v[98:99]\n"
    "v_pk_mul_f32 v[16:17], v[100:101], v[100:101]\n"
    "v_pk_mul_f32 v[18:19], v[102:103], v[102:103]\n"
    "v_pk_fma_f32 v[12:13], v[12:13], s[10:11], s[10:11] op_sel:[0,0,1] op_sel_hi:[1,0,1]\n"
    "v_pk_fma_f32 v[14:15], v[14:15], s[10:11], s[10:11] op_sel:[0,0,1] op_sel_hi:[1,0,1]\n"
    "v_pk_fma_f32 v[16:17], v[16:17], s[10:11], s[10:11] op_sel:[0,0,1] op_sel_hi:[1,0,1]\n"
    "v_pk_fma_f32 v[18:19], v[18:19], s[10:11], s[10:11] op_sel:[0,0,1] op_sel_hi:[1,0,1]\n"
    "v_pk_mul_f32 v[12:13], v[12:13], v[96:97]\n"
    "v_pk_mul_f32 v[14:15], v[14:15], v[98:99]\n"
    "v_pk_mul_f32 v[16:17], v[16:17], v[100:101]\n"
    "v_pk_mul_f32 v[18:19], v[18:19], v[102:103]\n"
    "v_exp_f32 v12, v12\n"
    "v_exp_f32 v13, v13\n"
    "v_exp_f32 v14, v14\n"
    "v_exp_f32 v15, v15\n"
    "v_exp_f32 v16, v16\n"
    "v_exp_f32 v17, v17\n"
    "v_exp_f32 v18, v18\n"
    "v_exp_f32 v19, v19\n"
    "v_pk_mul_f32 v[96:97], v[96:97], v[224:225]\n"
    "v_pk_mul_f32 v[98:99], v[98:99], v[226:227]\n"
    "v_pk_mul_f32 v[100:101], v[100:101], v[228:229]\n"
    "v_pk_mul_f32 v[102:103], v[102:103], v[230:231]\n"
    "v_pk_fma_f32 v[12:13], v[12:13], v[26:27], v[26:27] op_sel:[0,0,0] op_sel_hi:[1,0,0]\n"
    "v_pk_fma_f32 v[14:15], v[14:15], v[26:27], v[26:27] op_sel:[0,0,0] op_sel_hi:[1,0,0]\n"
    "v_pk_fma_f32 v[16:17], v[16:17], v[26:27], v[26:27] op_sel:[0,0,0] op_sel_hi:[1,0,0]\n"
    "v_pk_fma_f32 v[18:19], v[18:19], v[26:27], v[26:27] op_sel:[0,0,0] op_sel_hi:[1,0,0]\n"
    "v_rcp_f32 v12, v12\n"
    "v_rcp_f32 v13, v13\n"
    "v_rcp_f32 v14, v14\n"
    "v_rcp_f32 v15, v15\n"
    "v_rcp_f32 v16, v16\n"
    "v_rcp_f32 v17, v17\n"
    "v_rcp_f32 v18, v18\n"
    "v_rcp_f32 v19, v19\n"
    "v_pk_mul_f32 v[96:97], v[96:97], v[12:13]\n"
    "v_pk_mul_f32 v[98:99], v[98:99], v[14:15]\n"
    "v_pk_mul_f32 v[100:101], v[100:101], v[16:17]\n"
    "v_pk_mul_f32 v[102:103], v[102:103], v[18:19]\n"
    "v_cvt_pk_bf16_f32 v64, v96, v97\n"
    "v_cvt_pk_bf16_f32 v65, v98, v99\n"
    "v_cvt_pk_bf16_f32 v66, v100, v101\n"
    "v_cvt_pk_bf16_f32 v67, v102, v103\n"
    "global_load_dwordx2 v[168:169], v20, s[52:53] sc1\n"
    "v_pk_mul_f32 v[12:13], v[104:105], v[104:105]\n"
    "v_pk_mul_f32 v[14:15], v[106:107], v[106:107]\n"
    "v_pk_mul_f32 v[16:17], v[108:109], v[108:109]\n"
    "v_pk_mul_f32 v[18:19], v[110:111], v[110:111]\n"
    "v_pk_fma_f32 v[12:13], v[12:13], s[10:11], s[10:11] op_sel:[0,0,1] op_sel_hi:[1,0,1]\n"
    "v_pk_fma_f32 v[14:15], v[14:15], s[10:11], s[10:11] op_sel:[0,0,1] op_sel_hi:[1,0,1]\n"
    "v_pk_fma_f32 v[16:17], v[16:17], s[10:11], s[10:11] op_sel:[0,0,1] op_sel_hi:[1,0,1]\n"
    "v_pk_fma_f32 v[18:19], v[18:19], s[10:11], s[10:11] op_sel:[0,0,1] op_sel_hi:[1,0,1]\n"
    "v_pk_mul_f32 v[12:13], v[12:13], v[104:105]\n"
    "v_pk_mul_f32 v[14:15], v[14:15], v[106:107]\n"
    "v_pk_mul_f32 v[16:17], v[16:17], v[108:109]\n"
    "v_pk_mul_f32 v[18:19], v[18:19], v[110:111]\n"
    "v_exp_f32 v12, v12\n"
    "v_exp_f32 v13, v13\n"
    "v_exp_f32 v14, v14\n"
    "v_exp_f32 v15, v15\n"
    "v_exp_f32 v16, v16\n"
    "v_exp_f32 v17, v17\n"
    "v_exp_f32 v18, v18\n"
    "v_exp_f32 v19, v19\n"
    "v_pk_mul_f32 v[104:105], v[104:105], v[232:233]\n"
    "v_pk_mul_f32 v[106:107], v[106:107], v[234:235]\n"
    "v_pk_mul_f32 v[108:109], v[108:109], v[236:237]\n"
    "v_pk_mul_f32 v[110:111], v[110:111], v[238:239]\n"
    "v_pk_fma_f32 v[12:13], v[12:13], v[26:27], v[26:27] op_sel:[0,0,0] op_sel_hi:[1,0,0]\n"
    "v_pk_fma_f32 v[14:15], v[14:15], v[26:27], v[26:27] op_sel:[0,0,0] op_sel_hi:[1,0,0]\n"
    "v_pk_fma_f32 v[16:17], v[16:17], v[26:27], v[26:27] op_sel:[0,0,0] op_sel_hi:[1,0,0]\n"
    "v_pk_fma_f32 v[18:19], v[18:19], v[26:27], v[26:27] op_sel:[0,0,0] op_sel_hi:[1,0,0]\n"
    "v_rcp_f32 v12, v12\n"
    "v_rcp_f32 v13, v13\n"
    "v_rcp_f32 v14, v14\n"
    "v_rcp_f32 v15, v15\n"
    "v_rcp_f32 v16, v16\n"
    "v_rcp_f32 v17, v17\n"
    "v_rcp_f32 v18, v18\n"
    "v_rcp_f32 v19, v19\n"
    "v_pk_mul_f32 v[104:105], v[104:105], v[12:13]\n"
    "v_pk_mul_f32 v[106:107], v[106:107], v[14:15]\n"
    "v_pk_mul_f32 v[108:109], v[108:109], v[16:17]\n"
    "v_pk_mul_f32 v[110:111], v[110:111], v[18:19]\n"
    "v_cvt_pk_bf16_f32 v68, v104, v105\n"
    "v_cvt_pk_bf16_f32 v69, v106, v107\n"
    "v_cvt_pk_bf16_f32 v70, v108, v109\n"
    "TC2:\n"
    "s_cbranch_execz TC6\n"
    "v_cvt_pk_bf16_f32 v71, v110, v111\n"
    "v_pk_mul_f32 v[12:13], v[112:113], v[112:113]\n"
    "v_pk_mul_f32 v[14:15], v[114:115], v[114:115]\n"
    "v_pk_mul_f32 v[16:17], v[116:117], v[116:117]\n"
    "v_pk_mul_f32 v[18:19], v[118:119], v[118:119]\n"
    "v_pk_fma_f32 v[12:13], v[12:13], s[10:11], s[10:11] op_sel:[0,0,1] op_sel_hi:[1,0,1]\n"
    "v_pk_fma_f32 v[14:15], v[14:15], s[10:11], s[10:11] op_sel:[0,0,1] op_sel_hi:[1,0,1]\n"
    "v_pk_fma_f32 v[16:17], v[16:17], s[10:11], s[10:11] op_sel:[0,0,1] op_sel_hi:[1,0,1]\n"
    "v_pk_fma_f32 v[18:19], v[18:19], s[10:11], s[10:11] op_sel:[0,0,1] op_sel_hi:[1,0,1]\n"
    "v_pk_mul_f32 v[12:13], v[12:13], v[112:113]\n"
    "v_pk_mul_f32 v[14:15], v[14:15], v[114:115]\n"
    "v_pk_mul_f32 v[16:17], v[16:17], v[116:117]\n"
    "v_pk_mul_f32 v[18:19], v[18:19], v[118:119]\n"
    "v_exp_f32 v12, v12\n"
    "v_exp_f32 v13, v13\n"
    "v_exp_f32 v14, v14\n"
    "v_exp_f32 v15, v15\n"
    "v_exp_f32 v16, v16\n"
    "v_exp_f32 v17, v17\n"
    "v_exp_f32 v18, v18\n"
    "v_exp_f32 v19, v19\n"
    "v_pk_mul_f32 v[112:113], v[112:113], v[240:241]\n"
    "v_pk_mul_f32 v[114:115], v[114:115], v[242:243]\n"
    "v_pk_mul_f32 v[116:117], v[116:117], v[244:245]\n"
    "v_pk_mul_f32 v[118:119], v[118:119], v[246:247]\n"
    "v_pk_fma_f32 v[12:13], v[12:13], v[26:27], v[26:27] op_sel:[0,1,1] op_sel_hi:[1,1,1]\n"
    "v_pk_fma_f32 v[14:15], v[14:15], v[26:27], v[26:27] op_sel:[0,1,1] op_sel_hi:[1,1,1]\n"
    "v_pk_fma_f32 v[16:17], v[16:17], v[26:27], v[26:27] op_sel:[0,1,1] op_sel_hi:[1,1,1]\n"
    "v_pk_fma_f32 v[18:19], v[18:19], v[26:27], v[26:27] op_sel:[0,1,1] op_sel_hi:[1,1,1]\n"
    "v_rcp_f32 v12, v12\n"
    "v_rcp_f32 v13, v13\n"
    "v_rcp_f32 v14, v14\n"
    "v_rcp_f32 v15, v15\n"
    "v_rcp_f32 v16, v16\n"
    "v_rcp_f32 v17, v17\n"
    "v_rcp_f32 v18, v18\n"
    "v_rcp_f32 v19, v19\n"
    "v_pk_mul_f32 v[112:113], v[112:113], v[12:13]\n"
    "v_pk_mul_f32 v[114:115], v[114:115], v[14:15]\n"
    "v_pk_mul_f32 v[116:117], v[116:117], v[16:17]\n"
    "v_pk_mul_f32 v[118:119], v[118:119], v[18:19]\n"
    "v_cvt_pk_bf16_f32 v72, v112, v113\n"
    "v_cvt_pk_bf16_f32 v73, v114, v115\n"
    "v_cvt_pk_bf16_f32 v74, v116, v117\n"
    "v_cvt_pk_bf16_f32 v75, v118, v119\n"
    "s_waitcnt vmcnt(0)\n"
    "v_cmp_ne_u64 vcc, s[8:9], v[168:169]\n"
    "s_cbranch_vccz APX_ok\n"
    "s_mov_b32 s68, 0x40000\n"
    "APX:\n"
    "global_load_dwordx2 v[168:169], v20, s[52:53] sc1\n"
    "s_waitcnt vmcnt(0)\n"
    "v_cmp_ne_u64 vcc, s[8:9], v[168:169]\n"
    "s_cbranch_vccz APX_ok\n"
    "s_sleep 1\n"
    "s_sub_u32 s68, s68, 1\n"
    "s_cbranch_scc0 APX\n"
    "APX_ok:\n"
    "s_add_u32 m0, s98, 32768\n"
    "s_nop 0\n"
    "buffer_load_dwordx4 v10, s[64:67], 0 offen offset:0 sc1 lds\n"
    "buffer_load_dwordx4 v10, s[64:67], 0 offen offset:1024 sc1 lds\n"
    "buffer_load_dwordx4 v10, s[64:67], 0 offen offset:2048 sc1 lds\n"
    "buffer_load_dwordx4 v10, s[64:67], 0 offen offset:3072 sc1 lds\n"
    "s_add_u32 s64, s64, 32768\n"
    "s_addc_u32 s65, s65, 0\n"
    "s_add_u32 m0, s98, 81920\n"
    "s_nop 0\n"
    "buffer_load_dwordx4 v10, s[64:67], 0 offen offset:0 sc1 lds\n"
    "buffer_load_dwordx4 v10, s[64:67], 0 offen offset:1024 sc1 lds\n"
    "buffer_load_dwordx4 v10, s[64:67], 0 offen offset:2048 sc1 lds\n"
    "buffer_load_dwordx4 v10, s[64:67], 0 offen offset:3072 sc1 lds\n"
    "s_add_u32 s64, s64, 32768\n"
    "s_addc_u32 s65, s65, 0\n"
    "s_add_u32 m0, s98, 131072\n"
    "s_nop 0\n"
    "buffer_load_dwordx4 v10, s[64:67], 0 offen offset:0 sc1 lds\n"
    "buffer_load_dwordx4 v10, s[64:67], 0 offen offset:1024 sc1 lds\n"
    "buffer_load_dwordx4 v10, s[64:67], 0 offen offset:2048 sc1 lds\n"
    "buffer_load_dwordx4 v10, s[64:67], 0 offen offset:3072 sc1 lds\n"
    "s_add_u32 s64, s64, 32768\n"
    "s_addc_u32 s65, s65, 0\n"
    "v_pk_mul_f32 v[12:13], v[120:121], v[120:121]\n"
    "v_pk_mul_f32 v[14:15], v[122:123], v[122:123]\n"
    "v_pk_mul_f32 v[16:17], v[124:125], v[124:125]\n"
    "v_pk_mul_f32 v[18:19], v[126:127], v[126:127]\n"
    "v_pk_fma_f32 v[12:13], v[12:13], s[10:11], s[10:11] op_sel:[0,0,1] op_sel_hi:[1,0,1]\n"
    "v_pk_fma_f32 v[14:15], v[14:15], s[10:11], s[10:11] op_sel:[0,0,1] op_sel_hi:[1,0,1]\n"
    "v_pk_fma_f32 v[16:17], v[16:17], s[10:11], s[10:11] op_sel:[0,0,1] op_sel_hi:[1,0,1]\n"
    "v_pk_fma_f32 v[18:19], v[18:19], s[10:11], s[10:11] op_sel:[0,0,1] op_sel_hi:[1,0,1]\n"
    "v_pk_mul_f32 v[12:13], v[12:13], v[120:121]\n"
    "v_pk_mul_f32 v[14:15], v[14:15], v[122:123]\n"
    "v_pk_mul_f32 v[16:17], v[16:17], v[124:125]\n"
    "v_pk_mul_f32 v[18:19], v[18:19], v[126:127]\n"
    "v_exp_f32 v12, v12\n"
    "v_exp_f32 v13, v13\n"
    "v_exp_f32 v14, v14\n"
    "v_exp_f32 v15, v15\n"
    "v_exp_f32 v16, v16\n"
    "v_exp_f32 v17, v17\n"
    "v_exp_f32 v18, v18\n"
    "v_exp_f32 v19, v19\n"
    "v_pk_mul_f32 v[120:121], v[120:121], v[248:249]\n"
    "v_pk_mul_f32 v[122:123], v[122:123], v[250:251]\n"
    "v_pk_mul_f32 v[124:125], v[124:125], v[252:253]\n"
    "v_pk_mul_f32 v[126:127], v[126:127], v[254:255]\n"
    "v_pk_fma_f32 v[12:13], v[12:13], v[26:27], v[26:27] op_sel:[0,1,1] op_sel_hi:[1,1,1]\n"
    "v_pk_fma_f32 v[14:15], v[14:15], v[26:27], v[26:27] op_sel:[0,1,1] op_sel_hi:[1,1,1]\n"
    "v_pk_fma_f32 v[16:17], v[16:17], v[26:27], v[26:27] op_sel:[0,1,1] op_sel_hi:[1,1,1]\n"
    "v_pk_fma_f32 v[18:19], v[18:19], v[26:27], v[26:27] op_sel:[0,1,1] op_sel_hi:[1,1,1]\n"
    "v_rcp_f32 v12, v12\n"
    "v_rcp_f32 v13, v13\n"
    "v_rcp_f32 v14, v14\n"
    "v_rcp_f32 v15, v15\n"
    "v_rcp_f32 v16, v16\n"
    "v_rcp_f32 v17, v17\n"
    "v_rcp_f32 v18, v18\n"
    "v_rcp_f32 v19, v19\n"
    "v_pk_mul_f32 v[120:121], v[120:121], v[12:13]\n"
    "v_pk_mul_f32 v[122:123], v[122:123], v[14:15]\n"
    "v_pk_mul_f32 v[124:125], v[124:125], v[16:17]\n"
    "v_pk_mul_f32 v[126:127], v[126:127], v[18:19]\n"
    "v_cvt_pk_bf16_f32 v76, v120, v121\n"
    "v_cvt_pk_bf16_f32 v77, v122, v123\n"
    "v_cvt_pk_bf16_f32 v78, v124, v125\n"
    "v_cvt_pk_bf16_f32 v79, v126, v127\n"
    "v_accvgpr_read_b32 v160, a0\n"
    "v_accvgpr_read_b32 v161, a1\n"
    "buffer_store_dwordx4 v[32:35], v7, s[72:75], 0 offen offset:0 sc1\n"
    "v_accvgpr_read_b32 v162, a2\n"
    "v_accvgpr_read_b32 v163, a3\n"
    "v_accvgpr_read_b32 v164, a4\n"
    "v_accvgpr_read_b32 v165, a5\n"
    "v_accvgpr_read_b32 v166, a6\n"
    "v_accvgpr_read_b32 v167, a7\n"
    "v_pk_mul_f32 v[12:13], v[128:129], v[128:129]\n"
    "v_pk_mul_f32 v[14:15], v[130:131], v[130:131]\n"
    "v_pk_mul_f32 v[16:17], v[132:133], v[132:133]\n"
    "v_pk_mul_f32 v[18:19], v[134:135], v[134:135]\n"
    "v_pk_fma_f32 v[12:13], v[12:13], s[10:11], s[10:11] op_sel:[0,0,1] op_sel_hi:[1,0,1]\n"
    "v_pk_fma_f32 v[14:15], v[14:15], s[10:11], s[10:11] op_sel:[0,0,1] op_sel_hi:[1,0,1]\n"
    "v_pk_fma_f32 v[16:17], v[16:17], s[10:11], s[10:11] op_sel:[0,0,1] op_sel_hi:[1,0,1]\n"
    "v_pk_fma_f32 v[18:19], v[18:19], s[10:11], s[10:11] op_sel:[0,0,1] op_sel_hi:[1,0,1]\n"
    "v_pk_mul_f32 v[12:13], v[12:13], v[128:129]\n"
    "v_pk_mul_f32 v[14:15], v[14:15], v[130:131]\n"
    "v_pk_mul_f32 v[16:17], v[16:17], v[132:133]\n"
    "v_pk_mul_f32 v[18:19], v[18:19], v[134:135]\n"
    "v_exp_f32 v12, v12\n"
    "v_exp_f32 v13, v13\n"
    "v_exp_f32 v14, v14\n"
    "v_exp_f32 v15, v15\n"
    "v_exp_f32 v16, v16\n"
    "v_exp_f32 v17, v17\n"
    "v_exp_f32 v18, v18\n"
    "v_exp_f32 v19, v19\n"
    "v_pk_mul_f32 v[128:129], v[128:129], v[160:161]\n"
    "v_pk_mul_f32 v[130:131], v[130:131], v[162:163]\n"
    "v_pk_mul_f32 v[132:133], v[132:133], v[164:165]\n"
    "v_pk_mul_f32 v[134:135], v[134:135], v[166:167]\n"
    "v_pk_fma_f32 v[12:13], v[12:13], v[28:29], v[28:29] op_sel:[0,0,0] op_sel_hi:[1,0,0]\n"
    "v_pk_fma_f32 v[14:15], v[14:15], v[28:29], v[28:29] op_sel:[0,0,0] op_sel_hi:[1,0,0]\n"
    "v_pk_fma_f32 v[16:17], v[16:17], v[28:29], v[28:29] op_sel:[0,0,0] op_sel_hi:[1,0,0]\n"
    "v_pk_fma_f32 v[18:19], v[18:19], v[28:29], v[28:29] op_sel:[0,0,0] op_sel_hi:[1,0,0]\n"
    "v_rcp_f32 v12, v12\n"
    "v_rcp_f32 v13, v13\n"
    "v_rcp_f32 v14, v14\n"
    "v_rcp_f32 v15, v15\n"
    "v_rcp_f32 v16, v16\n"
    "v_rcp_f32 v17, v17\n"
    "v_rcp_f32 v18, v18\n"
    "v_rcp_f32 v19, v19\n"
    "v_pk_mul_f32 v[128:129], v[128:129], v[12:13]\n"
    "v_pk_mul_f32 v[130:131], v[130:131], v[14:15]\n"
    "v_pk_mul_f32 v[132:133], v[132:133], v[16:17]\n"
    "v_pk_mul_f32 v[134:135], v[134:135], v[18:19]\n"
    "v_cvt_pk_bf16_f32 v80, v128, v129\n"
    "v_cvt_pk_bf16_f32 v81, v130, v131\n"
    "v_cvt_pk_bf16_f32 v82, v132, v133\n"
    "v_cvt_pk_bf16_f32 v83, v134, v135\n"
    "v_accvgpr_read_b32 v160, a8\n"
    "v_accvgpr_read_b32 v161, a9\n"
    "buffer_store_dwordx4 v[36:39], v7, s[72:75], 0 offen offset:1024 sc1\n"
    "v_accvgpr_read_b32 v162, a10\n"
    "v_accvgpr_read_b32 v163, a11\n"
    "v_accvgpr_read_b32 v164, a12\n"
    "v_accvgpr_read_b32 v165, a13\n"
    "v_accvgpr_read_b32 v166, a14\n"
    "v_accvgpr_read_b32 v167, a15\n"
    "v_pk_mul_f32 v[12:13], v[136:137], v[136:137]\n"
    "v_pk_mul_f32 v[14:15], v[138:139], v[138:139]\n"
    "v_pk_mul_f32 v[16:17], v[140:141], v[140:141]\n"
    "v_pk_mul_f32 v[18:19], v[142:143], v[142:143]\n"
    "v_pk_fma_f32 v[12:13], v[12:13], s[10:11], s[10:11] op_sel:[0,0,1] op_sel_hi:[1,0,1]\n"
    "v_pk_fma_f32 v[14:15], v[14:15], s[10:11], s[10:11] op_sel:[0,0,1] op_sel_hi:[1,0,1]\n"
    "v_pk_fma_f32 v[16:17], v[16:17], s[10:11], s[10:11] op_sel:[0,0,1] op_sel_hi:[1,0,1]\n"
    "v_pk_fma_f32 v[18:19], v[18:19], s[10:11], s[10:11] op_sel:[0,0,1] op_sel_hi:[1,0,1]\n"
    "v_pk_mul_f32 v[12:13], v[12:13], v[136:137]\n"
    "v_pk_mul_f32 v[14:15], v[14:15], v[138:139]\n"
    "v_pk_mul_f32 v[16:17], v[16:17], v[140:141]\n"
    "v_pk_mul_f32 v[18:19], v[18:19], v[142:143]\n"
    "v_exp_f32 v12, v12\n"
    "v_exp_f32 v13, v13\n"
    "v_exp_f32 v14, v14\n"
    "v_exp_f32 v15, v15\n"
    "v_exp_f32 v16, v16\n"
    "v_exp_f32 v17, v17\n"
    "v_exp_f32 v18, v18\n"
    "v_exp_f32 v19, v19\n"
    "v_pk_mul_f32 v[136:137], v[136:137], v[160:161]\n"
    "v_pk_mul_f32 v[138:139], v[138:139], v[162:163]\n"
    "v_pk_mul_f32 v[140:141], v[140:141], v[164:165]\n"
    "v_pk_mul_f32 v[142:143], v[142:143], v[166:167]\n"
    "v_pk_fma_f32 v[12:13], v[12:13], v[28:29], v[28:29] op_sel:[0,0,0] op_sel_hi:[1,0,0]\n"
    "v_pk_fma_f32 v[14:15], v[14:15], v[28:29], v[28:29] op_sel:[0,0,0] op_sel_hi:[1,0,0]\n"
    "v_pk_fma_f32 v[16:17], v[16:17], v[28:29], v[28:29] op_sel:[0,0,0] op_sel_hi:[1,0,0]\n"
    "v_pk_fma_f32 v[18:19], v[18:19], v[28:29], v[28:29] op_sel:[0,0,0] op_sel_hi:[1,0,0]\n"
    "v_rcp_f32 v12, v12\n"
    "v_rcp_f32 v13, v13\n"
    "v_rcp_f32 v14, v14\n"
    "v_rcp_f32 v15, v15\n"
    "v_rcp_f32 v16, v16\n"
    "v_rcp_f32 v17, v17\n"
    "v_rcp_f32 v18, v18\n"
    "v_rcp_f32 v19, v19\n"
    "v_pk_mul_f32 v[136:137], v[136:137], v[12:13]\n"
    "v_pk_mul_f32 v[138:139], v[138:139], v[14:15]\n"
    "v_pk_mul_f32 v[140:141], v[140:141], v[16:17]\n"
    "v_pk_mul_f32 v[142:143], v[142:143], v[18:19]\n"
    "v_cvt_pk_bf16_f32 v84, v136, v137\n"
    "v_cvt_pk_bf16_f32 v85, v138, v139\n"
    "v_cvt_pk_bf16_f32 v86, v140, v141\n"
    "v_cvt_pk_bf16_f32 v87, v142, v143\n"
    "v_accvgpr_read_b32 v160, a16\n"
    "v_accvgpr_read_b32 v161, a17\n"
    "buffer_store_dwordx4 v[40:43], v7, s[72:75], 0 offen offset:2048 sc1\n"
    "v_accvgpr_read_b32 v162, a18\n"
    "v_accvgpr_read_b32 v163, a19\n"
    "v_accvgpr_read_b32 v164, a20\n"
    "v_accvgpr_read_b32 v165, a21\n"
    "v_accvgpr_read_b32 v166, a22\n"
    "v_accvgpr_read_b32 v167, a23\n"
    "v_pk_mul_f32 v[12:13], v[144:145], v[144:145]\n"
    "v_pk_mul_f32 v[14:15], v[146:147], v[146:147]\n"
    "v_pk_mul_f32 v[16:17], v[148:149], v[148:149]\n"
    "v_pk_mul_f32 v[18:19], v[150:151], v[150:151]\n"
    "v_pk_fma_f32 v[12:13], v[12:13], s[10:11], s[10:11] op_sel:[0,0,1] op_sel_hi:[1,0,1]\n"
    "v_pk_fma_f32 v[14:15], v[14:15], s[10:11], s[10:11] op_sel:[0,0,1] op_sel_hi:[1,0,1]\n"
    "v_pk_fma_f32 v[16:17], v[16:17], s[10:11], s[10:11] op_sel:[0,0,1] op_sel_hi:[1,0,1]\n"
    "v_pk_fma_f32 v[18:19], v[18:19], s[10:11], s[10:11] op_sel:[0,0,1] op_sel_hi:[1,0,1]\n"
    "v_pk_mul_f32 v[12:13], v[12:13], v[144:145]\n"
    "v_pk_mul_f32 v[14:15], v[14:15], v[146:147]\n"
    "v_pk_mul_f32 v[16:17], v[16:17], v[148:149]\n"
    "v_pk_mul_f32 v[18:19], v[18:19], v[150:151]\n"
    "v_exp_f32 v12, v12\n"
    "v_exp_f32 v13, v13\n"
    "v_exp_f32 v14, v14\n"
    "v_exp_f32 v15, v15\n"
    "v_exp_f32 v16, v16\n"
    "v_exp_f32 v17, v17\n"
    "v_exp_f32 v18, v18\n"
    "v_exp_f32 v19, v19\n"
    "v_pk_mul_f32 v[144:145], v[144:145], v[160:161]\n"
    "v_pk_mul_f32 v[146:147], v[146:147], v[162:163]\n"
    "v_pk_mul_f32 v[148:149], v[148:149], v[164:165]\n"
    "v_pk_mul_f32 v[150:151], v[150:151], v[166:167]\n"
    "v_pk_fma_f32 v[12:13], v[12:13], v[28:29], v[28:29] op_sel:[0,1,1] op_sel_hi:[1,1,1]\n"
    "v_pk_fma_f32 v[14:15], v[14:15], v[28:29], v[28:29] op_sel:[0,1,1] op_sel_hi:[1,1,1]\n"
    "v_pk_fma_f32 v[16:17], v[16:17], v[28:29], v[28:29] op_sel:[0,1,1] op_sel_hi:[1,1,1]\n"
    "v_pk_fma_f32 v[18:19], v[18:19], v[28:29], v[28:29] op_sel:[0,1,1] op_sel_hi:[1,1,1]\n"
    "v_rcp_f32 v12, v12\n"
    "v_rcp_f32 v13, v13\n"
    "v_rcp_f32 v14, v14\n"
    "v_rcp_f32 v15, v15\n"
    "v_rcp_f32 v16, v16\n"
    "v_rcp_f32 v17, v17\n"
    "v_rcp_f32 v18, v18\n"
    "v_rcp_f32 v19, v19\n"
    "v_pk_mul_f32 v[144:145], v[144:145], v[12:13]\n"
    "v_pk_mul_f32 v[146:147], v[146:147], v[14:15]\n"
    "v_pk_mul_f32 v[148:149], v[148:149], v[16:17]\n"
    "v_pk_mul_f32 v[150:151], v[150:151], v[18:19]\n"
    "v_cvt_pk_bf16_f32 v88, v144, v145\n"
    "v_cvt_pk_bf16_f32 v89, v146, v147\n"
    "v_cvt_pk_bf16_f32 v90, v148, v149\n"
    "v_cvt_pk_bf16_f32 v91, v150, v151\n"
    "v_accvgpr_read_b32 v160, a24\n"
    "v_accvgpr_read_b32 v161, a25\n"
    "buffer_store_dwordx4 v[44:47], v7, s[72:75], 0 offen offset:3072 sc1\n"
    "v_add_u32 v7, 4096, v7\n"
    "v_accvgpr_read_b32 v162, a26\n"
    "v_accvgpr_read_b32 v163, a27\n"
    "v_accvgpr_read_b32 v164, a28\n"
    "v_accvgpr_read_b32 v165, a29\n"
    "v_accvgpr_read_b32 v166, a30\n"
    "v_accvgpr_read_b32 v167, a31\n"
    "v_pk_mul_f32 v[12:13], v[152:153], v[152:153]\n"
    "v_pk_mul_f32 v[14:15], v[154:155], v[154:155]\n"
    "v_pk_mul_f32 v[16:17], v[156:157], v[156:157]\n"
    "v_pk_mul_f32 v[18:19], v[158:159], v[158:159]\n"
    "v_pk_fma_f32 v[12:13], v[12:13], s[10:11], s[10:11] op_sel:[0,0,1] op_sel_hi:[1,0,1]\n"
    "v_pk_fma_f32 v[14:15], v[14:15], s[10:11], s[10:11] op_sel:[0,0,1] op_sel_hi:[1,0,1]\n"
    "v_pk_fma_f32 v[16:17], v[16:17], s[10:11], s[10:11] op_sel:[0,0,1] op_sel_hi:[1,0,1]\n"
    "v_pk_fma_f32 v[18:19], v[18:19], s[10:11], s[10:11] op_sel:[0,0,1] op_sel_hi:[1,0,1]\n"
    "v_pk_mul_f32 v[12:13], v[12:13], v[152:153]\n"
    "v_pk_mul_f32 v[14:15], v[14:15], v[154:155]\n"
    "v_pk_mul_f32 v[16:17], v[16:17], v[156:157]\n"
    "v_pk_mul_f32 v[18:19], v[18:19], v[158:159]\n"
    "v_exp_f32 v12, v12\n"
    "v_exp_f32 v13, v13\n"
    "v_exp_f32 v14, v14\n"
    "v_exp_f32 v15, v15\n"
    "v_exp_f32 v16, v16\n"
    "v_exp_f32 v17, v17\n"
    "v_exp_f32 v18, v18\n"
    "v_exp_f32 v19, v19\n"
    "v_pk_mul_f32 v[152:153], v[152:153], v[160:161]\n"
    "v_pk_mul_f32 v[154:155], v[154:155], v[162:163]\n"
    "v_pk_mul_f32 v[156:157], v[156:157], v[164:165]\n"
    "v_pk_mul_f32 v[158:159], v[158:159], v[166:167]\n"
    "v_pk_fma_f32 v[12:13], v[12:13], v[28:29], v[28:29] op_sel:[0,1,1] op_sel_hi:[1,1,1]\n"
    "v_pk_fma_f32 v[14:15], v[14:15], v[28:29], v[28:29] op_sel:[0,1,1] op_sel_hi:[1,1,1]\n"
    "v_pk_fma_f32 v[16:17], v[16:17], v[28:29], v[28:29] op_sel:[0,1,1] op_sel_hi:[1,1,1]\n"
    "v_pk_fma_f32 v[18:19], v[18:19], v[28:29], v[28:29] op_sel:[0,1,1] op_sel_hi:[1,1,1]\n"
    "v_rcp_f32 v12, v12\n"
    "v_rcp_f32 v13, v13\n"
    "v_rcp_f32 v14, v14\n"
    "v_rcp_f32 v15, v15\n"
    "v_rcp_f32 v16, v16\n"
    "v_rcp_f32 v17, v17\n"
    "v_rcp_f32 v18, v18\n"
    "v_rcp_f32 v19, v19\n"
    "v_pk_mul_f32 v[152:153], v[152:153], v[12:13]\n"
    "v_pk_mul_f32 v[154:155], v[154:155], v[14:15]\n"
    "v_pk_mul_f32 v[156:157], v[156:157], v[16:17]\n"
    "v_pk_mul_f32 v[158:159], v[158:159], v[18:19]\n"
    "v_cvt_pk_bf16_f32 v92, v152, v153\n"
    "v_cvt_pk_bf16_f32 v93, v154, v155\n"
    "v_cvt_pk_bf16_f32 v94, v156, v157\n"
    "v_cvt_pk_bf16_f32 v95, v158, v159\n"
    "s_branch P2_start\n"
    "GRX_slow:\n"
    "s_mov_b32 s68, 0x40000\n"
    "GRX:\n"
    "s_lshl_b32 s94, s56, 8\n"
    "s_add_u32 s94, s94, 37748736\n"
    "s_movk_i32 s95, 0x300\n"
    "v_lshl_add_u32 v4, v31, 3, s94\n"
    "global_load_dwordx2 a[200:201], v4, s[52:53] offset:0 sc1\n"
    "global_load_dwordx2 a[202:203], v4, s[52:53] offset:32 sc1\n"
    "v_add_u32 v5, 256, v4\n"
    "v_bfi_b32 v4, s95, v5, v4\n"
    "global_load_dwordx2 a[204:205], v4, s[52:53] offset:0 sc1\n"
    "global_load_dwordx2 a[206:207], v4, s[52:53] offset:32 sc1\n"
    "v_add_u32 v5, 256, v4\n"
    "v_bfi_b32 v4, s95, v5, v4\n"
    "global_load_dwordx2 a[208:209], v4, s[52:53] offset:0 sc1\n"
    "global_load_dwordx2 a[210:211], v4, s[52:53] offset:32 sc1\n"
    "v_add_u32 v5, 256, v4\n"
    "v_bfi_b32 v4, s95, v5, v4\n"
    "global_load_dwordx2 a[212:213], v4, s[52:53] offset:0 sc1\n"
    "global_load_dwordx2 a[214:215], v4, s[52:53] offset:32 sc1\n"
    "s_waitcnt vmcnt(0)\n"
    "v_accvgpr_read_b32 v2, a201\n"
    "v_accvgpr_read_b32 v22, a200\n"
    "v_xor_b32 v2, s8, v2\n"
    "v_add_f32 v22, 1.0, v22\n"
    "v_accvgpr_read_b32 v3, a203\n"
    "v_accvgpr_read_b32 v23, a202\n"
    "v_xor_b32 v3, s8, v3\n"
    "v_add_f32 v23, 1.0, v23\n"
    "v_or_b32 v5, v2, v3\n"
    "v_accvgpr_read_b32 v2, a205\n"
    "v_accvgpr_read_b32 v24, a204\n"
    "v_xor_b32 v2, s8, v2\n"
    "v_add_f32 v24, 1.0, v24\n"
    "v_accvgpr_read_b32 v3, a207\n"
    "v_accvgpr_read_b32 v25, a206\n"
    "v_xor_b32 v3, s8, v3\n"
    "v_add_f32 v25, 1.0, v25\n"
    "v_or3_b32 v5, v2, v3, v5\n"
    "v_accvgpr_read_b32 v2, a209\n"
    "v_accvgpr_read_b32 v26, a208\n"
    "v_xor_b32 v2, s8, v2\n"
    "v_add_f32 v26, 1.0, v26\n"
    "v_accvgpr_read_b32 v3, a211\n"
    "v_accvgpr_read_b32 v27, a210\n"
    "v_xor_b32 v3, s8, v3\n"
    "v_add_f32 v27, 1.0, v27\n"
    "v_or3_b32 v5, v2, v3, v5\n"
    "v_accvgpr_read_b32 v2, a213\n"
    "v_accvgpr_read_b32 v28, a212\n"
    "v_xor_b32 v2, s8, v2\n"
    "v_add_f32 v28, 1.0, v28\n"
    "v_accvgpr_read_b32 v3, a215\n"
    "v_accvgpr_read_b32 v29, a214\n"
    "v_xor_b32 v3, s8, v3\n"
    "v_add_f32 v29, 1.0, v29\n"
    "v_or3_b32 v5, v2, v3, v5\n"
    "v_cmp_ne_u32 vcc, 0, v5\n"
    "s_cbranch_vccz GRX_ok\n"
    "s_sleep 1\n"
    "s_sub_u32 s68, s68, 1\n"
    "s_cbranch_scc0 GRX\n"
    "GRX_ok:\n"
    "s_branch GRX_back\n"
    "GRZ_slow:\n"
    "s_mov_b32 s68, 0x40000\n"
    "GRZ:\n"
    "s_lshl_b32 s94, s56, 8\n"
    "s_add_u32 s94, s94, 37748736\n"
    "s_movk_i32 s95, 0x300\n"
    "v_lshl_add_u32 v4, v31, 3, s94\n"
    "global_load_dwordx2 a[200:201], v4, s[52:53] offset:0 sc1\n"
    "global_load_dwordx2 a[202:203], v4, s[52:53] offset:32 sc1\n"
    "v_add_u32 v5, 256, v4\n"
    "v_bfi_b32 v4, s95, v5, v4\n"
    "global_load_dwordx2 a[204:205], v4, s[52:53] offset:0 sc1\n"
    "global_load_dwordx2 a[206:207], v4, s[52:53] offset:32 sc1\n"
    "v_add_u32 v5, 256, v4\n"
    "v_bfi_b32 v4, s95, v5, v4\n"
    "global_load_dwordx2 a[208:209], v4, s[52:53] offset:0 sc1\n"
    "global_load_dwordx2 a[210:211], v4, s[52:53] offset:32 sc1\n"
    "v_add_u32 v5, 256, v4\n"
    "v_bfi_b32 v4, s95, v5, v4\n"
    "global_load_dwordx2 a[212:213], v4, s[52:53] offset:0 sc1\n"
    "global_load_dwordx2 a[214:215], v4, s[52:53] offset:32 sc1\n"
    "s_waitcnt vmcnt(0)\n"
    "v_accvgpr_read_b32 v2, a201\n"
    "v_accvgpr_read_b32 v22, a200\n"
    "v_xor_b32 v2, s8, v2\n"
    "v_add_f32 v22, 1.0, v22\n"
    "v_accvgpr_read_b32 v3, a203\n"
    "v_accvgpr_read_b32 v23, a202\n"
    "v_xor_b32 v3, s8, v3\n"
    "v_add_f32 v23, 1.0, v23\n"
    "v_or_b32 v5, v2, v3\n"
    "v_accvgpr_read_b32 v2, a205\n"
    "v_accvgpr_read_b32 v24, a204\n"
    "v_xor_b32 v2, s8, v2\n"
    "v_add_f32 v24, 1.0, v24\n"
    "v_accvgpr_read_b32 v3, a207\n"
    "v_accvgpr_read_b32 v25, a206\n"
    "v_xor_b32 v3, s8, v3\n"
    "v_add_f32 v25, 1.0, v25\n"
    "v_or3_b32 v5, v2, v3, v5\n"
    "v_accvgpr_read_b32 v2, a209\n"
    "v_accvgpr_read_b32 v26, a208\n"
    "v_xor_b32 v2, s8, v2\n"
    "v_add_f32 v26, 1.0, v26\n"
    "v_accvgpr_read_b32 v3, a211\n"
    "v_accvgpr_read_b32 v27, a210\n"
    "v_xor_b32 v3, s8, v3\n"
    "v_add_f32 v27, 1.0, v27\n"
    "v_or3_b32 v5, v2, v3, v5\n"
    "v_accvgpr_read_b32 v2, a213\n"
    "v_accvgpr_read_b32 v28, a212\n"
    "v_xor_b32 v2, s8, v2\n"
    "v_add_f32 v28, 1.0, v28\n"
    "v_accvgpr_read_b32 v3, a215\n"
    "v_accvgpr_read_b32 v29, a214\n"
    "v_xor_b32 v3, s8, v3\n"
    "v_add_f32 v29, 1.0, v29\n"
    "v_or3_b32 v5, v2, v3, v5\n"
    "v_cmp_ne_u32 vcc, 0, v5\n"
    "s_cbranch_vccz GRZ_ok\n"
    "s_sleep 1\n"
    "s_sub_u32 s68, s68, 1\n"
    "s_cbranch_scc0 GRZ\n"
    "GRZ_ok:\n"
    "s_branch GRZ_back\n"
    "P1Z:\n"
    "s_mov_b32 s84, 15360\n"
    "s_mov_b32 s85, 30720\n"
    "s_mov_b32 s86, 46080\n"
    "s_mov_b32 s87, 65536\n"
    "s_mov_b32 s88, 80896\n"
    "s_mov_b32 s89, 96256\n"
    "s_mov_b32 s90, 111616\n"
    "s_mov_b32 s91, 131072\n"
    "s_mul_i32 s95, s55, 147456\n"
    "s_add_u32 s94, s55, 0\n"
    "s_and_b32 s94, s94, 1\n"
    "s_lshl_b32 s94, s94, 2\n"
    "v_lshrrev_b32 v1, 4, v0\n"
    "v_add_u32 v1, s94, v1\n"
    "v_and_b32 v2, 7, v0\n"
    "v_xor_b32 v1, v1, v2\n"
    "v_lshlrev_b32 v1, 4, v1\n"
    "v_lshrrev_b32 v2, 3, v0\n"
    "v_lshl_add_u32 v10, v2, 11, v1\n"
    "v_add_u32 v10, s95, v10\n"
    "s_add_u32 s94, s55, 1\n"
    "s_and_b32 s94, s94, 1\n"
    "s_lshl_b32 s94, s94, 2\n"
    "v_lshrrev_b32 v1, 4, v0\n"
    "v_add_u32 v1, s94, v1\n"
    "v_and_b32 v2, 7, v0\n"
    "v_xor_b32 v1, v1, v2\n"
    "v_lshlrev_b32 v1, 4, v1\n"
    "v_lshrrev_b32 v2, 3, v0\n"
    "v_lshl_add_u32 v11, v2, 11, v1\n"
    "v_add_u32 v11, s95, v11\n"
    "s_mul_i32 s98, s55, 9216\n"
    "s_lshl_b32 s94, s56, 3\n"
    "s_add_u32 s94, s94, s58\n"
    "s_lshl_b32 s94, s94, 2\n"
    "s_add_u32 s94, s94, s55\n"
    "s_lshl_b32 s95, s57, 7\n"
    "s_add_u32 s11, s94, s95\n"
    "s_lshl_b32 s95, s11, 12\n"
    "v_lshlrev_b32 v1, 5, v0\n"
    "v_add_u32 v2, s95, v1\n"
    "v_mov_b32 v3, 37781504\n"
    "s_lshl_b32 s94, s57, 19\n"
    "s_waitcnt lgkmcnt(0)\n"
    "global_load_dwordx2 v[6:7], v3, s[52:53] sc1\n"
    "global_load_dwordx4 v[22:25], v1, s[48:49]\n"
    "global_load_dwordx4 v[26:29], v1, s[48:49] offset:16\n"
    "global_load_dwordx4 v[32:35], v2, s[32:33]\n"
    "global_load_dwordx4 v[36:39], v2, s[32:33] offset:16\n"
    "global_load_dwordx4 v[40:43], v2, s[32:33] offset:2048\n"
    "global_load_dwordx4 v[44:47], v2, s[32:33] offset:2064\n"
    "s_add_u32 s60, s32, s94\n"
    "s_addc_u32 s61, s33, 0\n"
    "s_and_b32 s61, s61, 0xffff\n"
    "s_mov_b32 s62, 522368\n"
    "s_mov_b32 s63, 0x20000\n"
    "s_cmp_lt_u32 s56, 2\n"
    "s_cselect_b64 s[38:39], s[38:39], s[40:41]\n"
    "s_cselect_b64 s[34:35], s[34:35], s[36:37]\n"
    "s_and_b32 s94, s56, 1\n"
    "s_mul_i32 s94, s94, 589824\n"
    "s_add_u32 s94, s94, 917504\n"
    "s_add_u32 s64, s38, s94\n"
    "s_addc_u32 s65, s39, 0\n"
    "s_and_b32 s65, s65, 0xffff\n"
    "s_mov_b32 s66, 587904\n"
    "s_mov_b32 s67, 0x20000\n"
    "s_add_u32 m0, s92, 0\n"
    "s_nop 0\n"
    "buffer_load_dwordx4 v8, s[60:63], s76 offen offset:0  lds\n"
    "buffer_load_dwordx4 v9, s[60:63], s77 offen offset:1024  lds\n"
    "buffer_load_dwordx4 v8, s[60:63], s78 offen offset:2048  lds\n"
    "buffer_load_dwordx4 v9, s[60:63], s79 offen offset:3072  lds\n"
    "v_lshlrev_b32 v21, 4, v0\n"
    "s_lshr_b32 s94, s55, 1\n"
    "s_lshl_b32 s94, s94, 14\n"
    "v_and_b32 v1, 15, v0\n"
    "s_add_u32 m0, s92, 4096\n"
    "s_nop 0\n"
    "buffer_load_dwordx4 v8, s[60:63], s80 offen offset:0  lds\n"
    "v_lshrrev_b32 v2, 4, v0\n"
    "v_lshrrev_b32 v3, 1, v1\n"
    "v_xor_b32 v2, v2, v3\n"
    "v_lshlrev_b32 v2, 4, v2\n"
    "buffer_load_dwordx4 v9, s[60:63], s81 offen offset:1024  lds\n"
    "v_lshl_add_u32 v2, v1, 7, v2\n"
    "v_add_u32 v13, s94, v2\n"
    "v_xor_b32 v2, 64, v2\n"
    "v_add_u32 v14, s94, v2\n"
    "buffer_load_dwordx4 v8, s[60:63], s82 offen offset:2048  lds\n"
    "s_and_b32 s94, s55, 1\n"
    "s_mul_i32 s94, s94, 18432\n"
    "s_add_u32 s94, s94, 32768\n"
    "v_and_b32 v1, 15, v0\n"
    "buffer_load_dwordx4 v9, s[60:63], s83 offen offset:3072  lds\n"
    "v_lshrrev_b32 v2, 4, v0\n"
    "v_lshrrev_b32 v3, 1, v1\n"
    "v_xor_b32 v2, v2, v3\n"
    "v_lshlrev_b32 v2, 4, v2\n"
    "v_lshl_add_u32 v2, v1, 7, v2\n"
    "s_add_u32 m0, s98, 32768\n"
    "s_nop 0\n"
    "buffer_load_dwordx4 v10, s[64:67], 0 offen offset:0  lds\n"
    "v_add_u32 v15, s94, v2\n"
    "v_xor_b32 v2, 64, v2\n"
    "v_add_u32 v16, s94, v2\n"
    "v_and_b32 v1, 15, v0\n"
    "buffer_load_dwordx4 v11, s[64:67], s84 offen offset:1024  lds\n"
    "v_and_b32 v2, 12, v0\n"
    "s_lshl_b32 s94, s57, 1\n"
    "s_lshr_b32 s95, s55, 1\n"
    "s_add_u32 s94, s94, s95\n"
    "buffer_load_dwordx4 v10, s[64:67], s85 offen offset:2048  lds\n"
    "s_lshl_b32 s94, s94, 7\n"
    "v_add3_u32 v31, v1, v2, s94\n"
    "v_lshrrev_b32 v2, 4, v0\n"
    "v_lshlrev_b32 v1, 7, v1\n"
    "buffer_load_dwordx4 v11, s[64:67], s86 offen offset:3072  lds\n"
    "v_lshl_add_u32 v30, v2, 24, v1\n"
    "s_lshr_b32 s94, s54, 3\n"
    "s_lshl_b32 s95, s56, 5\n"
    "s_add_u32 s94, s94, s95\n"
    "s_add_u32 m0, s98, 36864\n"
    "s_nop 0\n"
    "buffer_load_dwordx4 v10, s[64:67], s87 offen offset:0  lds\n"
    "s_lshl_b32 s94, s94, 1\n"
    "s_lshr_b32 s95, s55, 1\n"
    "s_add_u32 s94, s94, s95\n"
    "s_mul_i32 s94, s94, 14\n"
    "s_and_b32 s95, s55, 1\n"
    "buffer_load_dwordx4 v11, s[64:67], s88 offen offset:1024  lds\n"
    "s_add_u32 s94, s94, s95\n"
    "s_lshl_b32 s94, s94, 11\n"
    "s_add_u32 s12, s46, s94\n"
    "s_addc_u32 s13, s47, 0\n"
    "s_and_b32 s13, s13, 0xffff\n"
    "buffer_load_dwordx4 v10, s[64:67], s89 offen offset:2048  lds\n"
    "s_mov_b32 s14, 65536\n"
    "s_mov_b32 s15, 0x20000\n"
    "s_lshl_b32 s94, s57, 3\n"
    "s_add_u32 s94, s94, s58\n"
    "buffer_load_dwordx4 v11, s[64:67], s90 offen offset:3072  lds\n"
    "s_lshl_b32 s94, s94, 2\n"
    "s_add_u32 s94, s94, s55\n"
    "s_lshl_b32 s94, s94, 4\n"
    "s_and_b32 s95, s56, 1\n"
    "s_add_u32 m0, s98, 40960\n"
    "s_nop 0\n"
    "buffer_load_dwordx4 v10, s[64:67], s91 offen offset:0  lds\n"
    "s_add_u32 s94, s94, s95\n"
    "s_lshl_b32 s94, s94, 11\n"
    "s_add_u32 s4, s34, s94\n"
    "s_addc_u32 s5, s35, 0\n"
    "s_sub_u32 s4, s4, 28672\n"
    "s_subb_u32 s5, s5, 0\n"
    "s_and_b32 s5, s5, 0xffff\n"
    "s_add_u32 s60, s60, 128\n"
    "s_addc_u32 s61, s61, 0\n"
    "s_add_u32 s64, s64, 128\n"
    "s_addc_u32 s65, s65, 0\n"
    "s_add_u32 s92, s92, s93\n"
    "s_add_u32 s98, s98, s93\n"
    "s_sub_u32 s93, 0, s93\n"
    "s_mov_b64 exec, 0\n"
    "s_cmp_eq_u32 s55, 0\n"
    "s_cbranch_scc1 TC0\n"
    "s_cmp_eq_u32 s55, 1\n"
    "s_cbranch_scc1 TC1\n"
    "s_cmp_eq_u32 s55, 2\n"
    "s_cbranch_scc1 TC2\n"
    "s_branch TC3\n"
    "TC_retZ:\n"
    "s_mov_b64 exec, -1\n"
    "s_waitcnt vmcnt(17)\n"
    "v_readfirstlane_b32 s94, v6\n"
    "v_readfirstlane_b32 s95, v7\n"
    "v_mov_b32 v4, 0\n"
    "v_mov_b32 v5, 0\n"
    "s_mul_i32 s9, s95, 0x4c957f2d\n"
    "s_mul_hi_u32 s95, s94, 0x4c957f2d\n"
    "s_add_u32 s9, s9, s95\n"
    "s_mul_i32 s95, s94, 0x5851f42d\n"
    "s_add_u32 s9, s9, s95\n"
    "s_mul_i32 s8, s94, 0x4c957f2d\n"
    "s_add_u32 s8, s8, 0xf767814f\n"
    "s_addc_u32 s9, s9, 0x14057b7e\n"
    "v_dot2_f32_bf16 v4, v32, v22, v4\n"
    "v_dot2_f32_bf16 v5, v40, v22, v5\n"
    "v_dot2_f32_bf16 v4, v33, v23, v4\n"
    "v_dot2_f32_bf16 v5, v41, v23, v5\n"
    "v_dot2_f32_bf16 v4, v34, v24, v4\n"
    "TC3:\n"
    "s_cbranch_execz TC7\n"
    "v_dot2_f32_bf16 v5, v42, v24, v5\n"
    "v_dot2_f32_bf16 v4, v35, v25, v4\n"
    "v_dot2_f32_bf16 v5, v43, v25, v5\n"
    "v_dot2_f32_bf16 v4, v36, v26, v4\n"
    "v_dot2_f32_bf16 v5, v44, v26, v5\n"
    "v_dot2_f32_bf16 v4, v37, v27, v4\n"
    "v_dot2_f32_bf16 v5, v45, v27, v5\n"
    "v_dot2_f32_bf16 v4, v38, v28, v4\n"
    "v_dot2_f32_bf16 v5, v46, v28, v5\n"
    "v_dot2_f32_bf16 v4, v39, v29, v4\n"
    "v_dot2_f32_bf16 v5, v47, v29, v5\n"
    "s_nop 2\n"
    "v_add_f32_dpp v4, v4, v4 row_ror:8 row_mask:0xf bank_mask:0xf\n"
    "v_add_f32_dpp v5, v5, v5 row_ror:8 row_mask:0xf bank_mask:0xf\n"
    "s_nop 0\n"
    "v_add_f32_dpp v4, v4, v4 row_ror:4 row_mask:0xf bank_mask:0xf\n"
    "v_add_f32_dpp v5, v5, v5 row_ror:4 row_mask:0xf bank_mask:0xf\n"
    "s_nop 0\n"
    "v_add_f32_dpp v4, v4, v4 row_ror:2 row_mask:0xf bank_mask:0xf\n"
    "v_add_f32_dpp v5, v5, v5 row_ror:2 row_mask:0xf bank_mask:0xf\n"
    "s_nop 0\n"
    "v_add_f32_dpp v4, v4, v4 row_ror:1 row_mask:0xf bank_mask:0xf\n"
    "v_add_f32_dpp v5, v5, v5 row_ror:1 row_mask:0xf bank_mask:0xf\n"
    "s_nop 0\n"
    "v_mov_b32 v6, v4\n"
    "v_mov_b32 v7, v5\n"
    "s_nop 1\n"
    "v_permlane16_swap_b32 v4, v6\n"
    "v_permlane16_swap_b32 v5, v7\n"
    "s_nop 1\n"
    "v_add_f32 v4, v4, v6\n"
    "v_add_f32 v5, v5, v7\n"
    "v_mov_b32 v6, v4\n"
    "v_mov_b32 v7, v5\n"
    "s_nop 1\n"
    "v_permlane32_swap_b32 v4, v6\n"
    "v_permlane32_swap_b32 v5, v7\n"
    "s_nop 1\n"
    "v_add_f32 v4, v4, v6\n"
    "v_add_f32 v5, v5, v7\n"
    "v_cmp_eq_u32 vcc, 0, v0\n"
    "s_lshl_b32 s95, s11, 4\n"
    "s_add_u32 s95, s95, 37748736\n"
    "v_cndmask_b32 v4, v5, v4, vcc\n"
    "v_mul_f32 v4, 0x3fb8aa3b, v4\n"
    "v_exp_f32 v4, v4\n"
    "v_lshl_add_u32 v1, v0, 3, s95\n"
    "v_mov_b32 v5, s8\n"
    "v_cmp_lt_f32 vcc, 0x7149f2ca, v4\n"
    "v_mov_b32 v2, 0x7149f2ca\n"
    "s_nop 0\n"
    "v_cndmask_b32 v4, v4, v2, vcc\n"
    "s_mov_b64 exec, 3\n"
    "global_store_dwordx2 v1, v[4:5], s[52:53] sc1\n"
    "s_mov_b64 exec, -1\n"
    "s_waitcnt vmcnt(1)\n"
    "s_barrier\n"
    "ds_read_b128 a[64:67], v13 offset:0\n"
    "ds_read_b128 a[96:99], v15 offset:0\n"
    "ds_read_b128 a[100:103], v15 offset:2048\n"
    "ds_read_b128 a[104:107], v15 offset:4096\n"
    "ds_read_b128 a[108:111], v15 offset:6144\n"
    "ds_read_b128 a[112:115], v15 offset:8192\n"
    "ds_read_b128 a[116:119], v15 offset:10240\n"
    "ds_read_b128 a[120:123], v15 offset:12288\n"
    "ds_read_b128 a[124:127], v15 offset:14336\n"
    "ds_read_b128 a[128:131], v15 offset:16384\n"
    "ds_read_b128 a[68:71], v13 offset:2048\n"
    "ds_read_b128 a[72:75], v13 offset:4096\n"
    "ds_read_b128 a[76:79], v13 offset:6144\n"
    "ds_read_b128 a[80:83], v13 offset:8192\n"
    "ds_read_b128 a[84:87], v13 offset:10240\n"
    "ds_read_b128 a[88:91], v13 offset:12288\n"
    "ds_read_b128 a[92:95], v13 offset:14336\n"
    "s_waitcnt lgkmcnt(7)\n"
    "s_add_u32 m0, s92, 0\n"
    "v_mfma_f32_16x16x32_bf16 v[32:35], a[64:67], a[96:99], 0\n"
    "buffer_load_dwordx4 v8, s[60:63], s76 offen offset:0  lds\n"
    "v_mfma_f32_16x16x32_bf16 v[64:67], a[64:67], a[100:103], 0\n"
    "v_mfma_f32_16x16x32_bf16 v[96:99], a[64:67], a[104:107], 0\n"
    "v_mfma_f32_16x16x32_bf16 v[128:131], a[64:67], a[108:111], 0\n"
    "buffer_load_dwordx4 v9, s[60:63], s77 offen offset:1024  lds\n"
    "v_mfma_f32_16x16x32_bf16 v[160:163], a[64:67], a[112:115], 0\n"
    "v_mfma_f32_16x16x32_bf16 v[192:195], a[64:67], a[116:119], 0\n"
    "v_mfma_f32_16x16x32_bf16 v[224:227], a[64:67], a[120:123], 0\n"
    "buffer_load_dwordx4 v8, s[60:63], s78 offen offset:2048  lds\n"
    "v_mfma_f32_16x16x32_bf16 a[0:3], a[64:67], a[124:127], 0\n"
    "v_mfma_f32_16x16x32_bf16 a[32:35], a[64:67], a[128:131], 0\n"
    "s_waitcnt lgkmcnt(0)\n"
    "ds_read_b128 a[132:135], v14 offset:0\n"
    "v_mfma_f32_16x16x32_bf16 a[36:39], a[68:71], a[128:131], 0\n"
    "ds_read_b128 a[164:167], v16 offset:0\n"
    "buffer_load_dwordx4 v9, s[60:63], s79 offen offset:3072  lds\n"
    "v_mfma_f32_16x16x32_bf16 a[4:7], a[68:71], a[124:127], 0\n"
    "ds_read_b128 a[168:171], v16 offset:2048\n"
    "v_mfma_f32_16x16x32_bf16 v[228:231], a[68:71], a[120:123], 0\n"
    "ds_read_b128 a[172:175], v16 offset:4096\n"
    "s_add_u32 m0, s92, 4096\n"
    "v_mfma_f32_16x16x32_bf16 v[196:199], a[68:71], a[116:119], 0\n"
    "ds_read_b128 a[176:179], v16 offset:6144\n"
    "buffer_load_dwordx4 v8, s[60:63], s80 offen offset:0  lds\n"
    "v_mfma_f32_16x16x32_bf16 v[164:167], a[68:71], a[112:115], 0\n"
    "ds_read_b128 a[180:183], v16 offset:8192\n"
    "v_mfma_f32_16x16x32_bf16 v[132:135], a[68:71], a[108:111], 0\n"
    "ds_read_b128 a[184:187], v16 offset:10240\n"
    "v_mfma_f32_16x16x32_bf16 v[100:103], a[68:71], a[104:107], 0\n"
    "ds_read_b128 a[188:191], v16 offset:12288\n"
    "buffer_load_dwordx4 v9, s[60:63], s81 offen offset:1024  lds\n"
    "v_mfma_f32_16x16x32_bf16 v[68:71], a[68:71], a[100:103], 0\n"
    "ds_read_b128 a[192:195], v16 offset:14336\n"
    "v_mfma_f32_16x16x32_bf16 v[36:39], a[68:71], a[96:99], 0\n"
    "ds_read_b128 a[196:199], v16 offset:16384\n"
    "v_mfma_f32_16x16x32_bf16 v[40:43], a[72:75], a[96:99], 0\n"
    "ds_read_b128 a[136:139], v14 offset:2048\n"
    "buffer_load_dwordx4 v8, s[60:63], s82 offen offset:2048  lds\n"
    "v_mfma_f32_16x16x32_bf16 v[72:75], a[72:75], a[100:103], 0\n"
    "ds_read_b128 a[140:143], v14 offset:4096\n"
    "v_mfma_f32_16x16x32_bf16 v[104:107], a[72:75], a[104:107], 0\n"
    "ds_read_b128 a[144:147], v14 offset:6144\n"
    "v_mfma_f32_16x16x32_bf16 v[136:139], a[72:75], a[108:111], 0\n"
    "ds_read_b128 a[148:151], v14 offset:8192\n"
    "buffer_load_dwordx4 v9, s[60:63], s83 offen offset:3072  lds\n"
    "v_mfma_f32_16x16x32_bf16 v[168:171], a[72:75], a[112:115], 0\n"
    "ds_read_b128 a[152:155], v14 offset:10240\n"
    "v_mfma_f32_16x16x32_bf16 v[200:203], a[72:75], a[116:119], 0\n"
    "ds_read_b128 a[156:159], v14 offset:12288\n"
    "s_add_u32 m0, s98, 32768\n"
    "v_mfma_f32_16x16x32_bf16 v[232:235], a[72:75], a[120:123], 0\n"
    "ds_read_b128 a[160:163], v14 offset:14336\n"
    "buffer_load_dwordx4 v10, s[64:67], 0 offen offset:0  lds\n"
    "v_mfma_f32_16x16x32_bf16 a[8:11], a[72:75], a[124:127], 0\n"
    "v_mfma_f32_16x16x32_bf16 a[40:43], a[72:75], a[128:131], 0\n"
    "v_mfma_f32_16x16x32_bf16 a[44:47], a[76:79], a[128:131], 0\n"
    "buffer_load_dwordx4 v11, s[64:67], s84 offen offset:1024  lds\n"
    "v_mfma_f32_16x16x32_bf16 a[12:15], a[76:79], a[124:127], 0\n"
    "v_mfma_f32_16x16x32_bf16 v[236:239], a[76:79], a[120:123], 0\n"
    "v_mfma_f32_16x16x32_bf16 v[204:207], a[76:79], a[116:119], 0\n"
    "buffer_load_dwordx4 v10, s[64:67], s85 offen offset:2048  lds\n"
    "v_mfma_f32_16x16x32_bf16 v[172:175], a[76:79], a[112:115], 0\n"
    "v_mfma_f32_16x16x32_bf16 v[140:143], a[76:79], a[108:111], 0\n"
    "v_mfma_f32_16x16x32_bf16 v[108:111], a[76:79], a[104:107], 0\n"
    "buffer_load_dwordx4 v11, s[64:67], s86 offen offset:3072  lds\n"
    "v_mfma_f32_16x16x32_bf16 v[76:79], a[76:79], a[100:103], 0\n"
    "v_mfma_f32_16x16x32_bf16 v[44:47], a[76:79], a[96:99], 0\n"
    "s_add_u32 m0, s98, 36864\n"
    "v_mfma_f32_16x16x32_bf16 v[48:51], a[80:83], a[96:99], 0\n"
    "buffer_load_dwordx4 v10, s[64:67], s87 offen offset:0  lds\n"
    "v_mfma_f32_16x16x32_bf16 v[80:83], a[80:83], a[100:103], 0\n"
    "v_mfma_f32_16x16x32_bf16 v[112:115], a[80:83], a[104:107], 0\n"
    "v_mfma_f32_16x16x32_bf16 v[144:147], a[80:83], a[108:111], 0\n"
    "buffer_load_dwordx4 v11, s[64:67], s88 offen offset:1024  lds\n"
    "v_mfma_f32_16x16x32_bf16 v[176:179], a[80:83], a[112:115], 0\n"
    "v_mfma_f32_16x16x32_bf16 v[208:211], a[80:83], a[116:119], 0\n"
    "s_waitcnt lgkmcnt(0)\n"
    "s_barrier\n"
    "v_mfma_f32_16x16x32_bf16 v[240:243], a[80:83], a[120:123], 0\n"
    "buffer_load_dwordx4 v10, s[64:67], s89 offen offset:2048  lds\n"
    "v_mfma_f32_16x16x32_bf16 a[16:19], a[80:83], a[124:127], 0\n"
    "v_mfma_f32_16x16x32_bf16 a[48:51], a[80:83], a[128:131], 0\n"
    "v_mfma_f32_16x16x32_bf16 a[52:55], a[84:87], a[128:131], 0\n"
    "buffer_load_dwordx4 v11, s[64:67], s90 offen offset:3072  lds\n"
    "v_mfma_f32_16x16x32_bf16 a[20:23], a[84:87], a[124:127], 0\n"
    "v_mfma_f32_16x16x32_bf16 v[244:247], a[84:87], a[120:123], 0\n"
    "s_add_u32 m0, s98, 40960\n"
    "v_mfma_f32_16x16x32_bf16 v[212:215], a[84:87], a[116:119], 0\n"
    "buffer_load_dwordx4 v10, s[64:67], s91 offen offset:0  lds\n"
    "v_mfma_f32_16x16x32_bf16 v[180:183], a[84:87], a[112:115], 0\n"
    "s_add_u32 s60, s60, 128\n"
    "s_addc_u32 s61, s61, 0\n"
    "s_add_u32 s64, s64, 128\n"
    "s_addc_u32 s65, s65, 0\n"
    "v_mfma_f32_16x16x32_bf16 v[148:151], a[84:87], a[108:111], 0\n"
    "s_add_u32 s92, s92, s93\n"
    "s_add_u32 s98, s98, s93\n"
    "v_mfma_f32_16x16x32_bf16 v[116:119], a[84:87], a[104:107], 0\n"
    "s_sub_u32 s93, 0, s93\n"
    "v_mfma_f32_16x16x32_bf16 v[84:87], a[84:87], a[100:103], 0\n"
    "v_mfma_f32_16x16x32_bf16 v[52:55], a[84:87], a[96:99], 0\n"
    "v_mfma_f32_16x16x32_bf16 v[56:59], a[88:91], a[96:99], 0\n"
    "s_add_u32 m0, s92, 0\n"
    "v_mfma_f32_16x16x32_bf16 v[88:91], a[88:91], a[100:103], 0\n"
    "buffer_load_dwordx4 v8, s[60:63], s76 offen offset:0  lds\n"
    "v_mfma_f32_16x16x32_bf16 v[120:123], a[88:91], a[104:107], 0\n"
    "v_add_u32 v13, s93, v13\n"
    "v_add_u32 v14, s93, v14\n"
    "v_mfma_f32_16x16x32_bf16 v[152:155], a[88:91], a[108:111], 0\n"
    "v_add_u32 v15, s93, v15\n"
    "v_add_u32 v16, s93, v16\n"
    "v_mfma_f32_16x16x32_bf16 v[184:187], a[88:91], a[112:115], 0\n"
    "v_mfma_f32_16x16x32_bf16 v[216:219], a[88:91], a[116:119], 0\n"
    "v_mfma_f32_16x16x32_bf16 v[248:251], a[88:91], a[120:123], 0\n"
    "buffer_load_dwordx4 v9, s[60:63], s77 offen offset:1024  lds\n"
    "v_mfma_f32_16x16x32_bf16 a[24:27], a[88:91], a[124:127], 0\n"
    "v_mfma_f32_16x16x32_bf16 a[56:59], a[88:91], a[128:131], 0\n"
    "v_mfma_f32_16x16x32_bf16 a[60:63], a[92:95], a[128:131], 0\n"
    "v_mfma_f32_16x16x32_bf16 a[28:31], a[92:95], a[124:127], 0\n"
    "v_mfma_f32_16x16x32_bf16 v[252:255], a[92:95], a[120:123], 0\n"
    "buffer_load_dwordx4 v8, s[60:63], s78 offen offset:2048  lds\n"
    "v_mfma_f32_16x16x32_bf16 v[220:223], a[92:95], a[116:119], 0\n"
    "v_mfma_f32_16x16x32_bf16 v[188:191], a[92:95], a[112:115], 0\n"
    "v_mfma_f32_16x16x32_bf16 v[156:159], a[92:95], a[108:111], 0\n"
    "v_mfma_f32_16x16x32_bf16 v[124:127], a[92:95], a[104:107], 0\n"
    "v_mfma_f32_16x16x32_bf16 v[92:95], a[92:95], a[100:103], 0\n"
    "buffer_load_dwordx4 v9, s[60:63], s79 offen offset:3072  lds\n"
    "v_mfma_f32_16x16x32_bf16 v[60:63], a[92:95], a[96:99], 0\n"
    "v_mfma_f32_16x16x32_bf16 v[32:35], a[132:135], a[164:167], v[32:35]\n"
    "v_mfma_f32_16x16x32_bf16 v[64:67], a[132:135], a[168:171], v[64:67]\n"
    "v_mfma_f32_16x16x32_bf16 v[96:99], a[132:135], a[172:175], v[96:99]\n"
    "s_add_u32 m0, s92, 4096\n"
    "v_mfma_f32_16x16x32_bf16 v[128:131], a[132:135], a[176:179], v[128:131]\n"
    "buffer_load_dwordx4 v8, s[60:63], s80 offen offset:0  lds\n"
    "v_mfma_f32_16x16x32_bf16 v[160:163], a[132:135], a[180:183], v[160:163]\n"
    "v_mfma_f32_16x16x32_bf16 v[192:195], a[132:135], a[184:187], v[192:195]\n"
    "v_mfma_f32_16x16x32_bf16 v[224:227], a[132:135], a[188:191], v[224:227]\n"
    "v_mfma_f32_16x16x32_bf16 a[0:3], a[132:135], a[192:195], a[0:3]\n"
    "v_mfma_f32_16x16x32_bf16 a[32:35], a[132:135], a[196:199], a[32:35]\n"
    "buffer_load_dwordx4 v9, s[60:63], s81 offen offset:1024  lds\n"
    "v_mfma_f32_16x16x32_bf16 a[36:39], a[136:139], a[196:199], a[36:39]\n"
    "v_mfma_f32_16x16x32_bf16 a[4:7], a[136:139], a[192:195], a[4:7]\n"
    "v_mfma_f32_16x16x32_bf16 v[228:231], a[136:139], a[188:191], v[228:231]\n"
    "v_mfma_f32_16x16x32_bf16 v[196:199], a[136:139], a[184:187], v[196:199]\n"
    "v_mfma_f32_16x16x32_bf16 v[164:167], a[136:139], a[180:183], v[164:167]\n"
    "buffer_load_dwordx4 v8, s[60:63], s82 offen offset:2048  lds\n"
    "v_mfma_f32_16x16x32_bf16 v[132:135], a[136:139], a[176:179], v[132:135]\n"
    "v_mfma_f32_16x16x32_bf16 v[100:103], a[136:139], a[172:175], v[100:103]\n"
    "v_mfma_f32_16x16x32_bf16 v[68:71], a[136:139], a[168:171], v[68:71]\n"
    "v_mfma_f32_16x16x32_bf16 v[36:39], a[136:139], a[164:167], v[36:39]\n"
    "v_mfma_f32_16x16x32_bf16 v[40:43], a[140:143], a[164:167], v[40:43]\n"
    "buffer_load_dwordx4 v9, s[60:63], s83 offen offset:3072  lds\n"
    "v_mfma_f32_16x16x32_bf16 v[72:75], a[140:143], a[168:171], v[72:75]\n"
    "v_mfma_f32_16x16x32_bf16 v[104:107], a[140:143], a[172:175], v[104:107]\n"
    "v_mfma_f32_16x16x32_bf16 v[136:139], a[140:143], a[176:179], v[136:139]\n"
    "v_mfma_f32_16x16x32_bf16 v[168:171], a[140:143], a[180:183], v[168:171]\n"
    "s_add_u32 m0, s98, 32768\n"
    "v_mfma_f32_16x16x32_bf16 v[200:203], a[140:143], a[184:187], v[200:203]\n"
    "buffer_load_dwordx4 v10, s[64:67], 0 offen offset:0  lds\n"
    "v_mfma_f32_16x16x32_bf16 v[232:235], a[140:143], a[188:191], v[232:235]\n"
    "v_mfma_f32_16x16x32_bf16 a[8:11], a[140:143], a[192:195], a[8:11]\n"
    "v_mfma_f32_16x16x32_bf16 a[40:43], a[140:143], a[196:199], a[40:43]\n"
    "v_mfma_f32_16x16x32_bf16 a[44:47], a[144:147], a[196:199], a[44:47]\n"
    "v_mfma_f32_16x16x32_bf16 a[12:15], a[144:147], a[192:195], a[12:15]\n"
    "buffer_load_dwordx4 v11, s[64:67], s84 offen offset:1024  lds\n"
    "v_mfma_f32_16x16x32_bf16 v[236:239], a[144:147], a[188:191], v[236:239]\n"
    "v_mfma_f32_16x16x32_bf16 v[204:207], a[144:147], a[184:187], v[204:207]\n"
    "v_mfma_f32_16x16x32_bf16 v[172:175], a[144:147], a[180:183], v[172:175]\n"
    "v_mfma_f32_16x16x32_bf16 v[140:143], a[144:147], a[176:179], v[140:143]\n"
    "v_mfma_f32_16x16x32_bf16 v[108:111], a[144:147], a[172:175], v[108:111]\n"
    "s_waitcnt vmcnt(10)\n"
    "s_barrier\n"
    "buffer_load_dwordx4 v10, s[64:67], s85 offen offset:2048  lds\n"
    "v_mfma_f32_16x16x32_bf16 v[76:79], a[144:147], a[168:171], v[76:79]\n"
    "v_mfma_f32_16x16x32_bf16 v[44:47], a[144:147], a[164:167], v[44:47]\n"
    "ds_read_b128 a[64:67], v13 offset:0\n"
    "buffer_load_dword a232, v30, s[12:15], 0 offen\n"
    "v_mfma_f32_16x16x32_bf16 v[48:51], a[148:151], a[164:167], v[48:51]\n"
    "ds_read_b128 a[96:99], v15 offset:0\n"
    "v_add_u32 v30, 4096, v30\n"
    "v_mfma_f32_16x16x32_bf16 v[80:83], a[148:151], a[168:171], v[80:83]\n"
    "ds_read_b128 a[100:103], v15 offset:2048\n"
    "v_mfma_f32_16x16x32_bf16 v[112:115], a[148:151], a[172:175], v[112:115]\n"
    "ds_read_b128 a[104:107], v15 offset:4096\n"
    "buffer_load_dwordx4 v11, s[64:67], s86 offen offset:3072  lds\n"
    "v_mfma_f32_16x16x32_bf16 v[144:147], a[148:151], a[176:179], v[144:147]\n"
    "ds_read_b128 a[108:111], v15 offset:6144\n"
    "v_mfma_f32_16x16x32_bf16 v[176:179], a[148:151], a[180:183], v[176:179]\n"
    "ds_read_b128 a[112:115], v15 offset:8192\n"
    "v_mfma_f32_16x16x32_bf16 v[208:211], a[148:151], a[184:187], v[208:211]\n"
    "ds_read_b128 a[116:119], v15 offset:10240\n"
    "v_mfma_f32_16x16x32_bf16 v[240:243], a[148:151], a[188:191], v[240:243]\n"
    "ds_read_b128 a[120:123], v15 offset:12288\n"
    "s_add_u32 m0, s98, 36864\n"
    "v_mfma_f32_16x16x32_bf16 a[16:19], a[148:151], a[192:195], a[16:19]\n"
    "ds_read_b128 a[124:127], v15 offset:14336\n"
    "buffer_load_dwordx4 v10, s[64:67], s87 offen offset:0  lds\n"
    "v_mfma_f32_16x16x32_bf16 a[48:51], a[148:151], a[196:199], a[48:51]\n"
    "ds_read_b128 a[128:131], v15 offset:16384\n"
    "v_mfma_f32_16x16x32_bf16 a[52:55], a[152:155], a[196:199], a[52:55]\n"
    "ds_read_b128 a[68:71], v13 offset:2048\n"
    "v_mfma_f32_16x16x32_bf16 a[20:23], a[152:155], a[192:195], a[20:23]\n"
    "ds_read_b128 a[72:75], v13 offset:4096\n"
    "v_mfma_f32_16x16x32_bf16 v[244:247], a[152:155], a[188:191], v[244:247]\n"
    "ds_read_b128 a[76:79], v13 offset:6144\n"
    "v_mfma_f32_16x16x32_bf16 v[212:215], a[152:155], a[184:187], v[212:215]\n"
    "ds_read_b128 a[80:83], v13 offset:8192\n"
    "buffer_load_dwordx4 v11, s[64:67], s88 offen offset:1024  lds\n"
    "v_mfma_f32_16x16x32_bf16 v[180:183], a[152:155], a[180:183], v[180:183]\n"
    "ds_read_b128 a[84:87], v13 offset:10240\n"
    "v_mfma_f32_16x16x32_bf16 v[148:151], a[152:155], a[176:179], v[148:151]\n"
    "ds_read_b128 a[88:91], v13 offset:12288\n"
    "v_mfma_f32_16x16x32_bf16 v[116:119], a[152:155], a[172:175], v[116:119]\n"
    "ds_read_b128 a[92:95], v13 offset:14336\n"
    "v_mfma_f32_16x16x32_bf16 v[84:87], a[152:155], a[168:171], v[84:87]\n"
    "v_mfma_f32_16x16x32_bf16 v[52:55], a[152:155], a[164:167], v[52:55]\n"
    "buffer_load_dwordx4 v10, s[64:67], s89 offen offset:2048  lds\n"
    "v_mfma_f32_16x16x32_bf16 v[56:59], a[156:159], a[164:167], v[56:59]\n"
    "v_mfma_f32_16x16x32_bf16 v[88:91], a[156:159], a[168:171], v[88:91]\n"
    "v_mfma_f32_16x16x32_bf16 v[120:123], a[156:159], a[172:175], v[120:123]\n"
    "v_mfma_f32_16x16x32_bf16 v[152:155], a[156:159], a[176:179], v[152:155]\n"
    "v_mfma_f32_16x16x32_bf16 v[184:187], a[156:159], a[180:183], v[184:187]\n"
    "buffer_load_dwordx4 v11, s[64:67], s90 offen offset:3072  lds\n"
    "v_mfma_f32_16x16x32_bf16 v[216:219], a[156:159], a[184:187], v[216:219]\n"
    "v_mfma_f32_16x16x32_bf16 v[248:251], a[156:159], a[188:191], v[248:251]\n"
    "v_mfma_f32_16x16x32_bf16 a[24:27], a[156:159], a[192:195], a[24:27]\n"
    "v_mfma_f32_16x16x32_bf16 a[56:59], a[156:159], a[196:199], a[56:59]\n"
    "s_add_u32 m0, s98, 40960\n"
    "v_mfma_f32_16x16x32_bf16 a[60:63], a[160:163], a[196:199], a[60:63]\n"
    "buffer_load_dwordx4 v10, s[64:67], s91 offen offset:0  lds\n"
    "v_mfma_f32_16x16x32_bf16 a[28:31], a[160:163], a[192:195], a[28:31]\n"
    "s_add_u32 s60, s60, 128\n"
    "s_addc_u32 s61, s61, 0\n"
    "s_add_u32 s64, s64, 128\n"
    "s_addc_u32 s65, s65, 0\n"
    "v_mfma_f32_16x16x32_bf16 v[252:255], a[160:163], a[188:191], v[252:255]\n"
    "s_add_u32 s92, s92, s93\n"
    "s_add_u32 s98, s98, s93\n"
    "v_mfma_f32_16x16x32_bf16 v[220:223], a[160:163], a[184:187], v[220:223]\n"
    "s_sub_u32 s93, 0, s93\n"
    "v_mfma_f32_16x16x32_bf16 v[188:191], a[160:163], a[180:183], v[188:191]\n"
    "v_mfma_f32_16x16x32_bf16 v[156:159], a[160:163], a[176:179], v[156:159]\n"
    "v_mfma_f32_16x16x32_bf16 v[124:127], a[160:163], a[172:175], v[124:127]\n"
    "v_mfma_f32_16x16x32_bf16 v[92:95], a[160:163], a[168:171], v[92:95]\n"
    "s_mov_b32 s59, 1\n"
    "v_mfma_f32_16x16x32_bf16 v[60:63], a[160:163], a[164:167], v[60:63]\n"
    "s_waitcnt lgkmcnt(0)\n"
    ".p2align 6\n"
    "P1Z_loop:\n"
    "v_mfma_f32_16x16x32_bf16 v[32:35], a[64:67], a[96:99], v[32:35]\n"
    "ds_read_b128 a[132:135], v14 offset:0\n"
    "v_mfma_f32_16x16x32_bf16 v[64:67], a[64:67], a[100:103], v[64:67]\n"
    "ds_read_b128 a[164:167], v16 offset:0\n"
    "v_mfma_f32_16x16x32_bf16 v[96:99], a[64:67], a[104:107], v[96:99]\n"
    "ds_read_b128 a[168:171], v16 offset:2048\n"
    "v_mfma_f32_16x16x32_bf16 v[128:131], a[64:67], a[108:111], v[128:131]\n"
    "ds_read_b128 a[172:175], v16 offset:4096\n"
    "v_mfma_f32_16x16x32_bf16 v[160:163], a[64:67], a[112:115], v[160:163]\n"
    "ds_read_b128 a[176:179], v16 offset:6144\n"
    "v_mfma_f32_16x16x32_bf16 v[192:195], a[64:67], a[116:119], v[192:195]\n"
    "ds_read_b128 a[180:183], v16 offset:8192\n"
    "v_mfma_f32_16x16x32_bf16 v[224:227], a[64:67], a[120:123], v[224:227]\n"
    "ds_read_b128 a[184:187], v16 offset:10240\n"
    "v_mfma_f32_16x16x32_bf16 a[0:3], a[64:67], a[124:127], a[0:3]\n"
    "ds_read_b128 a[188:191], v16 offset:12288\n"
    "v_mfma_f32_16x16x32_bf16 a[32:35], a[64:67], a[128:131], a[32:35]\n"
    "ds_read_b128 a[192:195], v16 offset:14336\n"
    "v_mfma_f32_16x16x32_bf16 a[36:39], a[68:71], a[128:131], a[36:39]\n"
    "ds_read_b128 a[196:199], v16 offset:16384\n"
    "v_mfma_f32_16x16x32_bf16 a[4:7], a[68:71], a[124:127], a[4:7]\n"
    "ds_read_b128 a[136:139], v14 offset:2048\n"
    "v_mfma_f32_16x16x32_bf16 v[228:231], a[68:71], a[120:123], v[228:231]\n"
    "ds_read_b128 a[140:143], v14 offset:4096\n"
    "v_mfma_f32_16x16x32_bf16 v[196:199], a[68:71], a[116:119], v[196:199]\n"
    "ds_read_b128 a[144:147], v14 offset:6144\n"
    "v_mfma_f32_16x16x32_bf16 v[164:167], a[68:71], a[112:115], v[164:167]\n"
    "ds_read_b128 a[148:151], v14 offset:8192\n"
    "v_mfma_f32_16x16x32_bf16 v[132:135], a[68:71], a[108:111], v[132:135]\n"
    "ds_read_b128 a[152:155], v14 offset:10240\n"
    "v_mfma_f32_16x16x32_bf16 v[100:103], a[68:71], a[104:107], v[100:103]\n"
    "ds_read_b128 a[156:159], v14 offset:12288\n"
    "v_mfma_f32_16x16x32_bf16 v[68:71], a[68:71], a[100:103], v[68:71]\n"
    "ds_read_b128 a[160:163], v14 offset:14336\n"
    "v_mfma_f32_16x16x32_bf16 v[36:39], a[68:71], a[96:99], v[36:39]\n"
    "v_mfma_f32_16x16x32_bf16 v[40:43], a[72:75], a[96:99], v[40:43]\n"
    "v_mfma_f32_16x16x32_bf16 v[72:75], a[72:75], a[100:103], v[72:75]\n"
    "v_mfma_f32_16x16x32_bf16 v[104:107], a[72:75], a[104:107], v[104:107]\n"
    "v_mfma_f32_16x16x32_bf16 v[136:139], a[72:75], a[108:111], v[136:139]\n"
    "v_mfma_f32_16x16x32_bf16 v[168:171], a[72:75], a[112:115], v[168:171]\n"
    "v_mfma_f32_16x16x32_bf16 v[200:203], a[72:75], a[116:119], v[200:203]\n"
    "v_mfma_f32_16x16x32_bf16 v[232:235], a[72:75], a[120:123], v[232:235]\n"
    "v_mfma_f32_16x16x32_bf16 a[8:11], a[72:75], a[124:127], a[8:11]\n"
    "v_mfma_f32_16x16x32_bf16 a[40:43], a[72:75], a[128:131], a[40:43]\n"
    "v_mfma_f32_16x16x32_bf16 a[44:47], a[76:79], a[128:131], a[44:47]\n"
    "v_mfma_f32_16x16x32_bf16 a[12:15], a[76:79], a[124:127], a[12:15]\n"
    "v_mfma_f32_16x16x32_bf16 v[236:239], a[76:79], a[120:123], v[236:239]\n"
    "v_mfma_f32_16x16x32_bf16 v[204:207], a[76:79], a[116:119], v[204:207]\n"
    "v_mfma_f32_16x16x32_bf16 v[172:175], a[76:79], a[112:115], v[172:175]\n"
    "v_mfma_f32_16x16x32_bf16 v[140:143], a[76:79], a[108:111], v[140:143]\n"
    "v_mfma_f32_16x16x32_bf16 v[108:111], a[76:79], a[104:107], v[108:111]\n"
    "s_waitcnt lgkmcnt(0)\n"
    "s_barrier\n"
    "v_mfma_f32_16x16x32_bf16 v[76:79], a[76:79], a[100:103], v[76:79]\n"
    "s_add_u32 m0, s92, 0\n"
    "v_mfma_f32_16x16x32_bf16 v[44:47], a[76:79], a[96:99], v[44:47]\n"
    "buffer_load_dwordx4 v8, s[60:63], s76 offen offset:0  lds\n"
    "v_mfma_f32_16x16x32_bf16 v[48:51], a[80:83], a[96:99], v[48:51]\n"
    "v_add_u32 v13, s93, v13\n"
    "v_mfma_f32_16x16x32_bf16 v[80:83], a[80:83], a[100:103], v[80:83]\n"
    "v_add_u32 v14, s93, v14\n"
    "v_mfma_f32_16x16x32_bf16 v[112:115], a[80:83], a[104:107], v[112:115]\n"
    "v_mfma_f32_16x16x32_bf16 v[144:147], a[80:83], a[108:111], v[144:147]\n"
    "buffer_load_dwordx4 v9, s[60:63], s77 offen offset:1024  lds\n"
    "v_mfma_f32_16x16x32_bf16 v[176:179], a[80:83], a[112:115], v[176:179]\n"
    "v_add_u32 v15, s93, v15\n"
    "v_mfma_f32_16x16x32_bf16 v[208:211], a[80:83], a[116:119], v[208:211]\n"
    "v_add_u32 v16, s93, v16\n"
    "v_mfma_f32_16x16x32_bf16 v[240:243], a[80:83], a[120:123], v[240:243]\n"
    "v_mfma_f32_16x16x32_bf16 a[16:19], a[80:83], a[124:127], a[16:19]\n"
    "buffer_load_dwordx4 v8, s[60:63], s78 offen offset:2048  lds\n"
    "v_mfma_f32_16x16x32_bf16 a[48:51], a[80:83], a[128:131], a[48:51]\n"
    "v_mfma_f32_16x16x32_bf16 a[52:55], a[84:87], a[128:131], a[52:55]\n"
    "v_mfma_f32_16x16x32_bf16 a[20:23], a[84:87], a[124:127], a[20:23]\n"
    "v_mfma_f32_16x16x32_bf16 v[244:247], a[84:87], a[120:123], v[244:247]\n"
    "buffer_load_dwordx4 v9, s[60:63], s79 offen offset:3072  lds\n"
    "v_mfma_f32_16x16x32_bf16 v[212:215], a[84:87], a[116:119], v[212:215]\n"
    "v_mfma_f32_16x16x32_bf16 v[180:183], a[84:87], a[112:115], v[180:183]\n"
    "v_mfma_f32_16x16x32_bf16 v[148:151], a[84:87], a[108:111], v[148:151]\n"
    "s_add_u32 m0, s92, 4096\n"
    "v_mfma_f32_16x16x32_bf16 v[116:119], a[84:87], a[104:107], v[116:119]\n"
    "buffer_load_dwordx4 v8, s[60:63], s80 offen offset:0  lds\n"
    "v_mfma_f32_16x16x32_bf16 v[84:87], a[84:87], a[100:103], v[84:87]\n"
    "v_mfma_f32_16x16x32_bf16 v[52:55], a[84:87], a[96:99], v[52:55]\n"
    "v_mfma_f32_16x16x32_bf16 v[56:59], a[88:91], a[96:99], v[56:59]\n"
    "v_mfma_f32_16x16x32_bf16 v[88:91], a[88:91], a[100:103], v[88:91]\n"
    "buffer_load_dwordx4 v9, s[60:63], s81 offen offset:1024  lds\n"
    "v_mfma_f32_16x16x32_bf16 v[120:123], a[88:91], a[104:107], v[120:123]\n"
    "v_mfma_f32_16x16x32_bf16 v[152:155], a[88:91], a[108:111], v[152:155]\n"
    "v_mfma_f32_16x16x32_bf16 v[184:187], a[88:91], a[112:115], v[184:187]\n"
    "v_mfma_f32_16x16x32_bf16 v[216:219], a[88:91], a[116:119], v[216:219]\n"
    "buffer_load_dwordx4 v8, s[60:63], s82 offen offset:2048  lds\n"
    "v_mfma_f32_16x16x32_bf16 v[248:251], a[88:91], a[120:123], v[248:251]\n"
    "v_mfma_f32_16x16x32_bf16 a[24:27], a[88:91], a[124:127], a[24:27]\n"
    "v_mfma_f32_16x16x32_bf16 a[56:59], a[88:91], a[128:131], a[56:59]\n"
    "v_mfma_f32_16x16x32_bf16 a[60:63], a[92:95], a[128:131], a[60:63]\n"
    "buffer_load_dwordx4 v9, s[60:63], s83 offen offset:3072  lds\n"
    "v_mfma_f32_16x16x32_bf16 a[28:31], a[92:95], a[124:127], a[28:31]\n"
    "v_mfma_f32_16x16x32_bf16 v[252:255], a[92:95], a[120:123], v[252:255]\n"
    "v_mfma_f32_16x16x32_bf16 v[220:223], a[92:95], a[116:119], v[220:223]\n"
    "s_add_u32 m0, s98, 32768\n"
    "v_mfma_f32_16x16x32_bf16 v[188:191], a[92:95], a[112:115], v[188:191]\n"
    "buffer_load_dwordx4 v10, s[64:67], 0 offen offset:0  lds\n"
    "v_mfma_f32_16x16x32_bf16 v[156:159], a[92:95], a[108:111], v[156:159]\n"
    "v_mfma_f32_16x16x32_bf16 v[124:127], a[92:95], a[104:107], v[124:127]\n"
    "v_mfma_f32_16x16x32_bf16 v[92:95], a[92:95], a[100:103], v[92:95]\n"
    "v_mfma_f32_16x16x32_bf16 v[60:63], a[92:95], a[96:99], v[60:63]\n"
    "buffer_load_dwordx4 v11, s[64:67], s84 offen offset:1024  lds\n"
    "v_mfma_f32_16x16x32_bf16 v[32:35], a[132:135], a[164:167], v[32:35]\n"
    "v_mfma_f32_16x16x32_bf16 v[64:67], a[132:135], a[168:171], v[64:67]\n"
    "v_mfma_f32_16x16x32_bf16 v[96:99], a[132:135], a[172:175], v[96:99]\n"
    "v_mfma_f32_16x16x32_bf16 v[128:131], a[132:135], a[176:179], v[128:131]\n"
    "buffer_load_dwordx4 v10, s[64:67], s85 offen offset:2048  lds\n"
    "v_mfma_f32_16x16x32_bf16 v[160:163], a[132:135], a[180:183], v[160:163]\n"
    "v_mfma_f32_16x16x32_bf16 v[192:195], a[132:135], a[184:187], v[192:195]\n"
    "v_mfma_f32_16x16x32_bf16 v[224:227], a[132:135], a[188:191], v[224:227]\n"
    "v_mfma_f32_16x16x32_bf16 a[0:3], a[132:135], a[192:195], a[0:3]\n"
    "buffer_load_dwordx4 v11, s[64:67], s86 offen offset:3072  lds\n"
    "v_mfma_f32_16x16x32_bf16 a[32:35], a[132:135], a[196:199], a[32:35]\n"
    "v_mfma_f32_16x16x32_bf16 a[36:39], a[136:139], a[196:199], a[36:39]\n"
    "v_mfma_f32_16x16x32_bf16 a[4:7], a[136:139], a[192:195], a[4:7]\n"
    "s_add_u32 m0, s98, 36864\n"
    "v_mfma_f32_16x16x32_bf16 v[228:231], a[136:139], a[188:191], v[228:231]\n"
    "buffer_load_dwordx4 v10, s[64:67], s87 offen offset:0  lds\n"
    "v_mfma_f32_16x16x32_bf16 v[196:199], a[136:139], a[184:187], v[196:199]\n"
    "v_mfma_f32_16x16x32_bf16 v[164:167], a[136:139], a[180:183], v[164:167]\n"
    "v_mfma_f32_16x16x32_bf16 v[132:135], a[136:139], a[176:179], v[132:135]\n"
    "v_mfma_f32_16x16x32_bf16 v[100:103], a[136:139], a[172:175], v[100:103]\n"
    "buffer_load_dwordx4 v11, s[64:67], s88 offen offset:1024  lds\n"
    "v_mfma_f32_16x16x32_bf16 v[68:71], a[136:139], a[168:171], v[68:71]\n"
    "v_mfma_f32_16x16x32_bf16 v[36:39], a[136:139], a[164:167], v[36:39]\n"
    "v_mfma_f32_16x16x32_bf16 v[40:43], a[140:143], a[164:167], v[40:43]\n"
    "v_mfma_f32_16x16x32_bf16 v[72:75], a[140:143], a[168:171], v[72:75]\n"
    "buffer_load_dwordx4 v10, s[64:67], s89 offen offset:2048  lds\n"
    "v_mfma_f32_16x16x32_bf16 v[104:107], a[140:143], a[172:175], v[104:107]\n"
    "v_mfma_f32_16x16x32_bf16 v[136:139], a[140:143], a[176:179], v[136:139]\n"
    "v_mfma_f32_16x16x32_bf16 v[168:171], a[140:143], a[180:183], v[168:171]\n"
    "v_mfma_f32_16x16x32_bf16 v[200:203], a[140:143], a[184:187], v[200:203]\n"
    "buffer_load_dwordx4 v11, s[64:67], s90 offen offset:3072  lds\n"
    "v_mfma_f32_16x16x32_bf16 v[232:235], a[140:143], a[188:191], v[232:235]\n"
    "v_mfma_f32_16x16x32_bf16 a[8:11], a[140:143], a[192:195], a[8:11]\n"
    "v_mfma_f32_16x16x32_bf16 a[40:43], a[140:143], a[196:199], a[40:43]\n"
    "s_add_u32 m0, s98, 40960\n"
    "v_mfma_f32_16x16x32_bf16 a[44:47], a[144:147], a[196:199], a[44:47]\n"
    "buffer_load_dwordx4 v10, s[64:67], s91 offen offset:0  lds\n"
    "v_mfma_f32_16x16x32_bf16 a[12:15], a[144:147], a[192:195], a[12:15]\n"
    "v_mfma_f32_16x16x32_bf16 v[236:239], a[144:147], a[188:191], v[236:239]\n"
    "v_mfma_f32_16x16x32_bf16 v[204:207], a[144:147], a[184:187], v[204:207]\n"
    "v_mfma_f32_16x16x32_bf16 v[172:175], a[144:147], a[180:183], v[172:175]\n"
    "v_mfma_f32_16x16x32_bf16 v[140:143], a[144:147], a[176:179], v[140:143]\n"
    "v_mfma_f32_16x16x32_bf16 v[108:111], a[144:147], a[172:175], v[108:111]\n"
    "s_waitcnt vmcnt(17)\n"
    "s_barrier\n"
    "v_mfma_f32_16x16x32_bf16 v[76:79], a[144:147], a[168:171], v[76:79]\n"
    "buffer_load_dword a232, v30, s[12:15], 0 offen\n"
    "v_mfma_f32_16x16x32_bf16 v[44:47], a[144:147], a[164:167], v[44:47]\n"
    "ds_read_b128 a[64:67], v13 offset:0\n"
    "v_add_u32 v30, 4096, v30\n"
    "v_mfma_f32_16x16x32_bf16 v[48:51], a[148:151], a[164:167], v[48:51]\n"
    "ds_read_b128 a[96:99], v15 offset:0\n"
    "v_mfma_f32_16x16x32_bf16 v[80:83], a[148:151], a[168:171], v[80:83]\n"
    "ds_read_b128 a[100:103], v15 offset:2048\n"
    "v_mfma_f32_16x16x32_bf16 v[112:115], a[148:151], a[172:175], v[112:115]\n"
    "ds_read_b128 a[104:107], v15 offset:4096\n"
    "v_mfma_f32_16x16x32_bf16 v[144:147], a[148:151], a[176:179], v[144:147]\n"
    "ds_read_b128 a[108:111], v15 offset:6144\n"
    "v_mfma_f32_16x16x32_bf16 v[176:179], a[148:151], a[180:183], v[176:179]\n"
    "ds_read_b128 a[112:115], v15 offset:8192\n"
    "v_mfma_f32_16x16x32_bf16 v[208:211], a[148:151], a[184:187], v[208:211]\n"
    "ds_read_b128 a[116:119], v15 offset:10240\n"
    "v_mfma_f32_16x16x32_bf16 v[240:243], a[148:151], a[188:191], v[240:243]\n"
    "ds_read_b128 a[120:123], v15 offset:12288\n"
    "v_mfma_f32_16x16x32_bf16 a[16:19], a[148:151], a[192:195], a[16:19]\n"
    "ds_read_b128 a[124:127], v15 offset:14336\n"
    "v_mfma_f32_16x16x32_bf16 a[48:51], a[148:151], a[196:199], a[48:51]\n"
    "ds_read_b128 a[128:131], v15 offset:16384\n"
    "v_mfma_f32_16x16x32_bf16 a[52:55], a[152:155], a[196:199], a[52:55]\n"
    "ds_read_b128 a[68:71], v13 offset:2048\n"
    "v_mfma_f32_16x16x32_bf16 a[20:23], a[152:155], a[192:195], a[20:23]\n"
    "ds_read_b128 a[72:75], v13 offset:4096\n"
    "v_mfma_f32_16x16x32_bf16 v[244:247], a[152:155], a[188:191], v[244:247]\n"
    "ds_read_b128 a[76:79], v13 offset:6144\n"
    "v_mfma_f32_16x16x32_bf16 v[212:215], a[152:155], a[184:187], v[212:215]\n"
    "ds_read_b128 a[80:83], v13 offset:8192\n"
    "v_mfma_f32_16x16x32_bf16 v[180:183], a[152:155], a[180:183], v[180:183]\n"
    "ds_read_b128 a[84:87], v13 offset:10240\n"
    "v_mfma_f32_16x16x32_bf16 v[148:151], a[152:155], a[176:179], v[148:151]\n"
    "ds_read_b128 a[88:91], v13 offset:12288\n"
    "v_mfma_f32_16x16x32_bf16 v[116:119], a[152:155], a[172:175], v[116:119]\n"
    "ds_read_b128 a[92:95], v13 offset:14336\n"
    "v_mfma_f32_16x16x32_bf16 v[84:87], a[152:155], a[168:171], v[84:87]\n"
    "v_mfma_f32_16x16x32_bf16 v[52:55], a[152:155], a[164:167], v[52:55]\n"
    "v_mfma_f32_16x16x32_bf16 v[56:59], a[156:159], a[164:167], v[56:59]\n"
    "v_mfma_f32_16x16x32_bf16 v[88:91], a[156:159], a[168:171], v[88:91]\n"
    "v_mfma_f32_16x16x32_bf16 v[120:123], a[156:159], a[172:175], v[120:123]\n"
    "v_mfma_f32_16x16x32_bf16 v[152:155], a[156:159], a[176:179], v[152:155]\n"
    "v_mfma_f32_16x16x32_bf16 v[184:187], a[156:159], a[180:183], v[184:187]\n"
    "v_mfma_f32_16x16x32_bf16 v[216:219], a[156:159], a[184:187], v[216:219]\n"
    "v_mfma_f32_16x16x32_bf16 v[248:251], a[156:159], a[188:191], v[248:251]\n"
    "v_mfma_f32_16x16x32_bf16 a[24:27], a[156:159], a[192:195], a[24:27]\n"
    "v_mfma_f32_16x16x32_bf16 a[56:59], a[156:159], a[196:199], a[56:59]\n"
    "v_mfma_f32_16x16x32_bf16 a[60:63], a[160:163], a[196:199], a[60:63]\n"
    "s_add_u32 s60, s60, 128\n"
    "s_addc_u32 s61, s61, 0\n"
    "v_mfma_f32_16x16x32_bf16 a[28:31], a[160:163], a[192:195], a[28:31]\n"
    "s_add_u32 s64, s64, 128\n"
    "s_addc_u32 s65, s65, 0\n"
    "v_mfma_f32_16x16x32_bf16 v[252:255], a[160:163], a[188:191], v[252:255]\n"
    "s_add_u32 s92, s92, s93\n"
    "s_add_u32 s98, s98, s93\n"
    "v_mfma_f32_16x16x32_bf16 v[220:223], a[160:163], a[184:187], v[220:223]\n"
    "s_sub_u32 s93, 0, s93\n"
    "v_mfma_f32_16x16x32_bf16 v[188:191], a[160:163], a[180:183], v[188:191]\n"
    "s_add_u32 s59, s59, 1\n"
    "s_cmp_eq_u32 s59, 7\n"
    "v_mfma_f32_16x16x32_bf16 v[156:159], a[160:163], a[176:179], v[156:159]\n"
    "TC4:\n"
    "s_cbranch_execz TC8\n"
    "s_cselect_b64 s[12:13], s[4:5], s[12:13]\n"
    "s_cmp_lt_u32 s59, 14\n"
    "v_mfma_f32_16x16x32_bf16 v[124:127], a[160:163], a[172:175], v[124:127]\n"
    "s_cselect_b32 s62, s62, 0\n"
    "s_cselect_b32 s66, s66, 0\n"
    "v_mfma_f32_16x16x32_bf16 v[92:95], a[160:163], a[168:171], v[92:95]\n"
    "s_cmp_lt_u32 s59, 15\n"
    "v_mfma_f32_16x16x32_bf16 v[60:63], a[160:163], a[164:167], v[60:63]\n"
    "s_waitcnt lgkmcnt(0)\n"
    "s_cbranch_scc1 P1Z_loop\n"
    "ds_read_b128 a[136:139], v14 offset:2048\n"
    "ds_read_b128 a[140:143], v14 offset:4096\n"
    "v_mfma_f32_16x16x32_bf16 v[32:35], a[64:67], a[96:99], v[32:35]\n"
    "ds_read_b128 a[144:147], v14 offset:6144\n"
    "s_lshr_b32 s94, s56, 1\n"
    "s_lshl_b32 s94, s94, 6\n"
    "v_mfma_f32_16x16x32_bf16 v[36:39], a[68:71], a[96:99], v[36:39]\n"
    "ds_read_b128 a[132:135], v14 offset:0\n"
    "s_add_u32 s94, s94, s57\n"
    "s_lshl_b32 s94, s94, 1\n"
    "v_mfma_f32_16x16x32_bf16 v[40:43], a[72:75], a[96:99], v[40:43]\n"
    "ds_read_b128 a[164:167], v16 offset:0\n"
    "s_lshr_b32 s95, s55, 1\n"
    "s_add_u32 s94, s94, s95\n"
    "v_mfma_f32_16x16x32_bf16 v[44:47], a[76:79], a[96:99], v[44:47]\n"
    "ds_read_b128 a[168:171], v16 offset:2048\n"
    "s_lshl_b32 s94, s94, 15\n"
    "s_add_u32 s94, s94, 16777216\n"
    "v_mfma_f32_16x16x32_bf16 v[64:67], a[64:67], a[100:103], v[64:67]\n"
    "ds_read_b128 a[172:175], v16 offset:4096\n"
    "s_add_u32 s12, s52, s94\n"
    "s_addc_u32 s13, s53, 0\n"
    "v_mfma_f32_16x16x32_bf16 v[68:71], a[68:71], a[100:103], v[68:71]\n"
    "ds_read_b128 a[176:179], v16 offset:6144\n"
    "s_and_b32 s13, s13, 0xffff\n"
    "s_mov_b32 s14, 4194304\n"
    "v_mfma_f32_16x16x32_bf16 v[72:75], a[72:75], a[100:103], v[72:75]\n"
    "ds_read_b128 a[180:183], v16 offset:8192\n"
    "s_mov_b32 s15, 0x20000\n"
    "s_add_u32 s94, s96, 0\n"
    "v_mfma_f32_16x16x32_bf16 v[76:79], a[76:79], a[100:103], v[76:79]\n"
    "ds_read_b128 a[184:187], v16 offset:10240\n"
    "s_and_b32 s95, s94, 2\n"
    "s_lshl_b32 s95, s95, 13\n"
    "v_mfma_f32_16x16x32_bf16 v[96:99], a[64:67], a[104:107], v[96:99]\n"
    "ds_read_b128 a[188:191], v16 offset:12288\n"
    "ds_read_b128 a[192:195], v16 offset:14336\n"
    "s_and_b32 s94, s94, 1\n"
    "s_lshl_b32 s94, s94, 10\n"
    "v_mfma_f32_16x16x32_bf16 v[100:103], a[68:71], a[104:107], v[100:103]\n"
    "ds_read_b128 a[196:199], v16 offset:16384\n"
    "s_add_u32 s84, s94, s95\n"
    "s_add_u32 s94, s96, 1\n"
    "v_mfma_f32_16x16x32_bf16 v[104:107], a[72:75], a[104:107], v[104:107]\n"
    "ds_read_b128 a[148:151], v14 offset:8192\n"
    "s_and_b32 s95, s94, 2\n"
    "s_lshl_b32 s95, s95, 13\n"
    "v_mfma_f32_16x16x32_bf16 v[108:111], a[76:79], a[104:107], v[108:111]\n"
    "ds_read_b128 a[152:155], v14 offset:10240\n"
    "s_and_b32 s94, s94, 1\n"
    "s_lshl_b32 s94, s94, 10\n"
    "v_mfma_f32_16x16x32_bf16 v[128:131], a[64:67], a[108:111], v[128:131]\n"
    "ds_read_b128 a[156:159], v14 offset:12288\n"
    "s_add_u32 s85, s94, s95\n"
    "s_add_u32 s94, s96, 2\n"
    "v_mfma_f32_16x16x32_bf16 v[132:135], a[68:71], a[108:111], v[132:135]\n"
    "ds_read_b128 a[160:163], v14 offset:14336\n"
    "s_and_b32 s95, s94, 2\n"
    "s_lshl_b32 s95, s95, 13\n"
    "v_mfma_f32_16x16x32_bf16 v[136:139], a[72:75], a[108:111], v[136:139]\n"
    "s_and_b32 s94, s94, 1\n"
    "s_lshl_b32 s94, s94, 10\n"
    "s_add_u32 s86, s94, s95\n"
    "v_mfma_f32_16x16x32_bf16 v[140:143], a[76:79], a[108:111], v[140:143]\n"
    "s_waitcnt lgkmcnt(12)\n"
    "s_add_u32 s94, s96, 3\n"
    "s_and_b32 s95, s94, 2\n"
    "v_mfma_f32_16x16x32_bf16 v[32:35], a[132:135], a[164:167], v[32:35]\n"
    "s_lshl_b32 s95, s95, 13\n"
    "s_and_b32 s94, s94, 1\n"
    "s_lshl_b32 s94, s94, 10\n"
    "v_mfma_f32_16x16x32_bf16 v[36:39], a[136:139], a[164:167], v[36:39]\n"
    "s_add_u32 s87, s94, s95\n"
    "s_and_b32 s11, s56, 1\n"
    "s_lshl_b32 s11, s11, 1\n"
    "v_mfma_f32_16x16x32_bf16 v[40:43], a[140:143], a[164:167], v[40:43]\n"
    "s_and_b32 s95, s55, 1\n"
    "s_add_u32 s11, s11, s95\n"
    "s_mul_i32 s11, s11, 9\n"
    "v_mfma_f32_16x16x32_bf16 v[44:47], a[144:147], a[164:167], v[44:47]\n"
    "s_add_u32 s11, s11, 28\n"
    "s_lshr_b32 s94, s11, 3\n"
    "s_lshl_b32 s94, s94, 19\n"
    "v_mfma_f32_16x16x32_bf16 v[160:163], a[64:67], a[112:115], v[160:163]\n"
    "s_and_b32 s95, s11, 7\n"
    "s_lshl_b32 s95, s95, 11\n"
    "s_add_u32 s94, s94, s95\n"
    "v_mfma_f32_16x16x32_bf16 v[164:167], a[68:71], a[112:115], v[164:167]\n"
    "v_add_u32 v7, s94, v21\n"
    "v_mfma_f32_16x16x32_bf16 v[168:171], a[72:75], a[112:115], v[168:171]\n"
    "v_mfma_f32_16x16x32_bf16 v[172:175], a[76:79], a[112:115], v[172:175]\n"
    "s_waitcnt lgkmcnt(0) vmcnt(1)\n"
    "s_barrier\n"
    "v_mfma_f32_16x16x32_bf16 v[64:67], a[132:135], a[168:171], v[64:67]\n"
    "v_mfma_f32_16x16x32_bf16 v[68:71], a[136:139], a[168:171], v[68:71]\n"
    "v_cvt_pk_bf16_f32 v32, v32, v33\n"
    "v_cvt_pk_bf16_f32 v33, v34, v35\n"
    "v_mfma_f32_16x16x32_bf16 v[72:75], a[140:143], a[168:171], v[72:75]\n"
    "v_cvt_pk_bf16_f32 v34, v36, v37\n"
    "v_cvt_pk_bf16_f32 v35, v38, v39\n"
    "v_mfma_f32_16x16x32_bf16 v[76:79], a[144:147], a[168:171], v[76:79]\n"
    "buffer_store_dwordx4 v[32:35], v7, s[12:15], s84 offen sc1\n"
    "v_mfma_f32_16x16x32_bf16 v[192:195], a[64:67], a[116:119], v[192:195]\n"
    "v_cvt_pk_bf16_f32 v40, v40, v41\n"
    "v_cvt_pk_bf16_f32 v41, v42, v43\n"
    "v_mfma_f32_16x16x32_bf16 v[196:199], a[68:71], a[116:119], v[196:199]\n"
    "v_cvt_pk_bf16_f32 v42, v44, v45\n"
    "v_cvt_pk_bf16_f32 v43, v46, v47\n"
    "v_mfma_f32_16x16x32_bf16 v[200:203], a[72:75], a[116:119], v[200:203]\n"
    "buffer_store_dwordx4 v[40:43], v7, s[12:15], s85 offen sc1\n"
    "v_mfma_f32_16x16x32_bf16 v[204:207], a[76:79], a[116:119], v[204:207]\n"
    "s_add_u32 s11, s11, 1\n"
    "s_lshr_b32 s94, s11, 3\n"
    "s_lshl_b32 s94, s94, 19\n"
    "v_mfma_f32_16x16x32_bf16 v[96:99], a[132:135], a[172:175], v[96:99]\n"
    "s_and_b32 s95, s11, 7\n"
    "s_lshl_b32 s95, s95, 11\n"
    "s_add_u32 s94, s94, s95\n"
    "v_mfma_f32_16x16x32_bf16 v[100:103], a[136:139], a[172:175], v[100:103]\n"
    "v_add_u32 v7, s94, v21\n"
    "v_cvt_pk_bf16_f32 v64, v64, v65\n"
    "v_mfma_f32_16x16x32_bf16 v[104:107], a[140:143], a[172:175], v[104:107]\n"
    "v_cvt_pk_bf16_f32 v65, v66, v67\n"
    "v_cvt_pk_bf16_f32 v66, v68, v69\n"
    "v_mfma_f32_16x16x32_bf16 v[108:111], a[144:147], a[172:175], v[108:111]\n"
    "v_cvt_pk_bf16_f32 v67, v70, v71\n"
    "v_mfma_f32_16x16x32_bf16 v[224:227], a[64:67], a[120:123], v[224:227]\n"
    "buffer_store_dwordx4 v[64:67], v7, s[12:15], s84 offen sc1\n"
    "v_mfma_f32_16x16x32_bf16 v[228:231], a[68:71], a[120:123], v[228:231]\n"
    "v_cvt_pk_bf16_f32 v72, v72, v73\n"
    "v_cvt_pk_bf16_f32 v73, v74, v75\n"
    "v_mfma_f32_16x16x32_bf16 v[232:235], a[72:75], a[120:123], v[232:235]\n"
    "v_cvt_pk_bf16_f32 v74, v76, v77\n"
    "v_cvt_pk_bf16_f32 v75, v78, v79\n"
    "v_mfma_f32_16x16x32_bf16 v[236:239], a[76:79], a[120:123], v[236:239]\n"
    "buffer_store_dwordx4 v[72:75], v7, s[12:15], s85 offen sc1\n"
    "v_mfma_f32_16x16x32_bf16 v[128:131], a[132:135], a[176:179], v[128:131]\n"
    "s_add_u32 s11, s11, 1\n"
    "s_lshr_b32 s94, s11, 3\n"
    "s_lshl_b32 s94, s94, 19\n"
    "v_mfma_f32_16x16x32_bf16 v[132:135], a[136:139], a[176:179], v[132:135]\n"
    "s_and_b32 s95, s11, 7\n"
    "s_lshl_b32 s95, s95, 11\n"
    "s_add_u32 s94, s94, s95\n"
    "v_mfma_f32_16x16x32_bf16 v[136:139], a[140:143], a[176:179], v[136:139]\n"
    "v_add_u32 v7, s94, v21\n"
    "v_cvt_pk_bf16_f32 v96, v96, v97\n"
    "v_mfma_f32_16x16x32_bf16 v[140:143], a[144:147], a[176:179], v[140:143]\n"
    "v_cvt_pk_bf16_f32 v97, v98, v99\n"
    "v_cvt_pk_bf16_f32 v98, v100, v101\n"
    "v_mfma_f32_16x16x32_bf16 a[0:3], a[64:67], a[124:127], a[0:3]\n"
    "v_cvt_pk_bf16_f32 v99, v102, v103\n"
    "v_mfma_f32_16x16x32_bf16 a[4:7], a[68:71], a[124:127], a[4:7]\n"
    "buffer_store_dwordx4 v[96:99], v7, s[12:15], s84 offen sc1\n"
    "v_mfma_f32_16x16x32_bf16 a[8:11], a[72:75], a[124:127], a[8:11]\n"
    "v_cvt_pk_bf16_f32 v104, v104, v105\n"
    "v_cvt_pk_bf16_f32 v105, v106, v107\n"
    "v_mfma_f32_16x16x32_bf16 a[12:15], a[76:79], a[124:127], a[12:15]\n"
    "v_cvt_pk_bf16_f32 v106, v108, v109\n"
    "v_cvt_pk_bf16_f32 v107, v110, v111\n"
    "v_mfma_f32_16x16x32_bf16 v[160:163], a[132:135], a[180:183], v[160:163]\n"
    "buffer_store_dwordx4 v[104:107], v7, s[12:15], s85 offen sc1\n"
    "v_mfma_f32_16x16x32_bf16 v[164:167], a[136:139], a[180:183], v[164:167]\n"
    "s_add_u32 s11, s11, 1\n"
    "s_lshr_b32 s94, s11, 3\n"
    "s_lshl_b32 s94, s94, 19\n"
    "v_mfma_f32_16x16x32_bf16 v[168:171], a[140:143], a[180:183], v[168:171]\n"
    "s_and_b32 s95, s11, 7\n"
    "s_lshl_b32 s95, s95, 11\n"
    "s_add_u32 s94, s94, s95\n"
    "v_mfma_f32_16x16x32_bf16 v[172:175], a[144:147], a[180:183], v[172:175]\n"
    "v_add_u32 v7, s94, v21\n"
    "v_cvt_pk_bf16_f32 v128, v128, v129\n"
    "v_mfma_f32_16x16x32_bf16 a[32:35], a[64:67], a[128:131], a[32:35]\n"
    "v_cvt_pk_bf16_f32 v129, v130, v131\n"
    "v_cvt_pk_bf16_f32 v130, v132, v133\n"
    "v_mfma_f32_16x16x32_bf16 a[36:39], a[68:71], a[128:131], a[36:39]\n"
    "v_cvt_pk_bf16_f32 v131, v134, v135\n"
    "v_mfma_f32_16x16x32_bf16 a[40:43], a[72:75], a[128:131], a[40:43]\n"
    "buffer_store_dwordx4 v[128:131], v7, s[12:15], s84 offen sc1\n"
    "v_mfma_f32_16x16x32_bf16 a[44:47], a[76:79], a[128:131], a[44:47]\n"
    "v_cvt_pk_bf16_f32 v136, v136, v137\n"
    "v_cvt_pk_bf16_f32 v137, v138, v139\n"
    "v_mfma_f32_16x16x32_bf16 v[192:195], a[132:135], a[184:187], v[192:195]\n"
    "v_cvt_pk_bf16_f32 v138, v140, v141\n"
    "v_cvt_pk_bf16_f32 v139, v142, v143\n"
    "v_mfma_f32_16x16x32_bf16 v[196:199], a[136:139], a[184:187], v[196:199]\n"
    "buffer_store_dwordx4 v[136:139], v7, s[12:15], s85 offen sc1\n"
    "v_mfma_f32_16x16x32_bf16 v[200:203], a[140:143], a[184:187], v[200:203]\n"
    "s_add_u32 s11, s11, 1\n"
    "s_lshr_b32 s94, s11, 3\n"
    "s_lshl_b32 s94, s94, 19\n"
    "v_mfma_f32_16x16x32_bf16 v[204:207], a[144:147], a[184:187], v[204:207]\n"
    "s_and_b32 s95, s11, 7\n"
    "s_lshl_b32 s95, s95, 11\n"
    "s_add_u32 s94, s94, s95\n"
    "v_mfma_f32_16x16x32_bf16 v[48:51], a[80:83], a[96:99], v[48:51]\n"
    "v_add_u32 v7, s94, v21\n"
    "v_cvt_pk_bf16_f32 v160, v160, v161\n"
    "v_mfma_f32_16x16x32_bf16 v[52:55], a[84:87], a[96:99], v[52:55]\n"
    "v_cvt_pk_bf16_f32 v161, v162, v163\n"
    "v_cvt_pk_bf16_f32 v162, v164, v165\n"
    "v_mfma_f32_16x16x32_bf16 v[56:59], a[88:91], a[96:99], v[56:59]\n"
    "v_cvt_pk_bf16_f32 v163, v166, v167\n"
    "v_mfma_f32_16x16x32_bf16 v[60:63], a[92:95], a[96:99], v[60:63]\n"
    "buffer_store_dwordx4 v[160:163], v7, s[12:15], s84 offen sc1\n"
    "v_mfma_f32_16x16x32_bf16 v[224:227], a[132:135], a[188:191], v[224:227]\n"
    "v_cvt_pk_bf16_f32 v168, v168, v169\n"
    "v_cvt_pk_bf16_f32 v169, v170, v171\n"
    "v_mfma_f32_16x16x32_bf16 v[228:231], a[136:139], a[188:191], v[228:231]\n"
    "v_cvt_pk_bf16_f32 v170, v172, v173\n"
    "v_cvt_pk_bf16_f32 v171, v174, v175\n"
    "v_mfma_f32_16x16x32_bf16 v[232:235], a[140:143], a[188:191], v[232:235]\n"
    "buffer_store_dwordx4 v[168:171], v7, s[12:15], s85 offen sc1\n"
    "v_mfma_f32_16x16x32_bf16 v[236:239], a[144:147], a[188:191], v[236:239]\n"
    "s_add_u32 s11, s11, 1\n"
    "s_lshr_b32 s94, s11, 3\n"
    "s_lshl_b32 s94, s94, 19\n"
    "v_mfma_f32_16x16x32_bf16 v[80:83], a[80:83], a[100:103], v[80:83]\n"
    "s_and_b32 s95, s11, 7\n"
    "s_lshl_b32 s95, s95, 11\n"
    "s_add_u32 s94, s94, s95\n"
    "v_mfma_f32_16x16x32_bf16 v[84:87], a[84:87], a[100:103], v[84:87]\n"
    "v_add_u32 v7, s94, v21\n"
    "v_cvt_pk_bf16_f32 v192, v192, v193\n"
    "v_mfma_f32_16x16x32_bf16 v[88:91], a[88:91], a[100:103], v[88:91]\n"
    "v_cvt_pk_bf16_f32 v193, v194, v195\n"
    "v_cvt_pk_bf16_f32 v194, v196, v197\n"
    "v_mfma_f32_16x16x32_bf16 v[92:95], a[92:95], a[100:103], v[92:95]\n"
    "v_cvt_pk_bf16_f32 v195, v198, v199\n"
    "v_mfma_f32_16x16x32_bf16 a[0:3], a[132:135], a[192:195], a[0:3]\n"
    "buffer_store_dwordx4 v[192:195], v7, s[12:15], s84 offen sc1\n"
    "v_mfma_f32_16x16x32_bf16 a[4:7], a[136:139], a[192:195], a[4:7]\n"
    "v_cvt_pk_bf16_f32 v200, v200, v201\n"
    "v_cvt_pk_bf16_f32 v201, v202, v203\n"
    "v_mfma_f32_16x16x32_bf16 a[8:11], a[140:143], a[192:195], a[8:11]\n"
    "v_cvt_pk_bf16_f32 v202, v204, v205\n"
    "v_cvt_pk_bf16_f32 v203, v206, v207\n"
    "v_mfma_f32_16x16x32_bf16 a[12:15], a[144:147], a[192:195], a[12:15]\n"
    "buffer_store_dwordx4 v[200:203], v7, s[12:15], s85 offen sc1\n"
    "v_mfma_f32_16x16x32_bf16 v[112:115], a[80:83], a[104:107], v[112:115]\n"
    "s_add_u32 s11, s11, 1\n"
    "s_lshr_b32 s94, s11, 3\n"
    "s_lshl_b32 s94, s94, 19\n"
    "v_mfma_f32_16x16x32_bf16 v[116:119], a[84:87], a[104:107], v[116:119]\n"
    "s_and_b32 s95, s11, 7\n"
    "s_lshl_b32 s95, s95, 11\n"
    "s_add_u32 s94, s94, s95\n"
    "v_mfma_f32_16x16x32_bf16 v[120:123], a[88:91], a[104:107], v[120:123]\n"
    "v_add_u32 v7, s94, v21\n"
    "v_cvt_pk_bf16_f32 v224, v224, v225\n"
    "v_mfma_f32_16x16x32_bf16 v[124:127], a[92:95], a[104:107], v[124:127]\n"
    "v_cvt_pk_bf16_f32 v225, v226, v227\n"
    "v_cvt_pk_bf16_f32 v226, v228, v229\n"
    "v_mfma_f32_16x16x32_bf16 a[32:35], a[132:135], a[196:199], a[32:35]\n"
    "v_cvt_pk_bf16_f32 v227, v230, v231\n"
    "v_mfma_f32_16x16x32_bf16 a[36:39], a[136:139], a[196:199], a[36:39]\n"
    "buffer_store_dwordx4 v[224:227], v7, s[12:15], s84 offen sc1\n"
    "v_mfma_f32_16x16x32_bf16 a[40:43], a[140:143], a[196:199], a[40:43]\n"
    "v_cvt_pk_bf16_f32 v232, v232, v233\n"
    "v_cvt_pk_bf16_f32 v233, v234, v235\n"
    "v_mfma_f32_16x16x32_bf16 a[44:47], a[144:147], a[196:199], a[44:47]\n"
    "v_cvt_pk_bf16_f32 v234, v236, v237\n"
    "v_cvt_pk_bf16_f32 v235, v238, v239\n"
    "v_mfma_f32_16x16x32_bf16 v[144:147], a[80:83], a[108:111], v[144:147]\n"
    "buffer_store_dwordx4 v[232:235], v7, s[12:15], s85 offen sc1\n"
    "v_mfma_f32_16x16x32_bf16 v[148:151], a[84:87], a[108:111], v[148:151]\n"
    "s_add_u32 s11, s11, 1\n"
    "s_lshr_b32 s94, s11, 3\n"
    "s_lshl_b32 s94, s94, 19\n"
    "v_mfma_f32_16x16x32_bf16 v[152:155], a[88:91], a[108:111], v[152:155]\n"
    "s_and_b32 s95, s11, 7\n"
    "s_lshl_b32 s95, s95, 11\n"
    "s_add_u32 s94, s94, s95\n"
    "v_mfma_f32_16x16x32_bf16 v[156:159], a[92:95], a[108:111], v[156:159]\n"
    "v_add_u32 v7, s94, v21\n"
    "v_accvgpr_read_b32 v64, a0\n"
    "v_mfma_f32_16x16x32_bf16 v[48:51], a[148:151], a[164:167], v[48:51]\n"
    "v_accvgpr_read_b32 v65, a1\n"
    "v_accvgpr_read_b32 v66, a2\n"
    "v_mfma_f32_16x16x32_bf16 v[52:55], a[152:155], a[164:167], v[52:55]\n"
    "v_accvgpr_read_b32 v67, a3\n"
    "v_accvgpr_read_b32 v68, a4\n"
    "v_mfma_f32_16x16x32_bf16 v[56:59], a[156:159], a[164:167], v[56:59]\n"
    "v_accvgpr_read_b32 v69, a5\n"
    "v_accvgpr_read_b32 v70, a6\n"
    "v_mfma_f32_16x16x32_bf16 v[60:63], a[160:163], a[164:167], v[60:63]\n"
    "v_accvgpr_read_b32 v71, a7\n"
    "v_cvt_pk_bf16_f32 v64, v64, v65\n"
    "v_mfma_f32_16x16x32_bf16 v[176:179], a[80:83], a[112:115], v[176:179]\n"
    "v_cvt_pk_bf16_f32 v65, v66, v67\n"
    "v_cvt_pk_bf16_f32 v66, v68, v69\n"
    "v_mfma_f32_16x16x32_bf16 v[180:183], a[84:87], a[112:115], v[180:183]\n"
    "v_cvt_pk_bf16_f32 v67, v70, v71\n"
    "v_mfma_f32_16x16x32_bf16 v[184:187], a[88:91], a[112:115], v[184:187]\n"
    "buffer_store_dwordx4 v[64:67], v7, s[12:15], s84 offen sc1\n"
    "v_mfma_f32_16x16x32_bf16 v[188:191], a[92:95], a[112:115], v[188:191]\n"
    "s_nop 1\n"
    "v_accvgpr_read_b32 v72, a8\n"
    "v_mfma_f32_16x16x32_bf16 v[80:83], a[148:151], a[168:171], v[80:83]\n"
    "v_accvgpr_read_b32 v73, a9\n"
    "v_accvgpr_read_b32 v74, a10\n"
    "v_mfma_f32_16x16x32_bf16 v[84:87], a[152:155], a[168:171], v[84:87]\n"
    "v_accvgpr_read_b32 v75, a11\n"
    "v_accvgpr_read_b32 v76, a12\n"
    "v_mfma_f32_16x16x32_bf16 v[88:91], a[156:159], a[168:171], v[88:91]\n"
    "v_accvgpr_read_b32 v77, a13\n"
    "v_accvgpr_read_b32 v78, a14\n"
    "v_mfma_f32_16x16x32_bf16 v[92:95], a[160:163], a[168:171], v[92:95]\n"
    "v_accvgpr_read_b32 v79, a15\n"
    "v_cvt_pk_bf16_f32 v72, v72, v73\n"
    "v_mfma_f32_16x16x32_bf16 v[208:211], a[80:83], a[116:119], v[208:211]\n"
    "v_cvt_pk_bf16_f32 v73, v74, v75\n"
    "v_cvt_pk_bf16_f32 v74, v76, v77\n"
    "v_mfma_f32_16x16x32_bf16 v[212:215], a[84:87], a[116:119], v[212:215]\n"
    "v_cvt_pk_bf16_f32 v75, v78, v79\n"
    "v_mfma_f32_16x16x32_bf16 v[216:219], a[88:91], a[116:119], v[216:219]\n"
    "buffer_store_dwordx4 v[72:75], v7, s[12:15], s85 offen sc1\n"
    "v_mfma_f32_16x16x32_bf16 v[220:223], a[92:95], a[116:119], v[220:223]\n"
    "s_nop 1\n"
    "s_add_u32 s11, s11, 1\n"
    "s_lshr_b32 s94, s11, 3\n"
    "v_mfma_f32_16x16x32_bf16 v[112:115], a[148:151], a[172:175], v[112:115]\n"
    "s_lshl_b32 s94, s94, 19\n"
    "s_and_b32 s95, s11, 7\n"
    "s_lshl_b32 s95, s95, 11\n"
    "v_mfma_f32_16x16x32_bf16 v[116:119], a[152:155], a[172:175], v[116:119]\n"
    "s_add_u32 s94, s94, s95\n"
    "v_add_u32 v7, s94, v21\n"
    "v_mfma_f32_16x16x32_bf16 v[120:123], a[156:159], a[172:175], v[120:123]\n"
    "s_and_b32 s94, s56, 2\n"
    "s_and_b32 s95, s54, 1\n"
    "s_add_u32 s94, s94, s95\n"
    "v_mfma_f32_16x16x32_bf16 v[124:127], a[160:163], a[172:175], v[124:127]\n"
    "s_lshl_b32 s94, s94, 3\n"
    "s_and_b32 s95, s57, 3\n"
    "s_add_u32 s94, s94, s95\n"
    "v_mfma_f32_16x16x32_bf16 v[240:243], a[80:83], a[120:123], v[240:243]\n"
    "s_lshl_b32 s94, s94, 1\n"
    "s_lshr_b32 s95, s55, 1\n"
    "s_add_u32 s94, s94, s95\n"
    "v_mfma_f32_16x16x32_bf16 v[244:247], a[84:87], a[120:123], v[244:247]\n"
    "s_lshl_b32 s94, s94, 6\n"
    "s_add_u32 s94, s94, 37879808\n"
    "v_mfma_f32_16x16x32_bf16 v[248:251], a[88:91], a[120:123], v[248:251]\n"
    "v_bfe_u32 v1, v0, 3, 2\n"
    "v_lshl_add_u32 v1, v1, 12, s94\n"
    "v_mfma_f32_16x16x32_bf16 v[252:255], a[92:95], a[120:123], v[252:255]\n"
    "v_lshrrev_b32 v2, 5, v0\n"
    "v_lshl_add_u32 v1, v2, 9, v1\n"
    "v_mfma_f32_16x16x32_bf16 v[144:147], a[148:151], a[176:179], v[144:147]\n"
    "v_mov_b32 v2, s11\n"
    "v_lshrrev_b32 v3, 3, v2\n"
    "v_mfma_f32_16x16x32_bf16 v[148:151], a[152:155], a[176:179], v[148:151]\n"
    "v_lshl_add_u32 v3, v3, 14, v1\n"
    "v_and_b32 v2, 7, v2\n"
    "v_mfma_f32_16x16x32_bf16 v[152:155], a[156:159], a[176:179], v[152:155]\n"
    "v_lshl_add_u32 v6, v2, 3, v3\n"
    "v_and_b32 v2, 7, v0\n"
    "v_mfma_f32_16x16x32_bf16 v[156:159], a[160:163], a[176:179], v[156:159]\n"
    "v_add3_u32 v2, v2, s11, -8\n"
    "v_lshrrev_b32 v3, 3, v2\n"
    "v_mfma_f32_16x16x32_bf16 a[16:19], a[80:83], a[124:127], a[16:19]\n"
    "v_lshl_add_u32 v3, v3, 14, v1\n"
    "v_and_b32 v2, 7, v2\n"
    "v_mfma_f32_16x16x32_bf16 a[20:23], a[84:87], a[124:127], a[20:23]\n"
    "v_lshl_add_u32 v4, v2, 3, v3\n"
    "v_accvgpr_read_b32 v32, a32\n"
    "v_mfma_f32_16x16x32_bf16 a[24:27], a[88:91], a[124:127], a[24:27]\n"
    "v_accvgpr_read_b32 v33, a33\n"
    "v_accvgpr_read_b32 v34, a34\n"
    "v_mfma_f32_16x16x32_bf16 a[28:31], a[92:95], a[124:127], a[28:31]\n"
    "v_accvgpr_read_b32 v35, a35\n"
    "v_accvgpr_read_b32 v36, a36\n"
    "v_mfma_f32_16x16x32_bf16 v[176:179], a[148:151], a[180:183], v[176:179]\n"
    "v_accvgpr_read_b32 v37, a37\n"
    "v_accvgpr_read_b32 v38, a38\n"
    "v_mfma_f32_16x16x32_bf16 v[180:183], a[152:155], a[180:183], v[180:183]\n"
    "v_accvgpr_read_b32 v39, a39\n"
    "v_cvt_pk_bf16_f32 v32, v32, v33\n"
    "v_mfma_f32_16x16x32_bf16 v[184:187], a[156:159], a[180:183], v[184:187]\n"
    "v_cvt_pk_bf16_f32 v33, v34, v35\n"
    "v_cvt_pk_bf16_f32 v34, v36, v37\n"
    "v_mfma_f32_16x16x32_bf16 v[188:191], a[160:163], a[180:183], v[188:191]\n"
    "v_cvt_pk_bf16_f32 v35, v38, v39\n"
    "v_mfma_f32_16x16x32_bf16 a[48:51], a[80:83], a[128:131], a[48:51]\n"
    "buffer_store_dwordx4 v[32:35], v7, s[12:15], s84 offen sc1\n"
    "v_mfma_f32_16x16x32_bf16 a[52:55], a[84:87], a[128:131], a[52:55]\n"
    "s_nop 1\n"
    "v_accvgpr_read_b32 v40, a40\n"
    "v_mfma_f32_16x16x32_bf16 a[56:59], a[88:91], a[128:131], a[56:59]\n"
    "v_accvgpr_read_b32 v41, a41\n"
    "v_accvgpr_read_b32 v42, a42\n"
    "v_mfma_f32_16x16x32_bf16 a[60:63], a[92:95], a[128:131], a[60:63]\n"
    "v_accvgpr_read_b32 v43, a43\n"
    "v_accvgpr_read_b32 v44, a44\n"
    "v_mfma_f32_16x16x32_bf16 v[208:211], a[148:151], a[184:187], v[208:211]\n"
    "v_accvgpr_read_b32 v45, a45\n"
    "v_accvgpr_read_b32 v46, a46\n"
    "v_mfma_f32_16x16x32_bf16 v[212:215], a[152:155], a[184:187], v[212:215]\n"
    "v_accvgpr_read_b32 v47, a47\n"
    "v_cvt_pk_bf16_f32 v40, v40, v41\n"
    "v_mfma_f32_16x16x32_bf16 v[216:219], a[156:159], a[184:187], v[216:219]\n"
    "v_cvt_pk_bf16_f32 v41, v42, v43\n"
    "v_cvt_pk_bf16_f32 v42, v44, v45\n"
    "v_mfma_f32_16x16x32_bf16 v[220:223], a[160:163], a[184:187], v[220:223]\n"
    "v_cvt_pk_bf16_f32 v43, v46, v47\n"
    "v_mfma_f32_16x16x32_bf16 v[240:243], a[148:151], a[188:191], v[240:243]\n"
    "buffer_store_dwordx4 v[40:43], v7, s[12:15], s85 offen sc1\n"
    "v_mfma_f32_16x16x32_bf16 v[244:247], a[152:155], a[188:191], v[244:247]\n"
    "s_nop 1\n"
    "s_add_u32 s11, s11, -8\n"
    "s_lshr_b32 s94, s11, 3\n"
    "v_mfma_f32_16x16x32_bf16 v[248:251], a[156:159], a[188:191], v[248:251]\n"
    "s_lshl_b32 s94, s94, 19\n"
    "s_and_b32 s95, s11, 7\n"
    "s_lshl_b32 s95, s95, 11\n"
    "v_mfma_f32_16x16x32_bf16 v[252:255], a[160:163], a[188:191], v[252:255]\n"
    "s_add_u32 s94, s94, s95\n"
    "v_add_u32 v7, s94, v21\n"
    "v_mfma_f32_16x16x32_bf16 a[16:19], a[148:151], a[192:195], a[16:19]\n"
    "v_cvt_pk_bf16_f32 v48, v48, v49\n"
    "v_cvt_pk_bf16_f32 v49, v50, v51\n"
    "v_mfma_f32_16x16x32_bf16 a[20:23], a[152:155], a[192:195], a[20:23]\n"
    "v_cvt_pk_bf16_f32 v50, v52, v53\n"
    "v_cvt_pk_bf16_f32 v51, v54, v55\n"
    "v_mfma_f32_16x16x32_bf16 a[24:27], a[156:159], a[192:195], a[24:27]\n"
    "buffer_store_dwordx4 v[48:51], v7, s[12:15], s86 offen sc1\n"
    "v_mfma_f32_16x16x32_bf16 a[28:31], a[160:163], a[192:195], a[28:31]\n"
    "v_cvt_pk_bf16_f32 v56, v56, v57\n"
    "v_cvt_pk_bf16_f32 v57, v58, v59\n"
    "v_mfma_f32_16x16x32_bf16 a[48:51], a[148:151], a[196:199], a[48:51]\n"
    "v_cvt_pk_bf16_f32 v58, v60, v61\n"
    "v_cvt_pk_bf16_f32 v59, v62, v63\n"
    "v_mfma_f32_16x16x32_bf16 a[52:55], a[152:155], a[196:199], a[52:55]\n"
    "buffer_store_dwordx4 v[56:59], v7, s[12:15], s87 offen sc1\n"
    "v_mfma_f32_16x16x32_bf16 a[56:59], a[156:159], a[196:199], a[56:59]\n"
    "s_add_u32 s11, s11, 1\n"
    "s_lshr_b32 s94, s11, 3\n"
    "s_lshl_b32 s94, s94, 19\n"
    "v_mfma_f32_16x16x32_bf16 a[60:63], a[160:163], a[196:199], a[60:63]\n"
    "s_and_b32 s95, s11, 7\n"
    "s_lshl_b32 s95, s95, 11\n"
    "s_add_u32 s94, s94, s95\n"
    "v_add_u32 v7, s94, v21\n"
    "v_cvt_pk_bf16_f32 v80, v80, v81\n"
    "v_cvt_pk_bf16_f32 v81, v82, v83\n"
    "v_cvt_pk_bf16_f32 v82, v84, v85\n"
    "v_cvt_pk_bf16_f32 v83, v86, v87\n"
    "buffer_store_dwordx4 v[80:83], v7, s[12:15], s86 offen sc1\n"
    "v_cvt_pk_bf16_f32 v88, v88, v89\n"
    "v_cvt_pk_bf16_f32 v89, v90, v91\n"
    "v_cvt_pk_bf16_f32 v90, v92, v93\n"
    "v_cvt_pk_bf16_f32 v91, v94, v95\n"
    "buffer_store_dwordx4 v[88:91], v7, s[12:15], s87 offen sc1\n"
    "s_waitcnt vmcnt(4)\n"
    "v_mov_b32 v2, s8\n"
    "v_mov_b32 v3, s9\n"
    "global_store_dwordx2 v6, v[2:3], s[52:53] sc1\n"
    "global_store_dwordx2 v4, v[2:3], s[52:53] sc1\n"
    "s_add_u32 s11, s11, 1\n"
    "s_lshr_b32 s94, s11, 3\n"
    "s_lshl_b32 s94, s94, 19\n"
    "s_and_b32 s95, s11, 7\n"
    "s_lshl_b32 s95, s95, 11\n"
    "s_add_u32 s94, s94, s95\n"
    "v_add_u32 v7, s94, v21\n"
    "v_cvt_pk_bf16_f32 v112, v112, v113\n"
    "v_cvt_pk_bf16_f32 v113, v114, v115\n"
    "v_cvt_pk_bf16_f32 v114, v116, v117\n"
    "v_cvt_pk_bf16_f32 v115, v118, v119\n"
    "buffer_store_dwordx4 v[112:115], v7, s[12:15], s86 offen sc1\n"
    "v_cvt_pk_bf16_f32 v120, v120, v121\n"
    "v_cvt_pk_bf16_f32 v121, v122, v123\n"
    "v_cvt_pk_bf16_f32 v122, v124, v125\n"
    "v_cvt_pk_bf16_f32 v123, v126, v127\n"
    "buffer_store_dwordx4 v[120:123], v7, s[12:15], s87 offen sc1\n"
    "s_add_u32 s11, s11, 1\n"
    "s_lshr_b32 s94, s11, 3\n"
    "s_lshl_b32 s94, s94, 19\n"
    "s_and_b32 s95, s11, 7\n"
    "s_lshl_b32 s95, s95, 11\n"
    "s_add_u32 s94, s94, s95\n"
    "v_add_u32 v7, s94, v21\n"
    "v_cvt_pk_bf16_f32 v144, v144, v145\n"
    "v_cvt_pk_bf16_f32 v145, v146, v147\n"
    "v_cvt_pk_bf16_f32 v146, v148, v149\n"
    "v_cvt_pk_bf16_f32 v147, v150, v151\n"
    "buffer_store_dwordx4 v[144:147], v7, s[12:15], s86 offen sc1\n"
    "v_cvt_pk_bf16_f32 v152, v152, v153\n"
    "v_cvt_pk_bf16_f32 v153, v154, v155\n"
    "v_cvt_pk_bf16_f32 v154, v156, v157\n"
    "v_cvt_pk_bf16_f32 v155, v158, v159\n"
    "buffer_store_dwordx4 v[152:155], v7, s[12:15], s87 offen sc1\n"
    "s_add_u32 s11, s11, 1\n"
    "s_lshr_b32 s94, s11, 3\n"
    "s_lshl_b32 s94, s94, 19\n"
    "s_and_b32 s95, s11, 7\n"
    "s_lshl_b32 s95, s95, 11\n"
    "s_add_u32 s94, s94, s95\n"
    "v_add_u32 v7, s94, v21\n"
    "v_cvt_pk_bf16_f32 v176, v176, v177\n"
    "v_cvt_pk_bf16_f32 v177, v178, v179\n"
    "v_cvt_pk_bf16_f32 v178, v180, v181\n"
    "v_cvt_pk_bf16_f32 v179, v182, v183\n"
    "buffer_store_dwordx4 v[176:179], v7, s[12:15], s86 offen sc1\n"
    "v_cvt_pk_bf16_f32 v184, v184, v185\n"
    "v_cvt_pk_bf16_f32 v185, v186, v187\n"
    "v_cvt_pk_bf16_f32 v186, v188, v189\n"
    "v_cvt_pk_bf16_f32 v187, v190, v191\n"
    "buffer_store_dwordx4 v[184:187], v7, s[12:15], s87 offen sc1\n"
    "s_add_u32 s11, s11, 1\n"
    "s_lshr_b32 s94, s11, 3\n"
    "s_lshl_b32 s94, s94, 19\n"
    "s_and_b32 s95, s11, 7\n"
    "s_lshl_b32 s95, s95, 11\n"
    "s_add_u32 s94, s94, s95\n"
    "v_add_u32 v7, s94, v21\n"
    "v_cvt_pk_bf16_f32 v208, v208, v209\n"
    "v_cvt_pk_bf16_f32 v209, v210, v211\n"
    "v_cvt_pk_bf16_f32 v210, v212, v213\n"
    "v_cvt_pk_bf16_f32 v211, v214, v215\n"
    "buffer_store_dwordx4 v[208:211], v7, s[12:15], s86 offen sc1\n"
    "v_cvt_pk_bf16_f32 v216, v216, v217\n"
    "v_cvt_pk_bf16_f32 v217, v218, v219\n"
    "v_cvt_pk_bf16_f32 v218, v220, v221\n"
    "v_cvt_pk_bf16_f32 v219, v222, v223\n"
    "buffer_store_dwordx4 v[216:219], v7, s[12:15], s87 offen sc1\n"
    "s_add_u32 s11, s11, 1\n"
    "s_lshr_b32 s94, s11, 3\n"
    "s_lshl_b32 s94, s94, 19\n"
    "s_and_b32 s95, s11, 7\n"
    "s_lshl_b32 s95, s95, 11\n"
    "s_add_u32 s94, s94, s95\n"
    "v_add_u32 v7, s94, v21\n"
    "v_cvt_pk_bf16_f32 v240, v240, v241\n"
    "v_cvt_pk_bf16_f32 v241, v242, v243\n"
    "v_cvt_pk_bf16_f32 v242, v244, v245\n"
    "v_cvt_pk_bf16_f32 v243, v246, v247\n"
    "buffer_store_dwordx4 v[240:243], v7, s[12:15], s86 offen sc1\n"
    "v_cvt_pk_bf16_f32 v248, v248, v249\n"
    "v_cvt_pk_bf16_f32 v249, v250, v251\n"
    "v_cvt_pk_bf16_f32 v250, v252, v253\n"
    "v_cvt_pk_bf16_f32 v251, v254, v255\n"
    "buffer_store_dwordx4 v[248:251], v7, s[12:15], s87 offen sc1\n"
    "s_add_u32 s11, s11, 1\n"
    "s_lshr_b32 s94, s11, 3\n"
    "s_lshl_b32 s94, s94, 19\n"
    "s_and_b32 s95, s11, 7\n"
    "s_lshl_b32 s95, s95, 11\n"
    "s_add_u32 s94, s94, s95\n"
    "v_add_u32 v7, s94, v21\n"
    "s_nop 7\n"
    "v_accvgpr_read_b32 v80, a16\n"
    "v_accvgpr_read_b32 v81, a17\n"
    "v_accvgpr_read_b32 v82, a18\n"
    "v_accvgpr_read_b32 v83, a19\n"
    "v_accvgpr_read_b32 v84, a20\n"
    "v_accvgpr_read_b32 v85, a21\n"
    "v_accvgpr_read_b32 v86, a22\n"
    "v_accvgpr_read_b32 v87, a23\n"
    "v_cvt_pk_bf16_f32 v80, v80, v81\n"
    "v_cvt_pk_bf16_f32 v81, v82, v83\n"
    "v_cvt_pk_bf16_f32 v82, v84, v85\n"
    "v_cvt_pk_bf16_f32 v83, v86, v87\n"
    "buffer_store_dwordx4 v[80:83], v7, s[12:15], s86 offen sc1\n"
    "s_nop 1\n"
    "v_accvgpr_read_b32 v88, a24\n"
    "v_accvgpr_read_b32 v89, a25\n"
    "v_accvgpr_read_b32 v90, a26\n"
    "v_accvgpr_read_b32 v91, a27\n"
    "v_accvgpr_read_b32 v92, a28\n"
    "v_accvgpr_read_b32 v93, a29\n"
    "v_accvgpr_read_b32 v94, a30\n"
    "v_accvgpr_read_b32 v95, a31\n"
    "v_cvt_pk_bf16_f32 v88, v88, v89\n"
    "v_cvt_pk_bf16_f32 v89, v90, v91\n"
    "v_cvt_pk_bf16_f32 v90, v92, v93\n"
    "v_cvt_pk_bf16_f32 v91, v94, v95\n"
    "buffer_store_dwordx4 v[88:91], v7, s[12:15], s87 offen sc1\n"
    "s_nop 1\n"
    "s_add_u32 s11, s11, 1\n"
    "s_lshr_b32 s94, s11, 3\n"
    "s_lshl_b32 s94, s94, 19\n"
    "s_and_b32 s95, s11, 7\n"
    "s_lshl_b32 s95, s95, 11\n"
    "s_add_u32 s94, s94, s95\n"
    "v_add_u32 v7, s94, v21\n"
    "s_nop 7\n"
    "v_accvgpr_read_b32 v48, a48\n"
    "v_accvgpr_read_b32 v49, a49\n"
    "v_accvgpr_read_b32 v50, a50\n"
    "v_accvgpr_read_b32 v51, a51\n"
    "v_accvgpr_read_b32 v52, a52\n"
    "v_accvgpr_read_b32 v53, a53\n"
    "v_accvgpr_read_b32 v54, a54\n"
    "v_accvgpr_read_b32 v55, a55\n"
    "v_cvt_pk_bf16_f32 v48, v48, v49\n"
    "v_cvt_pk_bf16_f32 v49, v50, v51\n"
    "v_cvt_pk_bf16_f32 v50, v52, v53\n"
    "v_cvt_pk_bf16_f32 v51, v54, v55\n"
    "buffer_store_dwordx4 v[48:51], v7, s[12:15], s86 offen sc1\n"
    "s_nop 1\n"
    "TC5:\n"
    "s_cbranch_execz TC9\n"
    "v_accvgpr_read_b32 v56, a56\n"
    "v_accvgpr_read_b32 v57, a57\n"
    "v_accvgpr_read_b32 v58, a58\n"
    "v_accvgpr_read_b32 v59, a59\n"
    "v_accvgpr_read_b32 v60, a60\n"
    "v_accvgpr_read_b32 v61, a61\n"
    "v_accvgpr_read_b32 v62, a62\n"
    "v_accvgpr_read_b32 v63, a63\n"
    "v_cvt_pk_bf16_f32 v56, v56, v57\n"
    "v_cvt_pk_bf16_f32 v57, v58, v59\n"
    "v_cvt_pk_bf16_f32 v58, v60, v61\n"
    "v_cvt_pk_bf16_f32 v59, v62, v63\n"
    "buffer_store_dwordx4 v[56:59], v7, s[12:15], s87 offen sc1\n"
    "s_nop 1\n"
    "s_add_u32 s11, s11, -8\n"
    "v_lshrrev_b32 v1, 4, v0\n"
    "v_add_u32 v1, 0, v1\n"
    "v_and_b32 v2, 7, v0\n"
    "v_xor_b32 v1, v1, v2\n"
    "v_lshlrev_b32 v1, 4, v1\n"
    "v_lshrrev_b32 v2, 3, v0\n"
    "v_and_b32 v3, 4, v2\n"
    "v_add_u32 v2, v2, v3\n"
    "v_lshl_add_u32 v8, v2, 12, v1\n"
    "v_lshrrev_b32 v1, 4, v0\n"
    "v_add_u32 v1, 4, v1\n"
    "v_and_b32 v2, 7, v0\n"
    "v_xor_b32 v1, v1, v2\n"
    "v_lshlrev_b32 v1, 4, v1\n"
    "v_lshrrev_b32 v2, 3, v0\n"
    "v_and_b32 v3, 4, v2\n"
    "v_add_u32 v2, v2, v3\n"
    "v_lshl_add_u32 v9, v2, 12, v1\n"
    "s_lshl_b32 s94, s55, 1\n"
    "s_add_u32 s95, s94, s56\n"
    "s_add_u32 s95, s95, 0\n"
    "s_and_b32 s95, s95, 3\n"
    "s_and_b32 s94, s94, 4\n"
    "s_add_u32 s94, s94, s95\n"
    "s_lshl_b32 s94, s94, 17\n"
    "s_add_u32 s76, s94, 0\n"
    "s_add_u32 s77, s94, 64512\n"
    "s_add_u32 s78, s94, 14336\n"
    "s_add_u32 s79, s94, 78848\n"
    "s_lshl_b32 s94, s55, 1\n"
    "s_add_u32 s95, s94, s56\n"
    "s_add_u32 s95, s95, 1\n"
    "s_and_b32 s95, s95, 3\n"
    "s_and_b32 s94, s94, 4\n"
    "s_add_u32 s94, s94, s95\n"
    "s_lshl_b32 s94, s94, 17\n"
    "s_add_u32 s80, s94, 0\n"
    "s_add_u32 s81, s94, 64512\n"
    "s_add_u32 s82, s94, 14336\n"
    "s_add_u32 s83, s94, 78848\n"
    "s_lshl_b32 s94, s57, 9\n"
    "s_and_b32 s95, s56, 1\n"
    "s_add_u32 s94, s94, s95\n"
    "s_lshl_b32 s94, s94, 11\n"
    "s_add_u32 s60, s34, s94\n"
    "s_addc_u32 s61, s35, 0\n"
    "s_and_b32 s61, s61, 0xffff\n"
    "s_mov_b32 s62, 1044608\n"
    "s_mov_b32 s63, 0x20000\n"
    "s_lshl_b32 s92, s55, 13\n"
    "s_lshl_b32 s98, s55, 12\n"
    "s_add_u32 m0, s92, 0\n"
    "s_nop 0\n"
    "buffer_load_dwordx4 v8, s[60:63], s76 offen offset:0 sc1 lds\n"
    "buffer_load_dwordx4 v9, s[60:63], s77 offen offset:1024 sc1 lds\n"
    "buffer_load_dwordx4 v8, s[60:63], s78 offen offset:2048 sc1 lds\n"
    "buffer_load_dwordx4 v9, s[60:63], s79 offen offset:3072 sc1 lds\n"
    "s_add_u32 m0, s92, 4096\n"
    "s_nop 0\n"
    "buffer_load_dwordx4 v8, s[60:63], s80 offen offset:0 sc1 lds\n"
    "buffer_load_dwordx4 v9, s[60:63], s81 offen offset:1024 sc1 lds\n"
    "buffer_load_dwordx4 v8, s[60:63], s82 offen offset:2048 sc1 lds\n"
    "buffer_load_dwordx4 v9, s[60:63], s83 offen offset:3072 sc1 lds\n"
    "s_add_u32 s60, s60, 256\n"
    "s_addc_u32 s61, s61, 0\n"
    "s_add_u32 s72, s52, 0\n"
    "s_addc_u32 s73, s53, 0\n"
    "s_and_b32 s73, s73, 0xffff\n"
    "s_mov_b32 s74, 0\n"
    "s_mov_b32 s75, 0x20000\n"
    "s_lshl_b32 s94, s54, 9\n"
    "s_add_u32 s94, s94, 37879808\n"
    "v_lshl_add_u32 v20, v0, 3, s94\n"
    "s_and_b32 s95, s56, 1\n"
    "s_lshr_b32 s94, s56, 1\n"
    "s_lshl_b32 s94, s94, 3\n"
    "s_add_u32 s94, s94, s58\n"
    "s_lshl_b32 s94, s94, 1\n"
    "s_add_u32 s94, s94, s95\n"
    "s_lshl_b32 s94, s94, 18\n"
    "v_add_u32 v10, s98, v21\n"
    "s_add_u32 s94, s94, 16777216\n"
    "s_add_u32 s64, s52, s94\n"
    "s_addc_u32 s65, s53, 0\n"
    "s_and_b32 s65, s65, 0xffff\n"
    "s_mov_b32 s66, 16384\n"
    "s_mov_b32 s67, 0x20000\n"
    "v_add_u32 v6, 131072, v6\n"
    "v_add_u32 v4, 131072, v4\n"
    "s_waitcnt vmcnt(8)\n"
    "v_mov_b32 v2, s8\n"
    "v_mov_b32 v3, s9\n"
    "global_store_dwordx2 v6, v[2:3], s[52:53] sc1\n"
    "global_store_dwordx2 v4, v[2:3], s[52:53] sc1\n"
    "s_add_u32 m0, s92, 49152\n"
    "s_nop 0\n"
    "buffer_load_dwordx4 v8, s[60:63], s76 offen offset:0 sc1 lds\n"
    "buffer_load_dwordx4 v9, s[60:63], s77 offen offset:1024 sc1 lds\n"
    "buffer_load_dwordx4 v8, s[60:63], s78 offen offset:2048 sc1 lds\n"
    "buffer_load_dwordx4 v9, s[60:63], s79 offen offset:3072 sc1 lds\n"
    "s_add_u32 m0, s92, 53248\n"
    "s_nop 0\n"
    "buffer_load_dwordx4 v8, s[60:63], s80 offen offset:0 sc1 lds\n"
    "buffer_load_dwordx4 v9, s[60:63], s81 offen offset:1024 sc1 lds\n"
    "buffer_load_dwordx4 v8, s[60:63], s82 offen offset:2048 sc1 lds\n"
    "buffer_load_dwordx4 v9, s[60:63], s83 offen offset:3072 sc1 lds\n"
    "s_add_u32 s60, s60, 256\n"
    "s_addc_u32 s61, s61, 0\n"
    "s_add_u32 m0, s92, 98304\n"
    "s_nop 0\n"
    "buffer_load_dwordx4 v8, s[60:63], s76 offen offset:0 sc1 lds\n"
    "buffer_load_dwordx4 v9, s[60:63], s77 offen offset:1024 sc1 lds\n"
    "buffer_load_dwordx4 v8, s[60:63], s78 offen offset:2048 sc1 lds\n"
    "buffer_load_dwordx4 v9, s[60:63], s79 offen offset:3072 sc1 lds\n"
    "s_add_u32 m0, s92, 102400\n"
    "s_nop 0\n"
    "buffer_load_dwordx4 v8, s[60:63], s80 offen offset:0 sc1 lds\n"
    "buffer_load_dwordx4 v9, s[60:63], s81 offen offset:1024 sc1 lds\n"
    "buffer_load_dwordx4 v8, s[60:63], s82 offen offset:2048 sc1 lds\n"
    "buffer_load_dwordx4 v9, s[60:63], s83 offen offset:3072 sc1 lds\n"
    "s_add_u32 s60, s60, 256\n"
    "s_addc_u32 s61, s61, 0\n"
    "s_lshl_b32 s94, s56, 8\n"
    "s_add_u32 s94, s94, 37748736\n"
    "s_movk_i32 s95, 0x300\n"
    "v_lshl_add_u32 v4, v31, 3, s94\n"
    "global_load_dwordx2 a[200:201], v4, s[52:53] offset:0 sc1\n"
    "global_load_dwordx2 a[202:203], v4, s[52:53] offset:32 sc1\n"
    "v_add_u32 v5, 256, v4\n"
    "v_bfi_b32 v4, s95, v5, v4\n"
    "global_load_dwordx2 a[204:205], v4, s[52:53] offset:0 sc1\n"
    "global_load_dwordx2 a[206:207], v4, s[52:53] offset:32 sc1\n"
    "v_add_u32 v5, 256, v4\n"
    "v_bfi_b32 v4, s95, v5, v4\n"
    "global_load_dwordx2 a[208:209], v4, s[52:53] offset:0 sc1\n"
    "global_load_dwordx2 a[210:211], v4, s[52:53] offset:32 sc1\n"
    "v_add_u32 v5, 256, v4\n"
    "v_bfi_b32 v4, s95, v5, v4\n"
    "global_load_dwordx2 a[212:213], v4, s[52:53] offset:0 sc1\n"
    "global_load_dwordx2 a[214:215], v4, s[52:53] offset:32 sc1\n"
    "s_mov_b32 s68, 0x40000\n"
    "APZ:\n"
    "global_load_dwordx2 v[168:169], v20, s[52:53] sc1\n"
    "s_waitcnt vmcnt(0)\n"
    "v_cmp_ne_u64 vcc, s[8:9], v[168:169]\n"
    "s_cbranch_vccz APZ_ok\n"
    "s_sleep 1\n"
    "s_sub_u32 s68, s68, 1\n"
    "s_cbranch_scc0 APZ\n"
    "APZ_ok:\n"
    "s_add_u32 m0, s98, 32768\n"
    "s_nop 0\n"
    "buffer_load_dwordx4 v10, s[64:67], 0 offen offset:0 sc1 lds\n"
    "buffer_load_dwordx4 v10, s[64:67], 0 offen offset:1024 sc1 lds\n"
    "buffer_load_dwordx4 v10, s[64:67], 0 offen offset:2048 sc1 lds\n"
    "buffer_load_dwordx4 v10, s[64:67], 0 offen offset:3072 sc1 lds\n"
    "s_add_u32 s64, s64, 32768\n"
    "s_addc_u32 s65, s65, 0\n"
    "s_add_u32 m0, s98, 81920\n"
    "s_nop 0\n"
    "buffer_load_dwordx4 v10, s[64:67], 0 offen offset:0 sc1 lds\n"
    "buffer_load_dwordx4 v10, s[64:67], 0 offen offset:1024 sc1 lds\n"
    "buffer_load_dwordx4 v10, s[64:67], 0 offen offset:2048 sc1 lds\n"
    "buffer_load_dwordx4 v10, s[64:67], 0 offen offset:3072 sc1 lds\n"
    "s_add_u32 s64, s64, 32768\n"
    "s_addc_u32 s65, s65, 0\n"
    "s_add_u32 m0, s98, 131072\n"
    "s_nop 0\n"
    "buffer_load_dwordx4 v10, s[64:67], 0 offen offset:0 sc1 lds\n"
    "buffer_load_dwordx4 v10, s[64:67], 0 offen offset:1024 sc1 lds\n"
    "buffer_load_dwordx4 v10, s[64:67], 0 offen offset:2048 sc1 lds\n"
    "buffer_load_dwordx4 v10, s[64:67], 0 offen offset:3072 sc1 lds\n"
    "s_add_u32 s64, s64, 32768\n"
    "s_addc_u32 s65, s65, 0\n"
    "v_accvgpr_read_b32 v2, a201\n"
    "v_accvgpr_read_b32 v22, a200\n"
    "v_xor_b32 v2, s8, v2\n"
    "v_add_f32 v22, 1.0, v22\n"
    "v_accvgpr_read_b32 v3, a203\n"
    "v_accvgpr_read_b32 v23, a202\n"
    "v_xor_b32 v3, s8, v3\n"
    "v_add_f32 v23, 1.0, v23\n"
    "v_or_b32 v5, v2, v3\n"
    "v_accvgpr_read_b32 v2, a205\n"
    "v_accvgpr_read_b32 v24, a204\n"
    "v_xor_b32 v2, s8, v2\n"
    "v_add_f32 v24, 1.0, v24\n"
    "v_accvgpr_read_b32 v3, a207\n"
    "v_accvgpr_read_b32 v25, a206\n"
    "v_xor_b32 v3, s8, v3\n"
    "v_add_f32 v25, 1.0, v25\n"
    "v_or3_b32 v5, v2, v3, v5\n"
    "v_accvgpr_read_b32 v2, a209\n"
    "v_accvgpr_read_b32 v26, a208\n"
    "v_xor_b32 v2, s8, v2\n"
    "v_add_f32 v26, 1.0, v26\n"
    "v_accvgpr_read_b32 v3, a211\n"
    "v_accvgpr_read_b32 v27, a210\n"
    "v_xor_b32 v3, s8, v3\n"
    "v_add_f32 v27, 1.0, v27\n"
    "v_or3_b32 v5, v2, v3, v5\n"
    "v_accvgpr_read_b32 v2, a213\n"
    "v_accvgpr_read_b32 v28, a212\n"
    "v_xor_b32 v2, s8, v2\n"
    "v_add_f32 v28, 1.0, v28\n"
    "v_accvgpr_read_b32 v3, a215\n"
    "v_accvgpr_read_b32 v29, a214\n"
    "v_xor_b32 v3, s8, v3\n"
    "v_add_f32 v29, 1.0, v29\n"
    "v_or3_b32 v5, v2, v3, v5\n"
    "v_cmp_ne_u32 vcc, 0, v5\n"
    "s_cbranch_vccnz GRZ_slow\n"
    "GRZ_back:\n"
    "P2_start:\n"
    "s_lshr_b32 s94, s55, 1\n"
    "s_lshl_b32 s94, s94, 14\n"
    "v_and_b32 v1, 15, v0\n"
    "v_lshrrev_b32 v2, 4, v0\n"
    "v_lshrrev_b32 v3, 1, v1\n"
    "v_xor_b32 v2, v2, v3\n"
    "v_lshlrev_b32 v2, 4, v2\n"
    "v_lshl_add_u32 v2, v1, 7, v2\n"
    "v_add_u32 v12, s94, v2\n"
    "v_xor_b32 v2, 64, v2\n"
    "v_add_u32 v13, s94, v2\n"
    "s_add_u32 s94, s94, 49152\n"
    "v_add_u32 v15, s94, v21\n"
    "s_and_b32 s94, s55, 1\n"
    "s_lshl_b32 s95, s94, 13\n"
    "s_add_u32 s95, s95, 32768\n"
    "v_add_u32 v14, s95, v21\n"
    "s_mov_b32 s96, 49152\n"
    "s_mov_b32 s97, 4294868992\n"
    "s_mov_b32 s99, 49152\n"
    "s_mov_b32 s93, 49152\n"
    "s_mov_b32 s91, 1\n"
    "s_cmp_eq_u32 s58, 7\n"
    "s_waitcnt vmcnt(12)\n"
    "s_cbranch_scc0 P2_w0\n"
    "s_waitcnt vmcnt(8)\n"
    "P2_w0:\n"
    "s_barrier\n"
    "ds_read_b128 a[64:67], v12 offset:0\n"
    "ds_read_b128 a[96:99], v14 offset:0\n"
    "ds_read_b128 a[100:103], v14 offset:2048\n"
    "ds_read_b128 a[104:107], v14 offset:4096\n"
    "ds_read_b128 a[108:111], v14 offset:6144\n"
    "ds_read_b128 a[68:71], v12 offset:2048\n"
    "ds_read_b128 a[72:75], v12 offset:4096\n"
    "ds_read_b128 a[76:79], v12 offset:6144\n"
    "ds_read_b128 a[80:83], v12 offset:8192\n"
    "ds_read_b128 a[84:87], v12 offset:10240\n"
    "ds_read_b128 a[88:91], v12 offset:12288\n"
    "ds_read_b128 a[92:95], v12 offset:14336\n"
    "s_waitcnt lgkmcnt(7)\n"
    "ds_read_b128 a[112:115], v13 offset:0\n"
    "v_mfma_f32_16x16x32_bf16 v[128:131], a[96:99], a[64:67], 0\n"
    "ds_read_b128 a[144:147], v14 offset:1024\n"
    "v_add_u32 v20, 131072, v20\n"
    "v_mfma_f32_16x16x32_bf16 v[132:135], a[100:103], a[64:67], 0\n"
    "ds_read_b128 a[148:151], v14 offset:3072\n"
    "s_lshl_b32 s94, s94, 2\n"
    "v_mfma_f32_16x16x32_bf16 v[136:139], a[104:107], a[64:67], 0\n"
    "ds_read_b128 a[152:155], v14 offset:5120\n"
    "v_lshrrev_b32 v3, 2, v0\n"
    "v_mfma_f32_16x16x32_bf16 v[140:143], a[108:111], a[64:67], 0\n"
    "s_waitcnt lgkmcnt(10)\n"
    "ds_read_b128 a[156:159], v14 offset:7168\n"
    "v_and_b32 v1, 7, v3\n"
    "v_mfma_f32_16x16x32_bf16 v[156:159], a[108:111], a[68:71], 0\n"
    "ds_read_b128 a[116:119], v13 offset:2048\n"
    "v_xor_b32 v1, s94, v1\n"
    "v_mfma_f32_16x16x32_bf16 v[152:155], a[104:107], a[68:71], 0\n"
    "ds_read_b128 a[120:123], v13 offset:4096\n"
    "v_and_b32 v2, 3, v0\n"
    "v_mfma_f32_16x16x32_bf16 v[148:151], a[100:103], a[68:71], 0\n"
    "ds_read_b128 a[124:127], v13 offset:6144\n"
    "v_lshlrev_b32 v2, 3, v2\n"
    "v_mfma_f32_16x16x32_bf16 v[144:147], a[96:99], a[68:71], 0\n"
    "s_waitcnt lgkmcnt(13)\n"
    "ds_read_b128 a[128:131], v13 offset:8192\n"
    "v_min_u32 v98, 6, v3\n"
    "v_mfma_f32_16x16x32_bf16 v[160:163], a[96:99], a[72:75], 0\n"
    "ds_read_b128 a[132:135], v13 offset:10240\n"
    "v_lshl_add_u32 v98, v98, 10, v2\n"
    "v_mfma_f32_16x16x32_bf16 v[164:167], a[100:103], a[72:75], 0\n"
    "ds_read_b128 a[136:139], v13 offset:12288\n"
    "v_lshl_add_u32 v3, v3, 8, v2\n"
    "v_mfma_f32_16x16x32_bf16 v[168:171], a[104:107], a[72:75], 0\n"
    "ds_read_b128 a[140:143], v13 offset:14336\n"
    "v_add_u32 v3, 81920, v3\n"
    "v_mfma_f32_16x16x32_bf16 v[172:175], a[108:111], a[72:75], 0\n"
    "s_waitcnt lgkmcnt(12)\n"
    "v_xor_b32 v2, 0, v1\n"
    "v_mfma_f32_16x16x32_bf16 v[188:191], a[108:111], a[76:79], 0\n"
    "v_add_u32 v12, 49152, v12\n"
    "v_lshl_add_u32 v16, v2, 5, v3\n"
    "v_mfma_f32_16x16x32_bf16 v[184:187], a[104:107], a[76:79], 0\n"
    "v_add_u32 v13, 49152, v13\n"
    "v_xor_b32 v2, 1, v1\n"
    "v_mfma_f32_16x16x32_bf16 v[180:183], a[100:103], a[76:79], 0\n"
    "v_add_u32 v14, 49152, v14\n"
    "v_lshl_add_u32 v17, v2, 5, v3\n"
    "v_mfma_f32_16x16x32_bf16 v[176:179], a[96:99], a[76:79], 0\n"
    "v_xor_b32 v2, 2, v1\n"
    "v_lshl_add_u32 v18, v2, 5, v3\n"
    "v_mfma_f32_16x16x32_bf16 v[192:195], a[96:99], a[80:83], 0\n"
    "v_xor_b32 v2, 3, v1\n"
    "v_lshl_add_u32 v19, v2, 5, v3\n"
    "v_mfma_f32_16x16x32_bf16 v[196:199], a[100:103], a[80:83], 0\n"
    "s_and_b32 s94, s54, 31\n"
    "s_lshl_b32 s94, s94, 5\n"
    "s_add_u32 s94, s94, 37773312\n"
    "v_mfma_f32_16x16x32_bf16 v[200:203], a[104:107], a[80:83], 0\n"
    "v_add_u32 v98, s94, v98\n"
    "s_lshl_b32 s94, s55, 15\n"
    "v_mfma_f32_16x16x32_bf16 v[204:207], a[108:111], a[80:83], 0\n"
    "v_lshrrev_b32 v1, 4, v0\n"
    "v_or_b32 v2, 0, v1\n"
    "v_mfma_f32_16x16x32_bf16 v[220:223], a[108:111], a[84:87], 0\n"
    "v_lshlrev_b32 v2, 1, v2\n"
    "v_and_b32 v3, 15, v0\n"
    "v_mfma_f32_16x16x32_bf16 v[216:219], a[104:107], a[84:87], 0\n"
    "v_xor_b32 v2, v2, v3\n"
    "v_lshlrev_b32 v2, 4, v2\n"
    "v_mfma_f32_16x16x32_bf16 v[212:215], a[100:103], a[84:87], 0\n"
    "v_mul_u32_u24 v1, 2048, v1\n"
    "v_add3_u32 v96, v1, v2, s94\n"
    "v_mfma_f32_16x16x32_bf16 v[208:211], a[96:99], a[84:87], 0\n"
    "v_xor_b32 v97, 128, v96\n"
    "s_cmp_eq_u32 s58, 7\n"
    "v_mfma_f32_16x16x32_bf16 v[224:227], a[96:99], a[88:91], 0\n"
    "v_mfma_f32_16x16x32_bf16 v[228:231], a[100:103], a[88:91], 0\n"
    "v_mfma_f32_16x16x32_bf16 v[232:235], a[104:107], a[88:91], 0\n"
    "v_mfma_f32_16x16x32_bf16 v[236:239], a[108:111], a[88:91], 0\n"
    "v_mfma_f32_16x16x32_bf16 v[252:255], a[108:111], a[92:95], 0\n"
    "v_mfma_f32_16x16x32_bf16 v[248:251], a[104:107], a[92:95], 0\n"
    "v_mfma_f32_16x16x32_bf16 v[244:247], a[100:103], a[92:95], 0\n"
    "v_mfma_f32_16x16x32_bf16 v[240:243], a[96:99], a[92:95], 0\n"
    "s_waitcnt lgkmcnt(0) vmcnt(8)\n"
    "s_cbranch_scc0 P2_w1\n"
    "s_waitcnt vmcnt(4)\n"
    "P2_w1:\n"
    "s_barrier\n"
    "ds_read_b128 a[64:67], v12 offset:0\n"
    "v_mfma_f32_16x16x32_bf16 v[128:131], a[144:147], a[112:115], v[128:131]\n"
    "ds_read_b128 a[96:99], v14 offset:0\n"
    "s_add_u32 m0, s92, 0\n"
    "v_mfma_f32_16x16x32_bf16 v[132:135], a[148:151], a[112:115], v[132:135]\n"
    "buffer_load_dwordx4 v8, s[60:63], s76 offen offset:0 sc1 lds\n"
    "v_mfma_f32_16x16x32_bf16 v[136:139], a[152:155], a[112:115], v[136:139]\n"
    "ds_read_b128 a[100:103], v14 offset:2048\n"
    "v_mfma_f32_16x16x32_bf16 v[140:143], a[156:159], a[112:115], v[140:143]\n"
    "ds_read_b128 a[104:107], v14 offset:4096\n"
    "v_mfma_f32_16x16x32_bf16 v[156:159], a[156:159], a[116:119], v[156:159]\n"
    "v_mfma_f32_16x16x32_bf16 v[152:155], a[152:155], a[116:119], v[152:155]\n"
    "ds_read_b128 a[108:111], v14 offset:6144\n"
    "v_mfma_f32_16x16x32_bf16 v[148:151], a[148:151], a[116:119], v[148:151]\n"
    "ds_read_b128 a[68:71], v12 offset:2048\n"
    "buffer_load_dwordx4 v9, s[60:63], s77 offen offset:1024 sc1 lds\n"
    "v_mfma_f32_16x16x32_bf16 v[144:147], a[144:147], a[116:119], v[144:147]\n"
    "v_mfma_f32_16x16x32_bf16 v[160:163], a[144:147], a[120:123], v[160:163]\n"
    "ds_read_b128 a[72:75], v12 offset:4096\n"
    "v_mfma_f32_16x16x32_bf16 v[164:167], a[148:151], a[120:123], v[164:167]\n"
    "ds_read_b128 a[76:79], v12 offset:6144\n"
    "v_mfma_f32_16x16x32_bf16 v[168:171], a[152:155], a[120:123], v[168:171]\n"
    "v_mfma_f32_16x16x32_bf16 v[172:175], a[156:159], a[120:123], v[172:175]\n"
    "ds_read_b128 a[80:83], v12 offset:8192\n"
    "buffer_load_dwordx4 v8, s[60:63], s78 offen offset:2048 sc1 lds\n"
    "v_mfma_f32_16x16x32_bf16 v[188:191], a[156:159], a[124:127], v[188:191]\n"
    "ds_read_b128 a[84:87], v12 offset:10240\n"
    "v_mfma_f32_16x16x32_bf16 v[184:187], a[152:155], a[124:127], v[184:187]\n"
    "buffer_store_dwordx4 v[48:51], v7, s[72:75], 0 offen offset:0 sc1\n"
    "v_mfma_f32_16x16x32_bf16 v[180:183], a[148:151], a[124:127], v[180:183]\n"
    "ds_read_b128 a[88:91], v12 offset:12288\n"
    "v_mfma_f32_16x16x32_bf16 v[176:179], a[144:147], a[124:127], v[176:179]\n"
    "ds_read_b128 a[92:95], v12 offset:14336\n"
    "v_mfma_f32_16x16x32_bf16 v[192:195], a[144:147], a[128:131], v[192:195]\n"
    "buffer_load_dwordx4 v9, s[60:63], s79 offen offset:3072 sc1 lds\n"
    "v_mfma_f32_16x16x32_bf16 v[196:199], a[148:151], a[128:131], v[196:199]\n"
    "v_mfma_f32_16x16x32_bf16 v[200:203], a[152:155], a[128:131], v[200:203]\n"
    "v_mfma_f32_16x16x32_bf16 v[204:207], a[156:159], a[128:131], v[204:207]\n"
    "v_mfma_f32_16x16x32_bf16 v[220:223], a[156:159], a[132:135], v[220:223]\n"
    "s_add_u32 m0, s92, 4096\n"
    "v_mfma_f32_16x16x32_bf16 v[216:219], a[152:155], a[132:135], v[216:219]\n"
    "buffer_load_dwordx4 v8, s[60:63], s80 offen offset:0 sc1 lds\n"
    "v_mfma_f32_16x16x32_bf16 v[212:215], a[148:151], a[132:135], v[212:215]\n"
    "v_mfma_f32_16x16x32_bf16 v[208:211], a[144:147], a[132:135], v[208:211]\n"
    "v_mfma_f32_16x16x32_bf16 v[224:227], a[144:147], a[136:139], v[224:227]\n"
    "v_mfma_f32_16x16x32_bf16 v[228:231], a[148:151], a[136:139], v[228:231]\n"
    "v_mfma_f32_16x16x32_bf16 v[232:235], a[152:155], a[136:139], v[232:235]\n"
    "buffer_load_dwordx4 v9, s[60:63], s81 offen offset:1024 sc1 lds\n"
    "v_mfma_f32_16x16x32_bf16 v[236:239], a[156:159], a[136:139], v[236:239]\n"
    "v_mfma_f32_16x16x32_bf16 v[252:255], a[156:159], a[140:143], v[252:255]\n"
    "v_mfma_f32_16x16x32_bf16 v[248:251], a[152:155], a[140:143], v[248:251]\n"
    "v_mfma_f32_16x16x32_bf16 v[244:247], a[148:151], a[140:143], v[244:247]\n"
    "v_mfma_f32_16x16x32_bf16 v[240:243], a[144:147], a[140:143], v[240:243]\n"
    "s_waitcnt lgkmcnt(0)\n"
    "buffer_load_dwordx4 v8, s[60:63], s82 offen offset:2048 sc1 lds\n"
    "v_mfma_f32_16x16x32_bf16 v[128:131], a[96:99], a[64:67], v[128:131]\n"
    "ds_read_b128 a[112:115], v13 offset:0\n"
    "v_mfma_f32_16x16x32_bf16 v[132:135], a[100:103], a[64:67], v[132:135]\n"
    "ds_read_b128 a[144:147], v14 offset:1024\n"
    "v_mfma_f32_16x16x32_bf16 v[136:139], a[104:107], a[64:67], v[136:139]\n"
    "v_mfma_f32_16x16x32_bf16 v[140:143], a[108:111], a[64:67], v[140:143]\n"
    "ds_read_b128 a[148:151], v14 offset:3072\n"
    "v_mfma_f32_16x16x32_bf16 v[156:159], a[108:111], a[68:71], v[156:159]\n"
    "ds_read_b128 a[152:155], v14 offset:5120\n"
    "buffer_load_dwordx4 v9, s[60:63], s83 offen offset:3072 sc1 lds\n"
    "v_mfma_f32_16x16x32_bf16 v[152:155], a[104:107], a[68:71], v[152:155]\n"
    "v_mfma_f32_16x16x32_bf16 v[148:151], a[100:103], a[68:71], v[148:151]\n"
    "ds_read_b128 a[156:159], v14 offset:7168\n"
    "buffer_store_dwordx4 v[52:55], v7, s[72:75], 0 offen offset:1024 sc1\n"
    "v_mfma_f32_16x16x32_bf16 v[144:147], a[96:99], a[68:71], v[144:147]\n"
    "ds_read_b128 a[116:119], v13 offset:2048\n"
    "v_mfma_f32_16x16x32_bf16 v[160:163], a[96:99], a[72:75], v[160:163]\n"
    "s_add_u32 m0, s98, 32768\n"
    "v_mfma_f32_16x16x32_bf16 v[164:167], a[100:103], a[72:75], v[164:167]\n"
    "ds_read_b128 a[120:123], v13 offset:4096\n"
    "buffer_load_dwordx4 v10, s[64:67], 0 offen offset:0 sc1 lds\n"
    "v_mfma_f32_16x16x32_bf16 v[168:171], a[104:107], a[72:75], v[168:171]\n"
    "ds_read_b128 a[124:127], v13 offset:6144\n"
    "v_mfma_f32_16x16x32_bf16 v[172:175], a[108:111], a[72:75], v[172:175]\n"
    "v_mfma_f32_16x16x32_bf16 v[188:191], a[108:111], a[76:79], v[188:191]\n"
    "ds_read_b128 a[128:131], v13 offset:8192\n"
    "v_mfma_f32_16x16x32_bf16 v[184:187], a[104:107], a[76:79], v[184:187]\n"
    "ds_read_b128 a[132:135], v13 offset:10240\n"
    "v_mfma_f32_16x16x32_bf16 v[180:183], a[100:103], a[76:79], v[180:183]\n"
    "buffer_load_dwordx4 v10, s[64:67], 0 offen offset:1024 sc1 lds\n"
    "v_mfma_f32_16x16x32_bf16 v[176:179], a[96:99], a[76:79], v[176:179]\n"
    "ds_read_b128 a[136:139], v13 offset:12288\n"
    "v_mfma_f32_16x16x32_bf16 v[192:195], a[96:99], a[80:83], v[192:195]\n"
    "ds_read_b128 a[140:143], v13 offset:14336\n"
    "v_mfma_f32_16x16x32_bf16 v[196:199], a[100:103], a[80:83], v[196:199]\n"
    "v_add_u32 v12, s96, v12\n"
    "v_mfma_f32_16x16x32_bf16 v[200:203], a[104:107], a[80:83], v[200:203]\n"
    "v_add_u32 v13, s96, v13\n"
    "v_mfma_f32_16x16x32_bf16 v[204:207], a[108:111], a[80:83], v[204:207]\n"
    "buffer_load_dwordx4 v10, s[64:67], 0 offen offset:2048 sc1 lds\n"
    "v_mfma_f32_16x16x32_bf16 v[220:223], a[108:111], a[84:87], v[220:223]\n"
    "v_add_u32 v14, s96, v14\n"
    "v_mfma_f32_16x16x32_bf16 v[216:219], a[104:107], a[84:87], v[216:219]\n"
    "v_mfma_f32_16x16x32_bf16 v[212:215], a[100:103], a[84:87], v[212:215]\n"
    "v_mfma_f32_16x16x32_bf16 v[208:211], a[96:99], a[84:87], v[208:211]\n"
    "v_mfma_f32_16x16x32_bf16 v[224:227], a[96:99], a[88:91], v[224:227]\n"
    "buffer_load_dwordx4 v10, s[64:67], 0 offen offset:3072 sc1 lds\n"
    "v_mfma_f32_16x16x32_bf16 v[228:231], a[100:103], a[88:91], v[228:231]\n"
    "v_mfma_f32_16x16x32_bf16 v[232:235], a[104:107], a[88:91], v[232:235]\n"
    "v_mfma_f32_16x16x32_bf16 v[236:239], a[108:111], a[88:91], v[236:239]\n"
    "v_mfma_f32_16x16x32_bf16 v[252:255], a[108:111], a[92:95], v[252:255]\n"
    "s_add_u32 s60, s60, 256\n"
    "s_addc_u32 s61, s61, 0\n"
    "v_mfma_f32_16x16x32_bf16 v[248:251], a[104:107], a[92:95], v[248:251]\n"
    "s_add_u32 s64, s64, 32768\n"
    "s_addc_u32 s65, s65, 0\n"
    "v_mfma_f32_16x16x32_bf16 v[244:247], a[100:103], a[92:95], v[244:247]\n"
    "v_mfma_f32_16x16x32_bf16 v[240:243], a[96:99], a[92:95], v[240:243]\n"
    "s_waitcnt lgkmcnt(0) vmcnt(14)\n"
    "s_barrier\n"
    "ds_read_b128 a[64:67], v12 offset:0\n"
    "v_mfma_f32_16x16x32_bf16 v[128:131], a[144:147], a[112:115], v[128:131]\n"
    "ds_read_b128 a[96:99], v14 offset:0\n"
    "s_add_u32 m0, s92, 49152\n"
    "v_mfma_f32_16x16x32_bf16 v[132:135], a[148:151], a[112:115], v[132:135]\n"
    "buffer_load_dwordx4 v8, s[60:63], s76 offen offset:0 sc1 lds\n"
    "v_mfma_f32_16x16x32_bf16 v[136:139], a[152:155], a[112:115], v[136:139]\n"
    "ds_read_b128 a[100:103], v14 offset:2048\n"
    "s_lshl_b32 s94, s57, 2\n"
    "s_add_u32 s94, s94, s56\n"
    "v_mfma_f32_16x16x32_bf16 v[140:143], a[156:159], a[112:115], v[140:143]\n"
    "ds_read_b128 a[104:107], v14 offset:4096\n"
    "s_mul_i32 s94, s94, 458752\n"
    "s_add_u32 s12, s52, s94\n"
    "s_addc_u32 s13, s53, 0\n"
    "v_mfma_f32_16x16x32_bf16 v[156:159], a[156:159], a[116:119], v[156:159]\n"
    "s_and_b32 s13, s13, 0xffff\n"
    "s_mov_b32 s14, 32768\n"
    "s_mov_b32 s15, 0x20000\n"
    "v_mfma_f32_16x16x32_bf16 v[152:155], a[152:155], a[116:119], v[152:155]\n"
    "ds_read_b128 a[108:111], v14 offset:6144\n"
    "s_lshl_b32 s84, s55, 13\n"
    "s_add_u32 s85, s84, 4096\n"
    "v_mfma_f32_16x16x32_bf16 v[148:151], a[148:151], a[116:119], v[148:151]\n"
    "ds_read_b128 a[68:71], v12 offset:2048\n"
    "buffer_load_dwordx4 v9, s[60:63], s77 offen offset:1024 sc1 lds\n"
    "v_mfma_f32_16x16x32_bf16 v[144:147], a[144:147], a[116:119], v[144:147]\n"
    "s_mov_b32 s86, 7168\n"
    "s_mov_b32 s87, 14336\n"
    "s_mov_b32 s88, 21504\n"
    "v_mfma_f32_16x16x32_bf16 v[160:163], a[144:147], a[120:123], v[160:163]\n"
    "ds_read_b128 a[72:75], v12 offset:4096\n"
    "v_mfma_f32_16x16x32_bf16 v[164:167], a[148:151], a[120:123], v[164:167]\n"
    "ds_read_b128 a[76:79], v12 offset:6144\n"
    "v_mfma_f32_16x16x32_bf16 v[168:171], a[152:155], a[120:123], v[168:171]\n"
    "v_mfma_f32_16x16x32_bf16 v[172:175], a[156:159], a[120:123], v[172:175]\n"
    "ds_read_b128 a[80:83], v12 offset:8192\n"
    "buffer_load_dwordx4 v8, s[60:63], s78 offen offset:2048 sc1 lds\n"
    "v_mfma_f32_16x16x32_bf16 v[188:191], a[156:159], a[124:127], v[188:191]\n"
    "ds_read_b128 a[84:87], v12 offset:10240\n"
    "v_mfma_f32_16x16x32_bf16 v[184:187], a[152:155], a[124:127], v[184:187]\n"
    "buffer_store_dwordx4 v[56:59], v7, s[72:75], 0 offen offset:2048 sc1\n"
    "v_mfma_f32_16x16x32_bf16 v[180:183], a[148:151], a[124:127], v[180:183]\n"
    "ds_read_b128 a[88:91], v12 offset:12288\n"
    "v_mfma_f32_16x16x32_bf16 v[176:179], a[144:147], a[124:127], v[176:179]\n"
    "ds_read_b128 a[92:95], v12 offset:14336\n"
    "v_mfma_f32_16x16x32_bf16 v[192:195], a[144:147], a[128:131], v[192:195]\n"
    "buffer_load_dwordx4 v9, s[60:63], s79 offen offset:3072 sc1 lds\n"
    "v_mfma_f32_16x16x32_bf16 v[196:199], a[148:151], a[128:131], v[196:199]\n"
    "v_mfma_f32_16x16x32_bf16 v[200:203], a[152:155], a[128:131], v[200:203]\n"
    "v_mfma_f32_16x16x32_bf16 v[204:207], a[156:159], a[128:131], v[204:207]\n"
    "v_mfma_f32_16x16x32_bf16 v[220:223], a[156:159], a[132:135], v[220:223]\n"
    "s_add_u32 m0, s92, 53248\n"
    "v_mfma_f32_16x16x32_bf16 v[216:219], a[152:155], a[132:135], v[216:219]\n"
    "buffer_load_dwordx4 v8, s[60:63], s80 offen offset:0 sc1 lds\n"
    "v_mfma_f32_16x16x32_bf16 v[212:215], a[148:151], a[132:135], v[212:215]\n"
    "v_mfma_f32_16x16x32_bf16 v[208:211], a[144:147], a[132:135], v[208:211]\n"
    "v_mfma_f32_16x16x32_bf16 v[224:227], a[144:147], a[136:139], v[224:227]\n"
    "v_mfma_f32_16x16x32_bf16 v[228:231], a[148:151], a[136:139], v[228:231]\n"
    "v_mfma_f32_16x16x32_bf16 v[232:235], a[152:155], a[136:139], v[232:235]\n"
    "buffer_load_dwordx4 v9, s[60:63], s81 offen offset:1024 sc1 lds\n"
    "v_mfma_f32_16x16x32_bf16 v[236:239], a[156:159], a[136:139], v[236:239]\n"
    "v_mfma_f32_16x16x32_bf16 v[252:255], a[156:159], a[140:143], v[252:255]\n"
    "v_mfma_f32_16x16x32_bf16 v[248:251], a[152:155], a[140:143], v[248:251]\n"
    "v_mfma_f32_16x16x32_bf16 v[244:247], a[148:151], a[140:143], v[244:247]\n"
    "v_mfma_f32_16x16x32_bf16 v[240:243], a[144:147], a[140:143], v[240:243]\n"
    "s_waitcnt lgkmcnt(0)\n"
    "buffer_load_dwordx4 v8, s[60:63], s82 offen offset:2048 sc1 lds\n"
    "v_mfma_f32_16x16x32_bf16 v[128:131], a[96:99], a[64:67], v[128:131]\n"
    "ds_read_b128 a[112:115], v13 offset:0\n"
    "v_mfma_f32_16x16x32_bf16 v[132:135], a[100:103], a[64:67], v[132:135]\n"
    "ds_read_b128 a[144:147], v14 offset:1024\n"
    "v_mfma_f32_16x16x32_bf16 v[136:139], a[104:107], a[64:67], v[136:139]\n"
    "v_mfma_f32_16x16x32_bf16 v[140:143], a[108:111], a[64:67], v[140:143]\n"
    "ds_read_b128 a[148:151], v14 offset:3072\n"
    "v_mfma_f32_16x16x32_bf16 v[156:159], a[108:111], a[68:71], v[156:159]\n"
    "ds_read_b128 a[152:155], v14 offset:5120\n"
    "buffer_load_dwordx4 v9, s[60:63], s83 offen offset:3072 sc1 lds\n"
    "v_mfma_f32_16x16x32_bf16 v[152:155], a[104:107], a[68:71], v[152:155]\n"
    "v_mfma_f32_16x16x32_bf16 v[148:151], a[100:103], a[68:71], v[148:151]\n"
    "ds_read_b128 a[156:159], v14 offset:7168\n"
    "buffer_store_dwordx4 v[60:63], v7, s[72:75], 0 offen offset:3072 sc1\n"
    "v_add_u32 v7, 4096, v7\n"
    "v_mfma_f32_16x16x32_bf16 v[144:147], a[96:99], a[68:71], v[144:147]\n"
    "ds_read_b128 a[116:119], v13 offset:2048\n"
    "v_mfma_f32_16x16x32_bf16 v[160:163], a[96:99], a[72:75], v[160:163]\n"
    "s_add_u32 m0, s98, 81920\n"
    "v_mfma_f32_16x16x32_bf16 v[164:167], a[100:103], a[72:75], v[164:167]\n"
    "ds_read_b128 a[120:123], v13 offset:4096\n"
    "buffer_load_dwordx4 v10, s[64:67], 0 offen offset:0 sc1 lds\n"
    "v_mfma_f32_16x16x32_bf16 v[168:171], a[104:107], a[72:75], v[168:171]\n"
    "ds_read_b128 a[124:127], v13 offset:6144\n"
    "v_mfma_f32_16x16x32_bf16 v[172:175], a[108:111], a[72:75], v[172:175]\n"
    "v_mfma_f32_16x16x32_bf16 v[188:191], a[108:111], a[76:79], v[188:191]\n"
    "ds_read_b128 a[128:131], v13 offset:8192\n"
    "v_mfma_f32_16x16x32_bf16 v[184:187], a[104:107], a[76:79], v[184:187]\n"
    "ds_read_b128 a[132:135], v13 offset:10240\n"
    "v_mfma_f32_16x16x32_bf16 v[180:183], a[100:103], a[76:79], v[180:183]\n"
    "buffer_load_dwordx4 v10, s[64:67], 0 offen offset:1024 sc1 lds\n"
    "v_mfma_f32_16x16x32_bf16 v[176:179], a[96:99], a[76:79], v[176:179]\n"
    "ds_read_b128 a[136:139], v13 offset:12288\n"
    "v_mfma_f32_16x16x32_bf16 v[192:195], a[96:99], a[80:83], v[192:195]\n"
    "ds_read_b128 a[140:143], v13 offset:14336\n"
    "v_mfma_f32_16x16x32_bf16 v[196:199], a[100:103], a[80:83], v[196:199]\n"
    "v_add_u32 v12, s97, v12\n"
    "v_mfma_f32_16x16x32_bf16 v[200:203], a[104:107], a[80:83], v[200:203]\n"
    "v_add_u32 v13, s97, v13\n"
    "v_mfma_f32_16x16x32_bf16 v[204:207], a[108:111], a[80:83], v[204:207]\n"
    "buffer_load_dwordx4 v10, s[64:67], 0 offen offset:2048 sc1 lds\n"
    "v_mfma_f32_16x16x32_bf16 v[220:223], a[108:111], a[84:87], v[220:223]\n"
    "v_add_u32 v14, s97, v14\n"
    "v_mfma_f32_16x16x32_bf16 v[216:219], a[104:107], a[84:87], v[216:219]\n"
    "v_mfma_f32_16x16x32_bf16 v[212:215], a[100:103], a[84:87], v[212:215]\n"
    "v_mfma_f32_16x16x32_bf16 v[208:211], a[96:99], a[84:87], v[208:211]\n"
    "v_mfma_f32_16x16x32_bf16 v[224:227], a[96:99], a[88:91], v[224:227]\n"
    "buffer_load_dwordx4 v10, s[64:67], 0 offen offset:3072 sc1 lds\n"
    "v_mfma_f32_16x16x32_bf16 v[228:231], a[100:103], a[88:91], v[228:231]\n"
    "v_mfma_f32_16x16x32_bf16 v[232:235], a[104:107], a[88:91], v[232:235]\n"
    "v_mfma_f32_16x16x32_bf16 v[236:239], a[108:111], a[88:91], v[236:239]\n"
    "v_mfma_f32_16x16x32_bf16 v[252:255], a[108:111], a[92:95], v[252:255]\n"
    "s_add_u32 s60, s60, 256\n"
    "s_addc_u32 s61, s61, 0\n"
    "v_mfma_f32_16x16x32_bf16 v[248:251], a[104:107], a[92:95], v[248:251]\n"
    "s_add_u32 s64, s64, 32768\n"
    "s_addc_u32 s65, s65, 0\n"
    "v_mfma_f32_16x16x32_bf16 v[244:247], a[100:103], a[92:95], v[244:247]\n"
    "v_mfma_f32_16x16x32_bf16 v[240:243], a[96:99], a[92:95], v[240:243]\n"
    "s_waitcnt lgkmcnt(0) vmcnt(14)\n"
    "s_barrier\n"
    "ds_read_b128 a[64:67], v12 offset:0\n"
    "v_mfma_f32_16x16x32_bf16 v[128:131], a[144:147], a[112:115], v[128:131]\n"
    "TC6:\n"
    "s_cbranch_execz TC10\n"
    "ds_read_b128 a[96:99], v14 offset:0\n"
    "s_add_u32 m0, s92, 98304\n"
    "v_mfma_f32_16x16x32_bf16 v[132:135], a[148:151], a[112:115], v[132:135]\n"
    "buffer_load_dwordx4 v8, s[60:63], s76 offen offset:0 sc1 lds\n"
    "v_mfma_f32_16x16x32_bf16 v[136:139], a[152:155], a[112:115], v[136:139]\n"
    "ds_read_b128 a[100:103], v14 offset:2048\n"
    "v_mfma_f32_16x16x32_bf16 v[140:143], a[156:159], a[112:115], v[140:143]\n"
    "ds_read_b128 a[104:107], v14 offset:4096\n"
    "v_mfma_f32_16x16x32_bf16 v[156:159], a[156:159], a[116:119], v[156:159]\n"
    "v_mfma_f32_16x16x32_bf16 v[152:155], a[152:155], a[116:119], v[152:155]\n"
    "ds_read_b128 a[108:111], v14 offset:6144\n"
    "v_mfma_f32_16x16x32_bf16 v[148:151], a[148:151], a[116:119], v[148:151]\n"
    "ds_read_b128 a[68:71], v12 offset:2048\n"
    "buffer_load_dwordx4 v9, s[60:63], s77 offen offset:1024 sc1 lds\n"
    "v_mfma_f32_16x16x32_bf16 v[144:147], a[144:147], a[116:119], v[144:147]\n"
    "v_mfma_f32_16x16x32_bf16 v[160:163], a[144:147], a[120:123], v[160:163]\n"
    "ds_read_b128 a[72:75], v12 offset:4096\n"
    "v_mfma_f32_16x16x32_bf16 v[164:167], a[148:151], a[120:123], v[164:167]\n"
    "ds_read_b128 a[76:79], v12 offset:6144\n"
    "v_mfma_f32_16x16x32_bf16 v[168:171], a[152:155], a[120:123], v[168:171]\n"
    "v_mfma_f32_16x16x32_bf16 v[172:175], a[156:159], a[120:123], v[172:175]\n"
    "ds_read_b128 a[80:83], v12 offset:8192\n"
    "buffer_load_dwordx4 v8, s[60:63], s78 offen offset:2048 sc1 lds\n"
    "v_mfma_f32_16x16x32_bf16 v[188:191], a[156:159], a[124:127], v[188:191]\n"
    "ds_read_b128 a[84:87], v12 offset:10240\n"
    "v_mfma_f32_16x16x32_bf16 v[184:187], a[152:155], a[124:127], v[184:187]\n"
    "buffer_store_dwordx4 v[64:67], v7, s[72:75], 0 offen offset:0 sc1\n"
    "v_mfma_f32_16x16x32_bf16 v[180:183], a[148:151], a[124:127], v[180:183]\n"
    "ds_read_b128 a[88:91], v12 offset:12288\n"
    "v_mfma_f32_16x16x32_bf16 v[176:179], a[144:147], a[124:127], v[176:179]\n"
    "ds_read_b128 a[92:95], v12 offset:14336\n"
    "v_mfma_f32_16x16x32_bf16 v[192:195], a[144:147], a[128:131], v[192:195]\n"
    "buffer_load_dwordx4 v9, s[60:63], s79 offen offset:3072 sc1 lds\n"
    "v_mfma_f32_16x16x32_bf16 v[196:199], a[148:151], a[128:131], v[196:199]\n"
    "v_mfma_f32_16x16x32_bf16 v[200:203], a[152:155], a[128:131], v[200:203]\n"
    "v_mfma_f32_16x16x32_bf16 v[204:207], a[156:159], a[128:131], v[204:207]\n"
    "v_mfma_f32_16x16x32_bf16 v[220:223], a[156:159], a[132:135], v[220:223]\n"
    "s_add_u32 m0, s92, 102400\n"
    "v_mfma_f32_16x16x32_bf16 v[216:219], a[152:155], a[132:135], v[216:219]\n"
    "buffer_load_dwordx4 v8, s[60:63], s80 offen offset:0 sc1 lds\n"
    "v_mfma_f32_16x16x32_bf16 v[212:215], a[148:151], a[132:135], v[212:215]\n"
    "v_mfma_f32_16x16x32_bf16 v[208:211], a[144:147], a[132:135], v[208:211]\n"
    "v_mfma_f32_16x16x32_bf16 v[224:227], a[144:147], a[136:139], v[224:227]\n"
    "v_mfma_f32_16x16x32_bf16 v[228:231], a[148:151], a[136:139], v[228:231]\n"
    "v_mfma_f32_16x16x32_bf16 v[232:235], a[152:155], a[136:139], v[232:235]\n"
    "buffer_load_dwordx4 v9, s[60:63], s81 offen offset:1024 sc1 lds\n"
    "v_mfma_f32_16x16x32_bf16 v[236:239], a[156:159], a[136:139], v[236:239]\n"
    "v_mfma_f32_16x16x32_bf16 v[252:255], a[156:159], a[140:143], v[252:255]\n"
    "v_mfma_f32_16x16x32_bf16 v[248:251], a[152:155], a[140:143], v[248:251]\n"
    "v_mfma_f32_16x16x32_bf16 v[244:247], a[148:151], a[140:143], v[244:247]\n"
    "v_mfma_f32_16x16x32_bf16 v[240:243], a[144:147], a[140:143], v[240:243]\n"
    "s_waitcnt lgkmcnt(0)\n"
    "buffer_load_dwordx4 v8, s[60:63], s82 offen offset:2048 sc1 lds\n"
    "v_mfma_f32_16x16x32_bf16 v[128:131], a[96:99], a[64:67], v[128:131]\n"
    "ds_read_b128 a[112:115], v13 offset:0\n"
    "v_mfma_f32_16x16x32_bf16 v[132:135], a[100:103], a[64:67], v[132:135]\n"
    "ds_read_b128 a[144:147], v14 offset:1024\n"
    "v_mfma_f32_16x16x32_bf16 v[136:139], a[104:107], a[64:67], v[136:139]\n"
    "v_mfma_f32_16x16x32_bf16 v[140:143], a[108:111], a[64:67], v[140:143]\n"
    "ds_read_b128 a[148:151], v14 offset:3072\n"
    "v_mfma_f32_16x16x32_bf16 v[156:159], a[108:111], a[68:71], v[156:159]\n"
    "ds_read_b128 a[152:155], v14 offset:5120\n"
    "buffer_load_dwordx4 v9, s[60:63], s83 offen offset:3072 sc1 lds\n"
    "v_mfma_f32_16x16x32_bf16 v[152:155], a[104:107], a[68:71], v[152:155]\n"
    "v_mfma_f32_16x16x32_bf16 v[148:151], a[100:103], a[68:71], v[148:151]\n"
    "ds_read_b128 a[156:159], v14 offset:7168\n"
    "buffer_store_dwordx4 v[68:71], v7, s[72:75], 0 offen offset:1024 sc1\n"
    "v_mfma_f32_16x16x32_bf16 v[144:147], a[96:99], a[68:71], v[144:147]\n"
    "ds_read_b128 a[116:119], v13 offset:2048\n"
    "v_mfma_f32_16x16x32_bf16 v[160:163], a[96:99], a[72:75], v[160:163]\n"
    "s_add_u32 m0, s98, 131072\n"
    "v_mfma_f32_16x16x32_bf16 v[164:167], a[100:103], a[72:75], v[164:167]\n"
    "ds_read_b128 a[120:123], v13 offset:4096\n"
    "buffer_load_dwordx4 v10, s[64:67], 0 offen offset:0 sc1 lds\n"
    "v_mfma_f32_16x16x32_bf16 v[168:171], a[104:107], a[72:75], v[168:171]\n"
    "ds_read_b128 a[124:127], v13 offset:6144\n"
    "v_mfma_f32_16x16x32_bf16 v[172:175], a[108:111], a[72:75], v[172:175]\n"
    "v_mfma_f32_16x16x32_bf16 v[188:191], a[108:111], a[76:79], v[188:191]\n"
    "ds_read_b128 a[128:131], v13 offset:8192\n"
    "v_mfma_f32_16x16x32_bf16 v[184:187], a[104:107], a[76:79], v[184:187]\n"
    "ds_read_b128 a[132:135], v13 offset:10240\n"
    "v_mfma_f32_16x16x32_bf16 v[180:183], a[100:103], a[76:79], v[180:183]\n"
    "buffer_load_dwordx4 v10, s[64:67], 0 offen offset:1024 sc1 lds\n"
    "v_mfma_f32_16x16x32_bf16 v[176:179], a[96:99], a[76:79], v[176:179]\n"
    "ds_read_b128 a[136:139], v13 offset:12288\n"
    "v_mfma_f32_16x16x32_bf16 v[192:195], a[96:99], a[80:83], v[192:195]\n"
    "ds_read_b128 a[140:143], v13 offset:14336\n"
    "v_mfma_f32_16x16x32_bf16 v[196:199], a[100:103], a[80:83], v[196:199]\n"
    "v_add_u32 v12, s96, v12\n"
    "v_mfma_f32_16x16x32_bf16 v[200:203], a[104:107], a[80:83], v[200:203]\n"
    "v_add_u32 v13, s96, v13\n"
    "v_mfma_f32_16x16x32_bf16 v[204:207], a[108:111], a[80:83], v[204:207]\n"
    "buffer_load_dwordx4 v10, s[64:67], 0 offen offset:2048 sc1 lds\n"
    "v_mfma_f32_16x16x32_bf16 v[220:223], a[108:111], a[84:87], v[220:223]\n"
    "v_add_u32 v14, s96, v14\n"
    "v_mfma_f32_16x16x32_bf16 v[216:219], a[104:107], a[84:87], v[216:219]\n"
    "global_load_dwordx2 v[100:101], v20, s[52:53] sc1\n"
    "v_mfma_f32_16x16x32_bf16 v[212:215], a[100:103], a[84:87], v[212:215]\n"
    "v_mfma_f32_16x16x32_bf16 v[208:211], a[96:99], a[84:87], v[208:211]\n"
    "v_mfma_f32_16x16x32_bf16 v[224:227], a[96:99], a[88:91], v[224:227]\n"
    "buffer_load_dwordx4 v10, s[64:67], 0 offen offset:3072 sc1 lds\n"
    "v_mfma_f32_16x16x32_bf16 v[228:231], a[100:103], a[88:91], v[228:231]\n"
    "v_mfma_f32_16x16x32_bf16 v[232:235], a[104:107], a[88:91], v[232:235]\n"
    "v_mfma_f32_16x16x32_bf16 v[236:239], a[108:111], a[88:91], v[236:239]\n"
    "v_mfma_f32_16x16x32_bf16 v[252:255], a[108:111], a[92:95], v[252:255]\n"
    "s_add_u32 s60, s60, 256\n"
    "s_addc_u32 s61, s61, 0\n"
    "v_mfma_f32_16x16x32_bf16 v[248:251], a[104:107], a[92:95], v[248:251]\n"
    "s_add_u32 s64, s64, 32768\n"
    "s_addc_u32 s65, s65, 0\n"
    "v_mfma_f32_16x16x32_bf16 v[244:247], a[100:103], a[92:95], v[244:247]\n"
    "v_mfma_f32_16x16x32_bf16 v[240:243], a[96:99], a[92:95], v[240:243]\n"
    "s_waitcnt lgkmcnt(0) vmcnt(15)\n"
    "s_barrier\n"
    "ds_read_b128 a[64:67], v12 offset:0\n"
    "v_mfma_f32_16x16x32_bf16 v[128:131], a[144:147], a[112:115], v[128:131]\n"
    "ds_read_b128 a[96:99], v14 offset:0\n"
    "s_add_u32 m0, s92, 0\n"
    "v_mfma_f32_16x16x32_bf16 v[132:135], a[148:151], a[112:115], v[132:135]\n"
    "buffer_load_dwordx4 v8, s[60:63], s76 offen offset:0 sc1 lds\n"
    "v_mfma_f32_16x16x32_bf16 v[136:139], a[152:155], a[112:115], v[136:139]\n"
    "ds_read_b128 a[100:103], v14 offset:2048\n"
    "v_mfma_f32_16x16x32_bf16 v[140:143], a[156:159], a[112:115], v[140:143]\n"
    "ds_read_b128 a[104:107], v14 offset:4096\n"
    "v_mfma_f32_16x16x32_bf16 v[156:159], a[156:159], a[116:119], v[156:159]\n"
    "v_mfma_f32_16x16x32_bf16 v[152:155], a[152:155], a[116:119], v[152:155]\n"
    "ds_read_b128 a[108:111], v14 offset:6144\n"
    "v_mfma_f32_16x16x32_bf16 v[148:151], a[148:151], a[116:119], v[148:151]\n"
    "ds_read_b128 a[68:71], v12 offset:2048\n"
    "buffer_load_dwordx4 v9, s[60:63], s77 offen offset:1024 sc1 lds\n"
    "v_mfma_f32_16x16x32_bf16 v[144:147], a[144:147], a[116:119], v[144:147]\n"
    "v_mfma_f32_16x16x32_bf16 v[160:163], a[144:147], a[120:123], v[160:163]\n"
    "ds_read_b128 a[72:75], v12 offset:4096\n"
    "v_mfma_f32_16x16x32_bf16 v[164:167], a[148:151], a[120:123], v[164:167]\n"
    "ds_read_b128 a[76:79], v12 offset:6144\n"
    "v_mfma_f32_16x16x32_bf16 v[168:171], a[152:155], a[120:123], v[168:171]\n"
    "v_mfma_f32_16x16x32_bf16 v[172:175], a[156:159], a[120:123], v[172:175]\n"
    "ds_read_b128 a[80:83], v12 offset:8192\n"
    "buffer_load_dwordx4 v8, s[60:63], s78 offen offset:2048 sc1 lds\n"
    "v_mfma_f32_16x16x32_bf16 v[188:191], a[156:159], a[124:127], v[188:191]\n"
    "ds_read_b128 a[84:87], v12 offset:10240\n"
    "v_mfma_f32_16x16x32_bf16 v[184:187], a[152:155], a[124:127], v[184:187]\n"
    "buffer_store_dwordx4 v[72:75], v7, s[72:75], 0 offen offset:2048 sc1\n"
    "v_mfma_f32_16x16x32_bf16 v[180:183], a[148:151], a[124:127], v[180:183]\n"
    "ds_read_b128 a[88:91], v12 offset:12288\n"
    "v_mfma_f32_16x16x32_bf16 v[176:179], a[144:147], a[124:127], v[176:179]\n"
    "ds_read_b128 a[92:95], v12 offset:14336\n"
    "v_mfma_f32_16x16x32_bf16 v[192:195], a[144:147], a[128:131], v[192:195]\n"
    "buffer_load_dwordx4 v9, s[60:63], s79 offen offset:3072 sc1 lds\n"
    "v_mfma_f32_16x16x32_bf16 v[196:199], a[148:151], a[128:131], v[196:199]\n"
    "v_mfma_f32_16x16x32_bf16 v[200:203], a[152:155], a[128:131], v[200:203]\n"
    "v_mfma_f32_16x16x32_bf16 v[204:207], a[156:159], a[128:131], v[204:207]\n"
    "v_mfma_f32_16x16x32_bf16 v[220:223], a[156:159], a[132:135], v[220:223]\n"
    "s_add_u32 m0, s92, 4096\n"
    "v_mfma_f32_16x16x32_bf16 v[216:219], a[152:155], a[132:135], v[216:219]\n"
    "buffer_load_dwordx4 v8, s[60:63], s80 offen offset:0 sc1 lds\n"
    "v_mfma_f32_16x16x32_bf16 v[212:215], a[148:151], a[132:135], v[212:215]\n"
    "v_mfma_f32_16x16x32_bf16 v[208:211], a[144:147], a[132:135], v[208:211]\n"
    "v_mfma_f32_16x16x32_bf16 v[224:227], a[144:147], a[136:139], v[224:227]\n"
    "v_mfma_f32_16x16x32_bf16 v[228:231], a[148:151], a[136:139], v[228:231]\n"
    "v_mfma_f32_16x16x32_bf16 v[232:235], a[152:155], a[136:139], v[232:235]\n"
    "buffer_load_dwordx4 v9, s[60:63], s81 offen offset:1024 sc1 lds\n"
    "v_mfma_f32_16x16x32_bf16 v[236:239], a[156:159], a[136:139], v[236:239]\n"
    "v_mfma_f32_16x16x32_bf16 v[252:255], a[156:159], a[140:143], v[252:255]\n"
    "v_mfma_f32_16x16x32_bf16 v[248:251], a[152:155], a[140:143], v[248:251]\n"
    "v_mfma_f32_16x16x32_bf16 v[244:247], a[148:151], a[140:143], v[244:247]\n"
    "v_mfma_f32_16x16x32_bf16 v[240:243], a[144:147], a[140:143], v[240:243]\n"
    "s_waitcnt lgkmcnt(0)\n"
    "buffer_load_dwordx4 v8, s[60:63], s82 offen offset:2048 sc1 lds\n"
    "v_mfma_f32_16x16x32_bf16 v[128:131], a[96:99], a[64:67], v[128:131]\n"
    "ds_read_b128 a[112:115], v13 offset:0\n"
    "v_mfma_f32_16x16x32_bf16 v[132:135], a[100:103], a[64:67], v[132:135]\n"
    "ds_read_b128 a[144:147], v14 offset:1024\n"
    "v_mfma_f32_16x16x32_bf16 v[136:139], a[104:107], a[64:67], v[136:139]\n"
    "v_mfma_f32_16x16x32_bf16 v[140:143], a[108:111], a[64:67], v[140:143]\n"
    "ds_read_b128 a[148:151], v14 offset:3072\n"
    "v_mfma_f32_16x16x32_bf16 v[156:159], a[108:111], a[68:71], v[156:159]\n"
    "ds_read_b128 a[152:155], v14 offset:5120\n"
    "buffer_load_dwordx4 v9, s[60:63], s83 offen offset:3072 sc1 lds\n"
    "v_mfma_f32_16x16x32_bf16 v[152:155], a[104:107], a[68:71], v[152:155]\n"
    "v_mfma_f32_16x16x32_bf16 v[148:151], a[100:103], a[68:71], v[148:151]\n"
    "ds_read_b128 a[156:159], v14 offset:7168\n"
    "buffer_store_dwordx4 v[76:79], v7, s[72:75], 0 offen offset:3072 sc1\n"
    "v_add_u32 v7, 4096, v7\n"
    "v_mfma_f32_16x16x32_bf16 v[144:147], a[96:99], a[68:71], v[144:147]\n"
    "ds_read_b128 a[116:119], v13 offset:2048\n"
    "v_mfma_f32_16x16x32_bf16 v[160:163], a[96:99], a[72:75], v[160:163]\n"
    "s_add_u32 m0, s98, 32768\n"
    "v_mfma_f32_16x16x32_bf16 v[164:167], a[100:103], a[72:75], v[164:167]\n"
    "ds_read_b128 a[120:123], v13 offset:4096\n"
    "buffer_load_dwordx4 v10, s[64:67], 0 offen offset:0 sc1 lds\n"
    "v_mfma_f32_16x16x32_bf16 v[168:171], a[104:107], a[72:75], v[168:171]\n"
    "ds_read_b128 a[124:127], v13 offset:6144\n"
    "v_mfma_f32_16x16x32_bf16 v[172:175], a[108:111], a[72:75], v[172:175]\n"
    "v_mfma_f32_16x16x32_bf16 v[188:191], a[108:111], a[76:79], v[188:191]\n"
    "ds_read_b128 a[128:131], v13 offset:8192\n"
    "v_mfma_f32_16x16x32_bf16 v[184:187], a[104:107], a[76:79], v[184:187]\n"
    "ds_read_b128 a[132:135], v13 offset:10240\n"
    "v_mfma_f32_16x16x32_bf16 v[180:183], a[100:103], a[76:79], v[180:183]\n"
    "buffer_load_dwordx4 v10, s[64:67], 0 offen offset:1024 sc1 lds\n"
    "v_mfma_f32_16x16x32_bf16 v[176:179], a[96:99], a[76:79], v[176:179]\n"
    "ds_read_b128 a[136:139], v13 offset:12288\n"
    "v_mfma_f32_16x16x32_bf16 v[192:195], a[96:99], a[80:83], v[192:195]\n"
    "ds_read_b128 a[140:143], v13 offset:14336\n"
    "v_mfma_f32_16x16x32_bf16 v[196:199], a[100:103], a[80:83], v[196:199]\n"
    "v_add_u32 v12, s96, v12\n"
    "v_mfma_f32_16x16x32_bf16 v[200:203], a[104:107], a[80:83], v[200:203]\n"
    "v_add_u32 v13, s96, v13\n"
    "v_mfma_f32_16x16x32_bf16 v[204:207], a[108:111], a[80:83], v[204:207]\n"
    "buffer_load_dwordx4 v10, s[64:67], 0 offen offset:2048 sc1 lds\n"
    "v_mfma_f32_16x16x32_bf16 v[220:223], a[108:111], a[84:87], v[220:223]\n"
    "v_add_u32 v14, s96, v14\n"
    "v_mfma_f32_16x16x32_bf16 v[216:219], a[104:107], a[84:87], v[216:219]\n"
    "v_mfma_f32_16x16x32_bf16 v[212:215], a[100:103], a[84:87], v[212:215]\n"
    "v_mfma_f32_16x16x32_bf16 v[208:211], a[96:99], a[84:87], v[208:211]\n"
    "v_mfma_f32_16x16x32_bf16 v[224:227], a[96:99], a[88:91], v[224:227]\n"
    "buffer_load_dwordx4 v10, s[64:67], 0 offen offset:3072 sc1 lds\n"
    "v_mfma_f32_16x16x32_bf16 v[228:231], a[100:103], a[88:91], v[228:231]\n"
    "v_mfma_f32_16x16x32_bf16 v[232:235], a[104:107], a[88:91], v[232:235]\n"
    "v_mfma_f32_16x16x32_bf16 v[236:239], a[108:111], a[88:91], v[236:239]\n"
    "v_mfma_f32_16x16x32_bf16 v[252:255], a[108:111], a[92:95], v[252:255]\n"
    "s_add_u32 s60, s60, 256\n"
    "s_addc_u32 s61, s61, 0\n"
    "v_mfma_f32_16x16x32_bf16 v[248:251], a[104:107], a[92:95], v[248:251]\n"
    "s_add_u32 s64, s64, 32768\n"
    "s_addc_u32 s65, s65, 0\n"
    "v_mfma_f32_16x16x32_bf16 v[244:247], a[100:103], a[92:95], v[244:247]\n"
    "v_mfma_f32_16x16x32_bf16 v[240:243], a[96:99], a[92:95], v[240:243]\n"
    "s_waitcnt lgkmcnt(0) vmcnt(14)\n"
    "s_barrier\n"
    "ds_read_b128 a[64:67], v12 offset:0\n"
    "v_mfma_f32_16x16x32_bf16 v[128:131], a[144:147], a[112:115], v[128:131]\n"
    "ds_read_b128 a[96:99], v14 offset:0\n"
    "s_add_u32 m0, s92, 49152\n"
    "v_mfma_f32_16x16x32_bf16 v[132:135], a[148:151], a[112:115], v[132:135]\n"
    "buffer_load_dwordx4 v8, s[60:63], s76 offen offset:0 sc1 lds\n"
    "v_mfma_f32_16x16x32_bf16 v[136:139], a[152:155], a[112:115], v[136:139]\n"
    "ds_read_b128 a[100:103], v14 offset:2048\n"
    "v_mfma_f32_16x16x32_bf16 v[140:143], a[156:159], a[112:115], v[140:143]\n"
    "ds_read_b128 a[104:107], v14 offset:4096\n"
    "v_mfma_f32_16x16x32_bf16 v[156:159], a[156:159], a[116:119], v[156:159]\n"
    "v_mfma_f32_16x16x32_bf16 v[152:155], a[152:155], a[116:119], v[152:155]\n"
    "ds_read_b128 a[108:111], v14 offset:6144\n"
    "v_mfma_f32_16x16x32_bf16 v[148:151], a[148:151], a[116:119], v[148:151]\n"
    "ds_read_b128 a[68:71], v12 offset:2048\n"
    "buffer_load_dwordx4 v9, s[60:63], s77 offen offset:1024 sc1 lds\n"
    "v_mfma_f32_16x16x32_bf16 v[144:147], a[144:147], a[116:119], v[144:147]\n"
    "v_mfma_f32_16x16x32_bf16 v[160:163], a[144:147], a[120:123], v[160:163]\n"
    "ds_read_b128 a[72:75], v12 offset:4096\n"
    "v_mfma_f32_16x16x32_bf16 v[164:167], a[148:151], a[120:123], v[164:167]\n"
    "ds_read_b128 a[76:79], v12 offset:6144\n"
    "v_mfma_f32_16x16x32_bf16 v[168:171], a[152:155], a[120:123], v[168:171]\n"
    "v_mfma_f32_16x16x32_bf16 v[172:175], a[156:159], a[120:123], v[172:175]\n"
    "ds_read_b128 a[80:83], v12 offset:8192\n"
    "buffer_load_dwordx4 v8, s[60:63], s78 offen offset:2048 sc1 lds\n"
    "v_mfma_f32_16x16x32_bf16 v[188:191], a[156:159], a[124:127], v[188:191]\n"
    "ds_read_b128 a[84:87], v12 offset:10240\n"
    "v_mfma_f32_16x16x32_bf16 v[184:187], a[152:155], a[124:127], v[184:187]\n"
    "buffer_store_dwordx4 v[80:83], v7, s[72:75], 0 offen offset:0 sc1\n"
    "v_mfma_f32_16x16x32_bf16 v[180:183], a[148:151], a[124:127], v[180:183]\n"
    "ds_read_b128 a[88:91], v12 offset:12288\n"
    "v_mfma_f32_16x16x32_bf16 v[176:179], a[144:147], a[124:127], v[176:179]\n"
    "ds_read_b128 a[92:95], v12 offset:14336\n"
    "v_mfma_f32_16x16x32_bf16 v[192:195], a[144:147], a[128:131], v[192:195]\n"
    "buffer_load_dwordx4 v9, s[60:63], s79 offen offset:3072 sc1 lds\n"
    "v_mfma_f32_16x16x32_bf16 v[196:199], a[148:151], a[128:131], v[196:199]\n"
    "v_mfma_f32_16x16x32_bf16 v[200:203], a[152:155], a[128:131], v[200:203]\n"
    "v_mfma_f32_16x16x32_bf16 v[204:207], a[156:159], a[128:131], v[204:207]\n"
    "v_mfma_f32_16x16x32_bf16 v[220:223], a[156:159], a[132:135], v[220:223]\n"
    "s_add_u32 m0, s92, 53248\n"
    "v_mfma_f32_16x16x32_bf16 v[216:219], a[152:155], a[132:135], v[216:219]\n"
    "buffer_load_dwordx4 v8, s[60:63], s80 offen offset:0 sc1 lds\n"
    "v_mfma_f32_16x16x32_bf16 v[212:215], a[148:151], a[132:135], v[212:215]\n"
    "v_mfma_f32_16x16x32_bf16 v[208:211], a[144:147], a[132:135], v[208:211]\n"
    "v_mfma_f32_16x16x32_bf16 v[224:227], a[144:147], a[136:139], v[224:227]\n"
    "v_mfma_f32_16x16x32_bf16 v[228:231], a[148:151], a[136:139], v[228:231]\n"
    "v_mfma_f32_16x16x32_bf16 v[232:235], a[152:155], a[136:139], v[232:235]\n"
    "buffer_load_dwordx4 v9, s[60:63], s81 offen offset:1024 sc1 lds\n"
    "v_mfma_f32_16x16x32_bf16 v[236:239], a[156:159], a[136:139], v[236:239]\n"
    "v_mfma_f32_16x16x32_bf16 v[252:255], a[156:159], a[140:143], v[252:255]\n"
    "v_mfma_f32_16x16x32_bf16 v[248:251], a[152:155], a[140:143], v[248:251]\n"
    "v_mfma_f32_16x16x32_bf16 v[244:247], a[148:151], a[140:143], v[244:247]\n"
    "v_mfma_f32_16x16x32_bf16 v[240:243], a[144:147], a[140:143], v[240:243]\n"
    "s_waitcnt lgkmcnt(0)\n"
    "buffer_load_dwordx4 v8, s[60:63], s82 offen offset:2048 sc1 lds\n"
    "v_mfma_f32_16x16x32_bf16 v[128:131], a[96:99], a[64:67], v[128:131]\n"
    "ds_read_b128 a[112:115], v13 offset:0\n"
    "v_mfma_f32_16x16x32_bf16 v[132:135], a[100:103], a[64:67], v[132:135]\n"
    "ds_read_b128 a[144:147], v14 offset:1024\n"
    "v_mfma_f32_16x16x32_bf16 v[136:139], a[104:107], a[64:67], v[136:139]\n"
    "v_mfma_f32_16x16x32_bf16 v[140:143], a[108:111], a[64:67], v[140:143]\n"
    "ds_read_b128 a[148:151], v14 offset:3072\n"
    "v_mfma_f32_16x16x32_bf16 v[156:159], a[108:111], a[68:71], v[156:159]\n"
    "ds_read_b128 a[152:155], v14 offset:5120\n"
    "buffer_load_dwordx4 v9, s[60:63], s83 offen offset:3072 sc1 lds\n"
    "v_mfma_f32_16x16x32_bf16 v[152:155], a[104:107], a[68:71], v[152:155]\n"
    "v_mfma_f32_16x16x32_bf16 v[148:151], a[100:103], a[68:71], v[148:151]\n"
    "ds_read_b128 a[156:159], v14 offset:7168\n"
    "buffer_store_dwordx4 v[84:87], v7, s[72:75], 0 offen offset:1024 sc1\n"
    "v_mfma_f32_16x16x32_bf16 v[144:147], a[96:99], a[68:71], v[144:147]\n"
    "ds_read_b128 a[116:119], v13 offset:2048\n"
    "v_mfma_f32_16x16x32_bf16 v[160:163], a[96:99], a[72:75], v[160:163]\n"
    "s_add_u32 m0, s98, 81920\n"
    "v_mfma_f32_16x16x32_bf16 v[164:167], a[100:103], a[72:75], v[164:167]\n"
    "ds_read_b128 a[120:123], v13 offset:4096\n"
    "buffer_load_dwordx4 v10, s[64:67], 0 offen offset:0 sc1 lds\n"
    "v_mfma_f32_16x16x32_bf16 v[168:171], a[104:107], a[72:75], v[168:171]\n"
    "ds_read_b128 a[124:127], v13 offset:6144\n"
    "v_mfma_f32_16x16x32_bf16 v[172:175], a[108:111], a[72:75], v[172:175]\n"
    "v_mfma_f32_16x16x32_bf16 v[188:191], a[108:111], a[76:79], v[188:191]\n"
    "ds_read_b128 a[128:131], v13 offset:8192\n"
    "v_mfma_f32_16x16x32_bf16 v[184:187], a[104:107], a[76:79], v[184:187]\n"
    "ds_read_b128 a[132:135], v13 offset:10240\n"
    "v_mfma_f32_16x16x32_bf16 v[180:183], a[100:103], a[76:79], v[180:183]\n"
    "buffer_load_dwordx4 v10, s[64:67], 0 offen offset:1024 sc1 lds\n"
    "v_mfma_f32_16x16x32_bf16 v[176:179], a[96:99], a[76:79], v[176:179]\n"
    "ds_read_b128 a[136:139], v13 offset:12288\n"
    "v_mfma_f32_16x16x32_bf16 v[192:195], a[96:99], a[80:83], v[192:195]\n"
    "ds_read_b128 a[140:143], v13 offset:14336\n"
    "v_mfma_f32_16x16x32_bf16 v[196:199], a[100:103], a[80:83], v[196:199]\n"
    "v_add_u32 v12, s97, v12\n"
    "v_mfma_f32_16x16x32_bf16 v[200:203], a[104:107], a[80:83], v[200:203]\n"
    "v_add_u32 v13, s97, v13\n"
    "v_mfma_f32_16x16x32_bf16 v[204:207], a[108:111], a[80:83], v[204:207]\n"
    "buffer_load_dwordx4 v10, s[64:67], 0 offen offset:2048 sc1 lds\n"
    "v_mfma_f32_16x16x32_bf16 v[220:223], a[108:111], a[84:87], v[220:223]\n"
    "v_add_u32 v14, s97, v14\n"
    "v_mfma_f32_16x16x32_bf16 v[216:219], a[104:107], a[84:87], v[216:219]\n"
    "v_mfma_f32_16x16x32_bf16 v[212:215], a[100:103], a[84:87], v[212:215]\n"
    "v_mfma_f32_16x16x32_bf16 v[208:211], a[96:99], a[84:87], v[208:211]\n"
    "v_mfma_f32_16x16x32_bf16 v[224:227], a[96:99], a[88:91], v[224:227]\n"
    "buffer_load_dwordx4 v10, s[64:67], 0 offen offset:3072 sc1 lds\n"
    "v_mfma_f32_16x16x32_bf16 v[228:231], a[100:103], a[88:91], v[228:231]\n"
    "v_mfma_f32_16x16x32_bf16 v[232:235], a[104:107], a[88:91], v[232:235]\n"
    "v_mfma_f32_16x16x32_bf16 v[236:239], a[108:111], a[88:91], v[236:239]\n"
    "v_cmp_ne_u64 vcc, s[8:9], v[100:101]\n"
    "v_mfma_f32_16x16x32_bf16 v[252:255], a[108:111], a[92:95], v[252:255]\n"
    "s_sub_u32 s60, s60, 1664\n"
    "s_subb_u32 s61, s61, 0\n"
    "s_cbranch_vccnz BP_slow\n"
    "BP_back:\n"
    "v_mfma_f32_16x16x32_bf16 v[248:251], a[104:107], a[92:95], v[248:251]\n"
    "s_sub_u32 s64, s64, 212992\n"
    "s_subb_u32 s65, s65, 0\n"
    "v_mfma_f32_16x16x32_bf16 v[244:247], a[100:103], a[92:95], v[244:247]\n"
    "v_mfma_f32_16x16x32_bf16 v[240:243], a[96:99], a[92:95], v[240:243]\n"
    "s_waitcnt lgkmcnt(0) vmcnt(14)\n"
    "s_barrier\n"
    "ds_read_b128 a[64:67], v12 offset:0\n"
    "v_mfma_f32_16x16x32_bf16 v[128:131], a[144:147], a[112:115], v[128:131]\n"
    "ds_read_b128 a[96:99], v14 offset:0\n"
    "s_add_u32 m0, s92, 98304\n"
    "v_mfma_f32_16x16x32_bf16 v[132:135], a[148:151], a[112:115], v[132:135]\n"
    "buffer_load_dwordx4 v8, s[60:63], s76 offen offset:0 sc1 lds\n"
    "v_mfma_f32_16x16x32_bf16 v[136:139], a[152:155], a[112:115], v[136:139]\n"
    "ds_read_b128 a[100:103], v14 offset:2048\n"
    "v_mfma_f32_16x16x32_bf16 v[140:143], a[156:159], a[112:115], v[140:143]\n"
    "ds_read_b128 a[104:107], v14 offset:4096\n"
    "v_mfma_f32_16x16x32_bf16 v[156:159], a[156:159], a[116:119], v[156:159]\n"
    "v_mfma_f32_16x16x32_bf16 v[152:155], a[152:155], a[116:119], v[152:155]\n"
    "ds_read_b128 a[108:111], v14 offset:6144\n"
    "v_mfma_f32_16x16x32_bf16 v[148:151], a[148:151], a[116:119], v[148:151]\n"
    "ds_read_b128 a[68:71], v12 offset:2048\n"
    "buffer_load_dwordx4 v9, s[60:63], s77 offen offset:1024 sc1 lds\n"
    "v_mfma_f32_16x16x32_bf16 v[144:147], a[144:147], a[116:119], v[144:147]\n"
    "v_mfma_f32_16x16x32_bf16 v[160:163], a[144:147], a[120:123], v[160:163]\n"
    "ds_read_b128 a[72:75], v12 offset:4096\n"
    "v_mfma_f32_16x16x32_bf16 v[164:167], a[148:151], a[120:123], v[164:167]\n"
    "ds_read_b128 a[76:79], v12 offset:6144\n"
    "v_mfma_f32_16x16x32_bf16 v[168:171], a[152:155], a[120:123], v[168:171]\n"
    "v_mfma_f32_16x16x32_bf16 v[172:175], a[156:159], a[120:123], v[172:175]\n"
    "ds_read_b128 a[80:83], v12 offset:8192\n"
    "buffer_load_dwordx4 v8, s[60:63], s78 offen offset:2048 sc1 lds\n"
    "v_mfma_f32_16x16x32_bf16 v[188:191], a[156:159], a[124:127], v[188:191]\n"
    "ds_read_b128 a[84:87], v12 offset:10240\n"
    "v_mfma_f32_16x16x32_bf16 v[184:187], a[152:155], a[124:127], v[184:187]\n"
    "buffer_store_dwordx4 v[88:91], v7, s[72:75], 0 offen offset:2048 sc1\n"
    "v_mfma_f32_16x16x32_bf16 v[180:183], a[148:151], a[124:127], v[180:183]\n"
    "ds_read_b128 a[88:91], v12 offset:12288\n"
    "v_mfma_f32_16x16x32_bf16 v[176:179], a[144:147], a[124:127], v[176:179]\n"
    "ds_read_b128 a[92:95], v12 offset:14336\n"
    "v_mfma_f32_16x16x32_bf16 v[192:195], a[144:147], a[128:131], v[192:195]\n"
    "buffer_load_dwordx4 v9, s[60:63], s79 offen offset:3072 sc1 lds\n"
    "v_mfma_f32_16x16x32_bf16 v[196:199], a[148:151], a[128:131], v[196:199]\n"
    "v_mfma_f32_16x16x32_bf16 v[200:203], a[152:155], a[128:131], v[200:203]\n"
    "v_mfma_f32_16x16x32_bf16 v[204:207], a[156:159], a[128:131], v[204:207]\n"
    "v_mfma_f32_16x16x32_bf16 v[220:223], a[156:159], a[132:135], v[220:223]\n"
    "s_add_u32 m0, s92, 102400\n"
    "v_mfma_f32_16x16x32_bf16 v[216:219], a[152:155], a[132:135], v[216:219]\n"
    "buffer_load_dwordx4 v8, s[60:63], s80 offen offset:0 sc1 lds\n"
    "v_mfma_f32_16x16x32_bf16 v[212:215], a[148:151], a[132:135], v[212:215]\n"
    "v_mfma_f32_16x16x32_bf16 v[208:211], a[144:147], a[132:135], v[208:211]\n"
    "v_mfma_f32_16x16x32_bf16 v[224:227], a[144:147], a[136:139], v[224:227]\n"
    "v_mfma_f32_16x16x32_bf16 v[228:231], a[148:151], a[136:139], v[228:231]\n"
    "v_mfma_f32_16x16x32_bf16 v[232:235], a[152:155], a[136:139], v[232:235]\n"
    "buffer_load_dwordx4 v9, s[60:63], s81 offen offset:1024 sc1 lds\n"
    "v_mfma_f32_16x16x32_bf16 v[236:239], a[156:159], a[136:139], v[236:239]\n"
    "v_mfma_f32_16x16x32_bf16 v[252:255], a[156:159], a[140:143], v[252:255]\n"
    "v_mfma_f32_16x16x32_bf16 v[248:251], a[152:155], a[140:143], v[248:251]\n"
    "v_mfma_f32_16x16x32_bf16 v[244:247], a[148:151], a[140:143], v[244:247]\n"
    "v_mfma_f32_16x16x32_bf16 v[240:243], a[144:147], a[140:143], v[240:243]\n"
    "s_waitcnt lgkmcnt(0)\n"
    "buffer_load_dwordx4 v8, s[60:63], s82 offen offset:2048 sc1 lds\n"
    "v_mfma_f32_16x16x32_bf16 v[128:131], a[96:99], a[64:67], v[128:131]\n"
    "ds_read_b128 a[112:115], v13 offset:0\n"
    "v_mfma_f32_16x16x32_bf16 v[132:135], a[100:103], a[64:67], v[132:135]\n"
    "ds_read_b128 a[144:147], v14 offset:1024\n"
    "v_mfma_f32_16x16x32_bf16 v[136:139], a[104:107], a[64:67], v[136:139]\n"
    "v_mfma_f32_16x16x32_bf16 v[140:143], a[108:111], a[64:67], v[140:143]\n"
    "ds_read_b128 a[148:151], v14 offset:3072\n"
    "v_mfma_f32_16x16x32_bf16 v[156:159], a[108:111], a[68:71], v[156:159]\n"
    "ds_read_b128 a[152:155], v14 offset:5120\n"
    "buffer_load_dwordx4 v9, s[60:63], s83 offen offset:3072 sc1 lds\n"
    "v_mfma_f32_16x16x32_bf16 v[152:155], a[104:107], a[68:71], v[152:155]\n"
    "v_mfma_f32_16x16x32_bf16 v[148:151], a[100:103], a[68:71], v[148:151]\n"
    "ds_read_b128 a[156:159], v14 offset:7168\n"
    "buffer_store_dwordx4 v[92:95], v7, s[72:75], 0 offen offset:3072 sc1\n"
    "v_add_u32 v7, 4096, v7\n"
    "v_mfma_f32_16x16x32_bf16 v[144:147], a[96:99], a[68:71], v[144:147]\n"
    "ds_read_b128 a[116:119], v13 offset:2048\n"
    "v_mfma_f32_16x16x32_bf16 v[160:163], a[96:99], a[72:75], v[160:163]\n"
    "s_add_u32 m0, s98, 131072\n"
    "v_mfma_f32_16x16x32_bf16 v[164:167], a[100:103], a[72:75], v[164:167]\n"
    "ds_read_b128 a[120:123], v13 offset:4096\n"
    "buffer_load_dwordx4 v10, s[64:67], 0 offen offset:0 sc1 lds\n"
    "v_mfma_f32_16x16x32_bf16 v[168:171], a[104:107], a[72:75], v[168:171]\n"
    "ds_read_b128 a[124:127], v13 offset:6144\n"
    "v_mfma_f32_16x16x32_bf16 v[172:175], a[108:111], a[72:75], v[172:175]\n"
    "v_mfma_f32_16x16x32_bf16 v[188:191], a[108:111], a[76:79], v[188:191]\n"
    "ds_read_b128 a[128:131], v13 offset:8192\n"
    "v_mfma_f32_16x16x32_bf16 v[184:187], a[104:107], a[76:79], v[184:187]\n"
    "ds_read_b128 a[132:135], v13 offset:10240\n"
    "v_mfma_f32_16x16x32_bf16 v[180:183], a[100:103], a[76:79], v[180:183]\n"
    "buffer_load_dwordx4 v10, s[64:67], 0 offen offset:1024 sc1 lds\n"
    "v_mfma_f32_16x16x32_bf16 v[176:179], a[96:99], a[76:79], v[176:179]\n"
    "ds_read_b128 a[136:139], v13 offset:12288\n"
    "v_mfma_f32_16x16x32_bf16 v[192:195], a[96:99], a[80:83], v[192:195]\n"
    "ds_read_b128 a[140:143], v13 offset:14336\n"
    "v_mfma_f32_16x16x32_bf16 v[196:199], a[100:103], a[80:83], v[196:199]\n"
    "v_add_u32 v12, s96, v12\n"
    "v_mfma_f32_16x16x32_bf16 v[200:203], a[104:107], a[80:83], v[200:203]\n"
    "v_add_u32 v13, s96, v13\n"
    "v_mfma_f32_16x16x32_bf16 v[204:207], a[108:111], a[80:83], v[204:207]\n"
    "buffer_load_dwordx4 v10, s[64:67], 0 offen offset:2048 sc1 lds\n"
    "v_mfma_f32_16x16x32_bf16 v[220:223], a[108:111], a[84:87], v[220:223]\n"
    "v_add_u32 v14, s96, v14\n"
    "v_mfma_f32_16x16x32_bf16 v[216:219], a[104:107], a[84:87], v[216:219]\n"
    "v_mfma_f32_16x16x32_bf16 v[212:215], a[100:103], a[84:87], v[212:215]\n"
    "v_mfma_f32_16x16x32_bf16 v[208:211], a[96:99], a[84:87], v[208:211]\n"
    "v_mfma_f32_16x16x32_bf16 v[224:227], a[96:99], a[88:91], v[224:227]\n"
    "buffer_load_dwordx4 v10, s[64:67], 0 offen offset:3072 sc1 lds\n"
    "v_mfma_f32_16x16x32_bf16 v[228:231], a[100:103], a[88:91], v[228:231]\n"
    "v_mfma_f32_16x16x32_bf16 v[232:235], a[104:107], a[88:91], v[232:235]\n"
    "v_mfma_f32_16x16x32_bf16 v[236:239], a[108:111], a[88:91], v[236:239]\n"
    "v_mfma_f32_16x16x32_bf16 v[252:255], a[108:111], a[92:95], v[252:255]\n"
    "s_add_u32 s60, s60, 256\n"
    "s_addc_u32 s61, s61, 0\n"
    "v_mfma_f32_16x16x32_bf16 v[248:251], a[104:107], a[92:95], v[248:251]\n"
    "s_add_u32 s64, s64, 32768\n"
    "s_addc_u32 s65, s65, 0\n"
    "v_mfma_f32_16x16x32_bf16 v[244:247], a[100:103], a[92:95], v[244:247]\n"
    "v_mfma_f32_16x16x32_bf16 v[240:243], a[96:99], a[92:95], v[240:243]\n"
    "s_waitcnt lgkmcnt(0) vmcnt(14)\n"
    "s_barrier\n"
    "ds_read_b128 a[64:67], v12 offset:0\n"
    "v_mfma_f32_16x16x32_bf16 v[128:131], a[144:147], a[112:115], v[128:131]\n"
    "ds_read_b128 a[96:99], v14 offset:0\n"
    "s_add_u32 m0, s92, 0\n"
    "v_mfma_f32_16x16x32_bf16 v[132:135], a[148:151], a[112:115], v[132:135]\n"
    "buffer_load_dwordx4 v8, s[60:63], s76 offen offset:0 sc1 lds\n"
    "v_mfma_f32_16x16x32_bf16 v[136:139], a[152:155], a[112:115], v[136:139]\n"
    "ds_read_b128 a[100:103], v14 offset:2048\n"
    "v_mfma_f32_16x16x32_bf16 v[140:143], a[156:159], a[112:115], v[140:143]\n"
    "ds_read_b128 a[104:107], v14 offset:4096\n"
    "v_mfma_f32_16x16x32_bf16 v[156:159], a[156:159], a[116:119], v[156:159]\n"
    "v_mfma_f32_16x16x32_bf16 v[152:155], a[152:155], a[116:119], v[152:155]\n"
    "ds_read_b128 a[108:111], v14 offset:6144\n"
    "v_mfma_f32_16x16x32_bf16 v[148:151], a[148:151], a[116:119], v[148:151]\n"
    "ds_read_b128 a[68:71], v12 offset:2048\n"
    "buffer_load_dwordx4 v9, s[60:63], s77 offen offset:1024 sc1 lds\n"
    "v_mfma_f32_16x16x32_bf16 v[144:147], a[144:147], a[116:119], v[144:147]\n"
    "v_mfma_f32_16x16x32_bf16 v[160:163], a[144:147], a[120:123], v[160:163]\n"
    "ds_read_b128 a[72:75], v12 offset:4096\n"
    "v_mfma_f32_16x16x32_bf16 v[164:167], a[148:151], a[120:123], v[164:167]\n"
    "ds_read_b128 a[76:79], v12 offset:6144\n"
    "v_mfma_f32_16x16x32_bf16 v[168:171], a[152:155], a[120:123], v[168:171]\n"
    "v_mfma_f32_16x16x32_bf16 v[172:175], a[156:159], a[120:123], v[172:175]\n"
    "ds_read_b128 a[80:83], v12 offset:8192\n"
    "buffer_load_dwordx4 v8, s[60:63], s78 offen offset:2048 sc1 lds\n"
    "v_mfma_f32_16x16x32_bf16 v[188:191], a[156:159], a[124:127], v[188:191]\n"
    "ds_read_b128 a[84:87], v12 offset:10240\n"
    "v_mfma_f32_16x16x32_bf16 v[184:187], a[152:155], a[124:127], v[184:187]\n"
    "v_mfma_f32_16x16x32_bf16 v[180:183], a[148:151], a[124:127], v[180:183]\n"
    "ds_read_b128 a[88:91], v12 offset:12288\n"
    "v_mfma_f32_16x16x32_bf16 v[176:179], a[144:147], a[124:127], v[176:179]\n"
    "ds_read_b128 a[92:95], v12 offset:14336\n"
    "v_mfma_f32_16x16x32_bf16 v[192:195], a[144:147], a[128:131], v[192:195]\n"
    "buffer_load_dwordx4 v9, s[60:63], s79 offen offset:3072 sc1 lds\n"
    "v_mfma_f32_16x16x32_bf16 v[196:199], a[148:151], a[128:131], v[196:199]\n"
    "v_mfma_f32_16x16x32_bf16 v[200:203], a[152:155], a[128:131], v[200:203]\n"
    "v_mfma_f32_16x16x32_bf16 v[204:207], a[156:159], a[128:131], v[204:207]\n"
    "v_mfma_f32_16x16x32_bf16 v[220:223], a[156:159], a[132:135], v[220:223]\n"
    "s_add_u32 m0, s92, 4096\n"
    "v_mfma_f32_16x16x32_bf16 v[216:219], a[152:155], a[132:135], v[216:219]\n"
    "buffer_load_dwordx4 v8, s[60:63], s80 offen offset:0 sc1 lds\n"
    "v_mfma_f32_16x16x32_bf16 v[212:215], a[148:151], a[132:135], v[212:215]\n"
    "v_mfma_f32_16x16x32_bf16 v[208:211], a[144:147], a[132:135], v[208:211]\n"
    "v_mfma_f32_16x16x32_bf16 v[224:227], a[144:147], a[136:139], v[224:227]\n"
    "v_mfma_f32_16x16x32_bf16 v[228:231], a[148:151], a[136:139], v[228:231]\n"
    "v_mfma_f32_16x16x32_bf16 v[232:235], a[152:155], a[136:139], v[232:235]\n"
    "buffer_load_dwordx4 v9, s[60:63], s81 offen offset:1024 sc1 lds\n"
    "v_mfma_f32_16x16x32_bf16 v[236:239], a[156:159], a[136:139], v[236:239]\n"
    "v_mfma_f32_16x16x32_bf16 v[252:255], a[156:159], a[140:143], v[252:255]\n"
    "v_mfma_f32_16x16x32_bf16 v[248:251], a[152:155], a[140:143], v[248:251]\n"
    "v_mfma_f32_16x16x32_bf16 v[244:247], a[148:151], a[140:143], v[244:247]\n"
    "v_mfma_f32_16x16x32_bf16 v[240:243], a[144:147], a[140:143], v[240:243]\n"
    "s_waitcnt lgkmcnt(0)\n"
    "buffer_load_dwordx4 v8, s[60:63], s82 offen offset:2048 sc1 lds\n"
    "v_mfma_f32_16x16x32_bf16 v[128:131], a[96:99], a[64:67], v[128:131]\n"
    "ds_read_b128 a[112:115], v13 offset:0\n"
    "v_mfma_f32_16x16x32_bf16 v[132:135], a[100:103], a[64:67], v[132:135]\n"
    "ds_read_b128 a[144:147], v14 offset:1024\n"
    "v_mfma_f32_16x16x32_bf16 v[136:139], a[104:107], a[64:67], v[136:139]\n"
    "v_mfma_f32_16x16x32_bf16 v[140:143], a[108:111], a[64:67], v[140:143]\n"
    "ds_read_b128 a[148:151], v14 offset:3072\n"
    "v_mfma_f32_16x16x32_bf16 v[156:159], a[108:111], a[68:71], v[156:159]\n"
    "ds_read_b128 a[152:155], v14 offset:5120\n"
    "buffer_load_dwordx4 v9, s[60:63], s83 offen offset:3072 sc1 lds\n"
    "v_mfma_f32_16x16x32_bf16 v[152:155], a[104:107], a[68:71], v[152:155]\n"
    "v_mfma_f32_16x16x32_bf16 v[148:151], a[100:103], a[68:71], v[148:151]\n"
    "ds_read_b128 a[156:159], v14 offset:7168\n"
    "TC7:\n"
    "s_cbranch_execz TC11\n"
    "v_mfma_f32_16x16x32_bf16 v[144:147], a[96:99], a[68:71], v[144:147]\n"
    "ds_read_b128 a[116:119], v13 offset:2048\n"
    "v_mfma_f32_16x16x32_bf16 v[160:163], a[96:99], a[72:75], v[160:163]\n"
    "s_add_u32 m0, s98, 32768\n"
    "v_mfma_f32_16x16x32_bf16 v[164:167], a[100:103], a[72:75], v[164:167]\n"
    "ds_read_b128 a[120:123], v13 offset:4096\n"
    "buffer_load_dwordx4 v10, s[64:67], 0 offen offset:0 sc1 lds\n"
    "v_mfma_f32_16x16x32_bf16 v[168:171], a[104:107], a[72:75], v[168:171]\n"
    "ds_read_b128 a[124:127], v13 offset:6144\n"
    "v_mfma_f32_16x16x32_bf16 v[172:175], a[108:111], a[72:75], v[172:175]\n"
    "v_mfma_f32_16x16x32_bf16 v[188:191], a[108:111], a[76:79], v[188:191]\n"
    "ds_read_b128 a[128:131], v13 offset:8192\n"
    "v_mfma_f32_16x16x32_bf16 v[184:187], a[104:107], a[76:79], v[184:187]\n"
    "ds_read_b128 a[132:135], v13 offset:10240\n"
    "v_mfma_f32_16x16x32_bf16 v[180:183], a[100:103], a[76:79], v[180:183]\n"
    "buffer_load_dwordx4 v10, s[64:67], 0 offen offset:1024 sc1 lds\n"
    "v_mfma_f32_16x16x32_bf16 v[176:179], a[96:99], a[76:79], v[176:179]\n"
    "ds_read_b128 a[136:139], v13 offset:12288\n"
    "v_mfma_f32_16x16x32_bf16 v[192:195], a[96:99], a[80:83], v[192:195]\n"
    "ds_read_b128 a[140:143], v13 offset:14336\n"
    "v_mfma_f32_16x16x32_bf16 v[196:199], a[100:103], a[80:83], v[196:199]\n"
    "v_add_u32 v12, s96, v12\n"
    "v_mfma_f32_16x16x32_bf16 v[200:203], a[104:107], a[80:83], v[200:203]\n"
    "v_add_u32 v13, s96, v13\n"
    "v_mfma_f32_16x16x32_bf16 v[204:207], a[108:111], a[80:83], v[204:207]\n"
    "buffer_load_dwordx4 v10, s[64:67], 0 offen offset:2048 sc1 lds\n"
    "v_mfma_f32_16x16x32_bf16 v[220:223], a[108:111], a[84:87], v[220:223]\n"
    "v_add_u32 v14, s96, v14\n"
    "v_mfma_f32_16x16x32_bf16 v[216:219], a[104:107], a[84:87], v[216:219]\n"
    "v_mfma_f32_16x16x32_bf16 v[212:215], a[100:103], a[84:87], v[212:215]\n"
    "v_mfma_f32_16x16x32_bf16 v[208:211], a[96:99], a[84:87], v[208:211]\n"
    "v_mfma_f32_16x16x32_bf16 v[224:227], a[96:99], a[88:91], v[224:227]\n"
    "buffer_load_dwordx4 v10, s[64:67], 0 offen offset:3072 sc1 lds\n"
    "v_mfma_f32_16x16x32_bf16 v[228:231], a[100:103], a[88:91], v[228:231]\n"
    "v_mfma_f32_16x16x32_bf16 v[232:235], a[104:107], a[88:91], v[232:235]\n"
    "v_mfma_f32_16x16x32_bf16 v[236:239], a[108:111], a[88:91], v[236:239]\n"
    "v_mfma_f32_16x16x32_bf16 v[252:255], a[108:111], a[92:95], v[252:255]\n"
    "s_add_u32 s60, s60, 256\n"
    "s_addc_u32 s61, s61, 0\n"
    "v_mfma_f32_16x16x32_bf16 v[248:251], a[104:107], a[92:95], v[248:251]\n"
    "s_add_u32 s64, s64, 32768\n"
    "s_addc_u32 s65, s65, 0\n"
    "v_mfma_f32_16x16x32_bf16 v[244:247], a[100:103], a[92:95], v[244:247]\n"
    "v_mfma_f32_16x16x32_bf16 v[240:243], a[96:99], a[92:95], v[240:243]\n"
    "s_waitcnt lgkmcnt(0) vmcnt(12)\n"
    "s_barrier\n"
    "ds_read_b128 a[64:67], v12 offset:0\n"
    "v_mfma_f32_16x16x32_bf16 v[128:131], a[144:147], a[112:115], v[128:131]\n"
    "ds_read_b128 a[96:99], v14 offset:0\n"
    "s_add_u32 m0, s92, 49152\n"
    "v_mfma_f32_16x16x32_bf16 v[132:135], a[148:151], a[112:115], v[132:135]\n"
    "buffer_load_dwordx4 v8, s[60:63], s76 offen offset:0 sc1 lds\n"
    "v_mfma_f32_16x16x32_bf16 v[136:139], a[152:155], a[112:115], v[136:139]\n"
    "ds_read_b128 a[100:103], v14 offset:2048\n"
    "s_lshl_b32 s69, s54, 2\n"
    "s_add_u32 s69, s69, s55\n"
    "v_mfma_f32_16x16x32_bf16 v[140:143], a[156:159], a[112:115], v[140:143]\n"
    "ds_read_b128 a[104:107], v14 offset:4096\n"
    "s_lshl_b32 s69, s69, 3\n"
    "s_add_u32 s69, s69, 37773312\n"
    "v_mfma_f32_16x16x32_bf16 v[156:159], a[156:159], a[116:119], v[156:159]\n"
    "v_mov_b32 v2, s8\n"
    "v_mov_b32 v3, s9\n"
    "v_mfma_f32_16x16x32_bf16 v[152:155], a[152:155], a[116:119], v[152:155]\n"
    "ds_read_b128 a[108:111], v14 offset:6144\n"
    "v_mov_b32 v4, s69\n"
    "v_mfma_f32_16x16x32_bf16 v[148:151], a[148:151], a[116:119], v[148:151]\n"
    "ds_read_b128 a[68:71], v12 offset:2048\n"
    "buffer_load_dwordx4 v9, s[60:63], s77 offen offset:1024 sc1 lds\n"
    "v_mfma_f32_16x16x32_bf16 v[144:147], a[144:147], a[116:119], v[144:147]\n"
    "v_mfma_f32_16x16x32_bf16 v[160:163], a[144:147], a[120:123], v[160:163]\n"
    "ds_read_b128 a[72:75], v12 offset:4096\n"
    "v_mfma_f32_16x16x32_bf16 v[164:167], a[148:151], a[120:123], v[164:167]\n"
    "ds_read_b128 a[76:79], v12 offset:6144\n"
    "v_mfma_f32_16x16x32_bf16 v[168:171], a[152:155], a[120:123], v[168:171]\n"
    "v_mfma_f32_16x16x32_bf16 v[172:175], a[156:159], a[120:123], v[172:175]\n"
    "ds_read_b128 a[80:83], v12 offset:8192\n"
    "buffer_load_dwordx4 v8, s[60:63], s78 offen offset:2048 sc1 lds\n"
    "v_mfma_f32_16x16x32_bf16 v[188:191], a[156:159], a[124:127], v[188:191]\n"
    "ds_read_b128 a[84:87], v12 offset:10240\n"
    "v_mfma_f32_16x16x32_bf16 v[184:187], a[152:155], a[124:127], v[184:187]\n"
    "v_mfma_f32_16x16x32_bf16 v[180:183], a[148:151], a[124:127], v[180:183]\n"
    "ds_read_b128 a[88:91], v12 offset:12288\n"
    "v_mfma_f32_16x16x32_bf16 v[176:179], a[144:147], a[124:127], v[176:179]\n"
    "ds_read_b128 a[92:95], v12 offset:14336\n"
    "v_mfma_f32_16x16x32_bf16 v[192:195], a[144:147], a[128:131], v[192:195]\n"
    "buffer_load_dwordx4 v9, s[60:63], s79 offen offset:3072 sc1 lds\n"
    "v_mfma_f32_16x16x32_bf16 v[196:199], a[148:151], a[128:131], v[196:199]\n"
    "v_mfma_f32_16x16x32_bf16 v[200:203], a[152:155], a[128:131], v[200:203]\n"
    "v_mfma_f32_16x16x32_bf16 v[204:207], a[156:159], a[128:131], v[204:207]\n"
    "v_mfma_f32_16x16x32_bf16 v[220:223], a[156:159], a[132:135], v[220:223]\n"
    "s_add_u32 m0, s92, 53248\n"
    "v_mfma_f32_16x16x32_bf16 v[216:219], a[152:155], a[132:135], v[216:219]\n"
    "buffer_load_dwordx4 v8, s[60:63], s80 offen offset:0 sc1 lds\n"
    "v_mfma_f32_16x16x32_bf16 v[212:215], a[148:151], a[132:135], v[212:215]\n"
    "v_mfma_f32_16x16x32_bf16 v[208:211], a[144:147], a[132:135], v[208:211]\n"
    "v_mfma_f32_16x16x32_bf16 v[224:227], a[144:147], a[136:139], v[224:227]\n"
    "v_mfma_f32_16x16x32_bf16 v[228:231], a[148:151], a[136:139], v[228:231]\n"
    "v_mfma_f32_16x16x32_bf16 v[232:235], a[152:155], a[136:139], v[232:235]\n"
    "buffer_load_dwordx4 v9, s[60:63], s81 offen offset:1024 sc1 lds\n"
    "v_mfma_f32_16x16x32_bf16 v[236:239], a[156:159], a[136:139], v[236:239]\n"
    "v_mfma_f32_16x16x32_bf16 v[252:255], a[156:159], a[140:143], v[252:255]\n"
    "v_mfma_f32_16x16x32_bf16 v[248:251], a[152:155], a[140:143], v[248:251]\n"
    "s_mov_b64 exec, 1\n"
    "global_store_dwordx2 v4, v[2:3], s[52:53] sc1\n"
    "s_mov_b64 exec, -1\n"
    "v_mfma_f32_16x16x32_bf16 v[244:247], a[148:151], a[140:143], v[244:247]\n"
    "v_mfma_f32_16x16x32_bf16 v[240:243], a[144:147], a[140:143], v[240:243]\n"
    "s_waitcnt lgkmcnt(0)\n"
    "buffer_load_dwordx4 v8, s[60:63], s82 offen offset:2048 sc1 lds\n"
    "v_mfma_f32_16x16x32_bf16 v[128:131], a[96:99], a[64:67], v[128:131]\n"
    "ds_read_b128 a[112:115], v13 offset:0\n"
    "v_mfma_f32_16x16x32_bf16 v[132:135], a[100:103], a[64:67], v[132:135]\n"
    "ds_read_b128 a[144:147], v14 offset:1024\n"
    "v_mfma_f32_16x16x32_bf16 v[136:139], a[104:107], a[64:67], v[136:139]\n"
    "v_mfma_f32_16x16x32_bf16 v[140:143], a[108:111], a[64:67], v[140:143]\n"
    "ds_read_b128 a[148:151], v14 offset:3072\n"
    "v_mfma_f32_16x16x32_bf16 v[156:159], a[108:111], a[68:71], v[156:159]\n"
    "ds_read_b128 a[152:155], v14 offset:5120\n"
    "buffer_load_dwordx4 v9, s[60:63], s83 offen offset:3072 sc1 lds\n"
    "v_mfma_f32_16x16x32_bf16 v[152:155], a[104:107], a[68:71], v[152:155]\n"
    "v_mfma_f32_16x16x32_bf16 v[148:151], a[100:103], a[68:71], v[148:151]\n"
    "ds_read_b128 a[156:159], v14 offset:7168\n"
    "v_mfma_f32_16x16x32_bf16 v[144:147], a[96:99], a[68:71], v[144:147]\n"
    "ds_read_b128 a[116:119], v13 offset:2048\n"
    "v_mfma_f32_16x16x32_bf16 v[160:163], a[96:99], a[72:75], v[160:163]\n"
    "s_add_u32 m0, s98, 81920\n"
    "v_mfma_f32_16x16x32_bf16 v[164:167], a[100:103], a[72:75], v[164:167]\n"
    "ds_read_b128 a[120:123], v13 offset:4096\n"
    "buffer_load_dwordx4 v10, s[64:67], 0 offen offset:0 sc1 lds\n"
    "v_mfma_f32_16x16x32_bf16 v[168:171], a[104:107], a[72:75], v[168:171]\n"
    "ds_read_b128 a[124:127], v13 offset:6144\n"
    "v_mfma_f32_16x16x32_bf16 v[172:175], a[108:111], a[72:75], v[172:175]\n"
    "v_mfma_f32_16x16x32_bf16 v[188:191], a[108:111], a[76:79], v[188:191]\n"
    "ds_read_b128 a[128:131], v13 offset:8192\n"
    "v_mfma_f32_16x16x32_bf16 v[184:187], a[104:107], a[76:79], v[184:187]\n"
    "ds_read_b128 a[132:135], v13 offset:10240\n"
    "v_mfma_f32_16x16x32_bf16 v[180:183], a[100:103], a[76:79], v[180:183]\n"
    "buffer_load_dwordx4 v10, s[64:67], 0 offen offset:1024 sc1 lds\n"
    "v_mfma_f32_16x16x32_bf16 v[176:179], a[96:99], a[76:79], v[176:179]\n"
    "ds_read_b128 a[136:139], v13 offset:12288\n"
    "v_mfma_f32_16x16x32_bf16 v[192:195], a[96:99], a[80:83], v[192:195]\n"
    "ds_read_b128 a[140:143], v13 offset:14336\n"
    "v_mfma_f32_16x16x32_bf16 v[196:199], a[100:103], a[80:83], v[196:199]\n"
    "v_add_u32 v12, s97, v12\n"
    "v_mfma_f32_16x16x32_bf16 v[200:203], a[104:107], a[80:83], v[200:203]\n"
    "v_add_u32 v13, s97, v13\n"
    "v_mfma_f32_16x16x32_bf16 v[204:207], a[108:111], a[80:83], v[204:207]\n"
    "buffer_load_dwordx4 v10, s[64:67], 0 offen offset:2048 sc1 lds\n"
    "v_mfma_f32_16x16x32_bf16 v[220:223], a[108:111], a[84:87], v[220:223]\n"
    "v_add_u32 v14, s97, v14\n"
    "v_mfma_f32_16x16x32_bf16 v[216:219], a[104:107], a[84:87], v[216:219]\n"
    "v_mfma_f32_16x16x32_bf16 v[212:215], a[100:103], a[84:87], v[212:215]\n"
    "v_mfma_f32_16x16x32_bf16 v[208:211], a[96:99], a[84:87], v[208:211]\n"
    "v_mfma_f32_16x16x32_bf16 v[224:227], a[96:99], a[88:91], v[224:227]\n"
    "buffer_load_dwordx4 v10, s[64:67], 0 offen offset:3072 sc1 lds\n"
    "v_mfma_f32_16x16x32_bf16 v[228:231], a[100:103], a[88:91], v[228:231]\n"
    "v_mfma_f32_16x16x32_bf16 v[232:235], a[104:107], a[88:91], v[232:235]\n"
    "v_mfma_f32_16x16x32_bf16 v[236:239], a[108:111], a[88:91], v[236:239]\n"
    "v_mfma_f32_16x16x32_bf16 v[252:255], a[108:111], a[92:95], v[252:255]\n"
    "s_add_u32 s60, s60, 256\n"
    "s_addc_u32 s61, s61, 0\n"
    "v_mfma_f32_16x16x32_bf16 v[248:251], a[104:107], a[92:95], v[248:251]\n"
    "s_add_u32 s64, s64, 32768\n"
    "s_addc_u32 s65, s65, 0\n"
    "v_mfma_f32_16x16x32_bf16 v[244:247], a[100:103], a[92:95], v[244:247]\n"
    "v_mfma_f32_16x16x32_bf16 v[240:243], a[96:99], a[92:95], v[240:243]\n"
    "s_waitcnt lgkmcnt(0) vmcnt(13)\n"
    "s_barrier\n"
    "ds_read_b128 a[64:67], v12 offset:0\n"
    "v_mfma_f32_16x16x32_bf16 v[128:131], a[144:147], a[112:115], v[128:131]\n"
    "ds_read_b128 a[96:99], v14 offset:0\n"
    "s_add_u32 m0, s92, 98304\n"
    "v_mfma_f32_16x16x32_bf16 v[132:135], a[148:151], a[112:115], v[132:135]\n"
    "buffer_load_dwordx4 v8, s[60:63], s76 offen offset:0 sc1 lds\n"
    "v_mfma_f32_16x16x32_bf16 v[136:139], a[152:155], a[112:115], v[136:139]\n"
    "ds_read_b128 a[100:103], v14 offset:2048\n"
    "v_mfma_f32_16x16x32_bf16 v[140:143], a[156:159], a[112:115], v[140:143]\n"
    "ds_read_b128 a[104:107], v14 offset:4096\n"
    "v_mfma_f32_16x16x32_bf16 v[156:159], a[156:159], a[116:119], v[156:159]\n"
    "v_mfma_f32_16x16x32_bf16 v[152:155], a[152:155], a[116:119], v[152:155]\n"
    "ds_read_b128 a[108:111], v14 offset:6144\n"
    "v_mfma_f32_16x16x32_bf16 v[148:151], a[148:151], a[116:119], v[148:151]\n"
    "ds_read_b128 a[68:71], v12 offset:2048\n"
    "buffer_load_dwordx4 v9, s[60:63], s77 offen offset:1024 sc1 lds\n"
    "v_mfma_f32_16x16x32_bf16 v[144:147], a[144:147], a[116:119], v[144:147]\n"
    "v_mfma_f32_16x16x32_bf16 v[160:163], a[144:147], a[120:123], v[160:163]\n"
    "ds_read_b128 a[72:75], v12 offset:4096\n"
    "v_mfma_f32_16x16x32_bf16 v[164:167], a[148:151], a[120:123], v[164:167]\n"
    "ds_read_b128 a[76:79], v12 offset:6144\n"
    "v_mfma_f32_16x16x32_bf16 v[168:171], a[152:155], a[120:123], v[168:171]\n"
    "v_mfma_f32_16x16x32_bf16 v[172:175], a[156:159], a[120:123], v[172:175]\n"
    "ds_read_b128 a[80:83], v12 offset:8192\n"
    "buffer_load_dwordx4 v8, s[60:63], s78 offen offset:2048 sc1 lds\n"
    "v_mfma_f32_16x16x32_bf16 v[188:191], a[156:159], a[124:127], v[188:191]\n"
    "ds_read_b128 a[84:87], v12 offset:10240\n"
    "v_mfma_f32_16x16x32_bf16 v[184:187], a[152:155], a[124:127], v[184:187]\n"
    "v_mfma_f32_16x16x32_bf16 v[180:183], a[148:151], a[124:127], v[180:183]\n"
    "ds_read_b128 a[88:91], v12 offset:12288\n"
    "v_mfma_f32_16x16x32_bf16 v[176:179], a[144:147], a[124:127], v[176:179]\n"
    "ds_read_b128 a[92:95], v12 offset:14336\n"
    "v_mfma_f32_16x16x32_bf16 v[192:195], a[144:147], a[128:131], v[192:195]\n"
    "buffer_load_dwordx4 v9, s[60:63], s79 offen offset:3072 sc1 lds\n"
    "v_mfma_f32_16x16x32_bf16 v[196:199], a[148:151], a[128:131], v[196:199]\n"
    "v_mfma_f32_16x16x32_bf16 v[200:203], a[152:155], a[128:131], v[200:203]\n"
    "v_mfma_f32_16x16x32_bf16 v[204:207], a[156:159], a[128:131], v[204:207]\n"
    "v_mfma_f32_16x16x32_bf16 v[220:223], a[156:159], a[132:135], v[220:223]\n"
    "s_add_u32 m0, s92, 102400\n"
    "v_mfma_f32_16x16x32_bf16 v[216:219], a[152:155], a[132:135], v[216:219]\n"
    "buffer_load_dwordx4 v8, s[60:63], s80 offen offset:0 sc1 lds\n"
    "v_mfma_f32_16x16x32_bf16 v[212:215], a[148:151], a[132:135], v[212:215]\n"
    "v_mfma_f32_16x16x32_bf16 v[208:211], a[144:147], a[132:135], v[208:211]\n"
    "v_mfma_f32_16x16x32_bf16 v[224:227], a[144:147], a[136:139], v[224:227]\n"
    "v_mfma_f32_16x16x32_bf16 v[228:231], a[148:151], a[136:139], v[228:231]\n"
    "v_mfma_f32_16x16x32_bf16 v[232:235], a[152:155], a[136:139], v[232:235]\n"
    "buffer_load_dwordx4 v9, s[60:63], s81 offen offset:1024 sc1 lds\n"
    "v_mfma_f32_16x16x32_bf16 v[236:239], a[156:159], a[136:139], v[236:239]\n"
    "v_mfma_f32_16x16x32_bf16 v[252:255], a[156:159], a[140:143], v[252:255]\n"
    "v_mfma_f32_16x16x32_bf16 v[248:251], a[152:155], a[140:143], v[248:251]\n"
    "v_mfma_f32_16x16x32_bf16 v[244:247], a[148:151], a[140:143], v[244:247]\n"
    "v_mfma_f32_16x16x32_bf16 v[240:243], a[144:147], a[140:143], v[240:243]\n"
    "s_waitcnt lgkmcnt(0)\n"
    "buffer_load_dwordx4 v8, s[60:63], s82 offen offset:2048 sc1 lds\n"
    "v_mfma_f32_16x16x32_bf16 v[128:131], a[96:99], a[64:67], v[128:131]\n"
    "ds_read_b128 a[112:115], v13 offset:0\n"
    "v_mfma_f32_16x16x32_bf16 v[132:135], a[100:103], a[64:67], v[132:135]\n"
    "ds_read_b128 a[144:147], v14 offset:1024\n"
    "v_mfma_f32_16x16x32_bf16 v[136:139], a[104:107], a[64:67], v[136:139]\n"
    "v_mfma_f32_16x16x32_bf16 v[140:143], a[108:111], a[64:67], v[140:143]\n"
    "ds_read_b128 a[148:151], v14 offset:3072\n"
    "v_mfma_f32_16x16x32_bf16 v[156:159], a[108:111], a[68:71], v[156:159]\n"
    "ds_read_b128 a[152:155], v14 offset:5120\n"
    "buffer_load_dwordx4 v9, s[60:63], s83 offen offset:3072 sc1 lds\n"
    "v_mfma_f32_16x16x32_bf16 v[152:155], a[104:107], a[68:71], v[152:155]\n"
    "v_mfma_f32_16x16x32_bf16 v[148:151], a[100:103], a[68:71], v[148:151]\n"
    "ds_read_b128 a[156:159], v14 offset:7168\n"
    "v_mfma_f32_16x16x32_bf16 v[144:147], a[96:99], a[68:71], v[144:147]\n"
    "ds_read_b128 a[116:119], v13 offset:2048\n"
    "v_mfma_f32_16x16x32_bf16 v[160:163], a[96:99], a[72:75], v[160:163]\n"
    "s_add_u32 m0, s98, 131072\n"
    "v_mfma_f32_16x16x32_bf16 v[164:167], a[100:103], a[72:75], v[164:167]\n"
    "ds_read_b128 a[120:123], v13 offset:4096\n"
    "buffer_load_dwordx4 v10, s[64:67], 0 offen offset:0 sc1 lds\n"
    "v_mfma_f32_16x16x32_bf16 v[168:171], a[104:107], a[72:75], v[168:171]\n"
    "ds_read_b128 a[124:127], v13 offset:6144\n"
    "v_mfma_f32_16x16x32_bf16 v[172:175], a[108:111], a[72:75], v[172:175]\n"
    "v_mfma_f32_16x16x32_bf16 v[188:191], a[108:111], a[76:79], v[188:191]\n"
    "ds_read_b128 a[128:131], v13 offset:8192\n"
    "v_mfma_f32_16x16x32_bf16 v[184:187], a[104:107], a[76:79], v[184:187]\n"
    "ds_read_b128 a[132:135], v13 offset:10240\n"
    "v_mfma_f32_16x16x32_bf16 v[180:183], a[100:103], a[76:79], v[180:183]\n"
    "buffer_load_dwordx4 v10, s[64:67], 0 offen offset:1024 sc1 lds\n"
    "v_mfma_f32_16x16x32_bf16 v[176:179], a[96:99], a[76:79], v[176:179]\n"
    "ds_read_b128 a[136:139], v13 offset:12288\n"
    "v_mfma_f32_16x16x32_bf16 v[192:195], a[96:99], a[80:83], v[192:195]\n"
    "ds_read_b128 a[140:143], v13 offset:14336\n"
    "v_mfma_f32_16x16x32_bf16 v[196:199], a[100:103], a[80:83], v[196:199]\n"
    "v_add_u32 v12, s96, v12\n"
    "v_mfma_f32_16x16x32_bf16 v[200:203], a[104:107], a[80:83], v[200:203]\n"
    "v_add_u32 v13, s96, v13\n"
    "v_mfma_f32_16x16x32_bf16 v[204:207], a[108:111], a[80:83], v[204:207]\n"
    "buffer_load_dwordx4 v10, s[64:67], 0 offen offset:2048 sc1 lds\n"
    "v_mfma_f32_16x16x32_bf16 v[220:223], a[108:111], a[84:87], v[220:223]\n"
    "v_add_u32 v14, s96, v14\n"
    "v_mfma_f32_16x16x32_bf16 v[216:219], a[104:107], a[84:87], v[216:219]\n"
    "v_mfma_f32_16x16x32_bf16 v[212:215], a[100:103], a[84:87], v[212:215]\n"
    "v_mfma_f32_16x16x32_bf16 v[208:211], a[96:99], a[84:87], v[208:211]\n"
    "v_mfma_f32_16x16x32_bf16 v[224:227], a[96:99], a[88:91], v[224:227]\n"
    "buffer_load_dwordx4 v10, s[64:67], 0 offen offset:3072 sc1 lds\n"
    "v_mfma_f32_16x16x32_bf16 v[228:231], a[100:103], a[88:91], v[228:231]\n"
    "v_mfma_f32_16x16x32_bf16 v[232:235], a[104:107], a[88:91], v[232:235]\n"
    "v_mfma_f32_16x16x32_bf16 v[236:239], a[108:111], a[88:91], v[236:239]\n"
    "v_mfma_f32_16x16x32_bf16 v[252:255], a[108:111], a[92:95], v[252:255]\n"
    "s_add_u32 s60, s60, 256\n"
    "s_addc_u32 s61, s61, 0\n"
    "v_mfma_f32_16x16x32_bf16 v[248:251], a[104:107], a[92:95], v[248:251]\n"
    "s_add_u32 s64, s64, 32768\n"
    "s_addc_u32 s65, s65, 0\n"
    "v_mfma_f32_16x16x32_bf16 v[244:247], a[100:103], a[92:95], v[244:247]\n"
    "v_mfma_f32_16x16x32_bf16 v[240:243], a[96:99], a[92:95], v[240:243]\n"
    "s_waitcnt lgkmcnt(0) vmcnt(12)\n"
    "s_barrier\n"
    "ds_read_b128 a[64:67], v12 offset:0\n"
    "v_mfma_f32_16x16x32_bf16 v[128:131], a[144:147], a[112:115], v[128:131]\n"
    "ds_read_b128 a[96:99], v14 offset:0\n"
    "s_add_u32 m0, s92, 0\n"
    "v_mfma_f32_16x16x32_bf16 v[132:135], a[148:151], a[112:115], v[132:135]\n"
    "buffer_load_dwordx4 v8, s[60:63], s76 offen offset:0 sc1 lds\n"
    "v_mfma_f32_16x16x32_bf16 v[136:139], a[152:155], a[112:115], v[136:139]\n"
    "ds_read_b128 a[100:103], v14 offset:2048\n"
    "v_mfma_f32_16x16x32_bf16 v[140:143], a[156:159], a[112:115], v[140:143]\n"
    "ds_read_b128 a[104:107], v14 offset:4096\n"
    "v_mfma_f32_16x16x32_bf16 v[156:159], a[156:159], a[116:119], v[156:159]\n"
    "v_mfma_f32_16x16x32_bf16 v[152:155], a[152:155], a[116:119], v[152:155]\n"
    "ds_read_b128 a[108:111], v14 offset:6144\n"
    "v_mfma_f32_16x16x32_bf16 v[148:151], a[148:151], a[116:119], v[148:151]\n"
    "ds_read_b128 a[68:71], v12 offset:2048\n"
    "buffer_load_dwordx4 v9, s[60:63], s77 offen offset:1024 sc1 lds\n"
    "v_mfma_f32_16x16x32_bf16 v[144:147], a[144:147], a[116:119], v[144:147]\n"
    "v_mfma_f32_16x16x32_bf16 v[160:163], a[144:147], a[120:123], v[160:163]\n"
    "ds_read_b128 a[72:75], v12 offset:4096\n"
    "v_mfma_f32_16x16x32_bf16 v[164:167], a[148:151], a[120:123], v[164:167]\n"
    "ds_read_b128 a[76:79], v12 offset:6144\n"
    "v_mfma_f32_16x16x32_bf16 v[168:171], a[152:155], a[120:123], v[168:171]\n"
    "v_mfma_f32_16x16x32_bf16 v[172:175], a[156:159], a[120:123], v[172:175]\n"
    "ds_read_b128 a[80:83], v12 offset:8192\n"
    "buffer_load_dwordx4 v8, s[60:63], s78 offen offset:2048 sc1 lds\n"
    "v_mfma_f32_16x16x32_bf16 v[188:191], a[156:159], a[124:127], v[188:191]\n"
    "ds_read_b128 a[84:87], v12 offset:10240\n"
    "v_mfma_f32_16x16x32_bf16 v[184:187], a[152:155], a[124:127], v[184:187]\n"
    "v_mfma_f32_16x16x32_bf16 v[180:183], a[148:151], a[124:127], v[180:183]\n"
    "ds_read_b128 a[88:91], v12 offset:12288\n"
    "v_mfma_f32_16x16x32_bf16 v[176:179], a[144:147], a[124:127], v[176:179]\n"
    "ds_read_b128 a[92:95], v12 offset:14336\n"
    "v_mfma_f32_16x16x32_bf16 v[192:195], a[144:147], a[128:131], v[192:195]\n"
    "buffer_load_dwordx4 v9, s[60:63], s79 offen offset:3072 sc1 lds\n"
    "v_mfma_f32_16x16x32_bf16 v[196:199], a[148:151], a[128:131], v[196:199]\n"
    "v_mfma_f32_16x16x32_bf16 v[200:203], a[152:155], a[128:131], v[200:203]\n"
    "v_mfma_f32_16x16x32_bf16 v[204:207], a[156:159], a[128:131], v[204:207]\n"
    "v_mfma_f32_16x16x32_bf16 v[220:223], a[156:159], a[132:135], v[220:223]\n"
    "s_add_u32 m0, s92, 4096\n"
    "v_mfma_f32_16x16x32_bf16 v[216:219], a[152:155], a[132:135], v[216:219]\n"
    "buffer_load_dwordx4 v8, s[60:63], s80 offen offset:0 sc1 lds\n"
    "v_mfma_f32_16x16x32_bf16 v[212:215], a[148:151], a[132:135], v[212:215]\n"
    "v_mfma_f32_16x16x32_bf16 v[208:211], a[144:147], a[132:135], v[208:211]\n"
    "v_mfma_f32_16x16x32_bf16 v[224:227], a[144:147], a[136:139], v[224:227]\n"
    "v_mfma_f32_16x16x32_bf16 v[228:231], a[148:151], a[136:139], v[228:231]\n"
    "v_mfma_f32_16x16x32_bf16 v[232:235], a[152:155], a[136:139], v[232:235]\n"
    "buffer_load_dwordx4 v9, s[60:63], s81 offen offset:1024 sc1 lds\n"
    "v_mfma_f32_16x16x32_bf16 v[236:239], a[156:159], a[136:139], v[236:239]\n"
    "v_mfma_f32_16x16x32_bf16 v[252:255], a[156:159], a[140:143], v[252:255]\n"
    "v_mfma_f32_16x16x32_bf16 v[248:251], a[152:155], a[140:143], v[248:251]\n"
    "v_mfma_f32_16x16x32_bf16 v[244:247], a[148:151], a[140:143], v[244:247]\n"
    "v_mfma_f32_16x16x32_bf16 v[240:243], a[144:147], a[140:143], v[240:243]\n"
    "s_waitcnt lgkmcnt(0)\n"
    "buffer_load_dwordx4 v8, s[60:63], s82 offen offset:2048 sc1 lds\n"
    "v_mfma_f32_16x16x32_bf16 v[128:131], a[96:99], a[64:67], v[128:131]\n"
    "ds_read_b128 a[112:115], v13 offset:0\n"
    "v_mfma_f32_16x16x32_bf16 v[132:135], a[100:103], a[64:67], v[132:135]\n"
    "ds_read_b128 a[144:147], v14 offset:1024\n"
    "v_mfma_f32_16x16x32_bf16 v[136:139], a[104:107], a[64:67], v[136:139]\n"
    "v_mfma_f32_16x16x32_bf16 v[140:143], a[108:111], a[64:67], v[140:143]\n"
    "ds_read_b128 a[148:151], v14 offset:3072\n"
    "v_mfma_f32_16x16x32_bf16 v[156:159], a[108:111], a[68:71], v[156:159]\n"
    "ds_read_b128 a[152:155], v14 offset:5120\n"
    "buffer_load_dwordx4 v9, s[60:63], s83 offen offset:3072 sc1 lds\n"
    "v_mfma_f32_16x16x32_bf16 v[152:155], a[104:107], a[68:71], v[152:155]\n"
    "v_mfma_f32_16x16x32_bf16 v[148:151], a[100:103], a[68:71], v[148:151]\n"
    "ds_read_b128 a[156:159], v14 offset:7168\n"
    "v_mfma_f32_16x16x32_bf16 v[144:147], a[96:99], a[68:71], v[144:147]\n"
    "ds_read_b128 a[116:119], v13 offset:2048\n"
    "v_mfma_f32_16x16x32_bf16 v[160:163], a[96:99], a[72:75], v[160:163]\n"
    "s_add_u32 m0, s98, 32768\n"
    "v_mfma_f32_16x16x32_bf16 v[164:167], a[100:103], a[72:75], v[164:167]\n"
    "ds_read_b128 a[120:123], v13 offset:4096\n"
    "buffer_load_dwordx4 v10, s[64:67], 0 offen offset:0 sc1 lds\n"
    "v_mfma_f32_16x16x32_bf16 v[168:171], a[104:107], a[72:75], v[168:171]\n"
    "ds_read_b128 a[124:127], v13 offset:6144\n"
    "v_mfma_f32_16x16x32_bf16 v[172:175], a[108:111], a[72:75], v[172:175]\n"
    "v_mfma_f32_16x16x32_bf16 v[188:191], a[108:111], a[76:79], v[188:191]\n"
    "ds_read_b128 a[128:131], v13 offset:8192\n"
    "v_mfma_f32_16x16x32_bf16 v[184:187], a[104:107], a[76:79], v[184:187]\n"
    "ds_read_b128 a[132:135], v13 offset:10240\n"
    "v_mfma_f32_16x16x32_bf16 v[180:183], a[100:103], a[76:79], v[180:183]\n"
    "buffer_load_dwordx4 v10, s[64:67], 0 offen offset:1024 sc1 lds\n"
    "v_mfma_f32_16x16x32_bf16 v[176:179], a[96:99], a[76:79], v[176:179]\n"
    "ds_read_b128 a[136:139], v13 offset:12288\n"
    "v_mfma_f32_16x16x32_bf16 v[192:195], a[96:99], a[80:83], v[192:195]\n"
    "ds_read_b128 a[140:143], v13 offset:14336\n"
    "v_mfma_f32_16x16x32_bf16 v[196:199], a[100:103], a[80:83], v[196:199]\n"
    "v_add_u32 v12, s96, v12\n"
    "v_mfma_f32_16x16x32_bf16 v[200:203], a[104:107], a[80:83], v[200:203]\n"
    "v_add_u32 v13, s96, v13\n"
    "v_mfma_f32_16x16x32_bf16 v[204:207], a[108:111], a[80:83], v[204:207]\n"
    "buffer_load_dwordx4 v10, s[64:67], 0 offen offset:2048 sc1 lds\n"
    "v_mfma_f32_16x16x32_bf16 v[220:223], a[108:111], a[84:87], v[220:223]\n"
    "v_add_u32 v14, s96, v14\n"
    "v_mfma_f32_16x16x32_bf16 v[216:219], a[104:107], a[84:87], v[216:219]\n"
    "v_mfma_f32_16x16x32_bf16 v[212:215], a[100:103], a[84:87], v[212:215]\n"
    "v_mfma_f32_16x16x32_bf16 v[208:211], a[96:99], a[84:87], v[208:211]\n"
    "v_mfma_f32_16x16x32_bf16 v[224:227], a[96:99], a[88:91], v[224:227]\n"
    "buffer_load_dwordx4 v10, s[64:67], 0 offen offset:3072 sc1 lds\n"
    "v_mfma_f32_16x16x32_bf16 v[228:231], a[100:103], a[88:91], v[228:231]\n"
    "v_mfma_f32_16x16x32_bf16 v[232:235], a[104:107], a[88:91], v[232:235]\n"
    "v_mfma_f32_16x16x32_bf16 v[236:239], a[108:111], a[88:91], v[236:239]\n"
    "v_mfma_f32_16x16x32_bf16 v[252:255], a[108:111], a[92:95], v[252:255]\n"
    "s_add_u32 s60, s60, 256\n"
    "s_addc_u32 s61, s61, 0\n"
    "v_mfma_f32_16x16x32_bf16 v[248:251], a[104:107], a[92:95], v[248:251]\n"
    "s_add_u32 s64, s64, 32768\n"
    "s_addc_u32 s65, s65, 0\n"
    "v_mfma_f32_16x16x32_bf16 v[244:247], a[100:103], a[92:95], v[244:247]\n"
    "v_mfma_f32_16x16x32_bf16 v[240:243], a[96:99], a[92:95], v[240:243]\n"
    "s_waitcnt lgkmcnt(0) vmcnt(12)\n"
    "s_barrier\n"
    "ds_read_b128 a[64:67], v12 offset:0\n"
    "v_mfma_f32_16x16x32_bf16 v[128:131], a[144:147], a[112:115], v[128:131]\n"
    "ds_read_b128 a[96:99], v14 offset:0\n"
    "s_add_u32 m0, s92, 49152\n"
    "v_mfma_f32_16x16x32_bf16 v[132:135], a[148:151], a[112:115], v[132:135]\n"
    "buffer_load_dwordx4 v8, s[60:63], s76 offen offset:0 sc1 lds\n"
    "v_mfma_f32_16x16x32_bf16 v[136:139], a[152:155], a[112:115], v[136:139]\n"
    "ds_read_b128 a[100:103], v14 offset:2048\n"
    "v_mfma_f32_16x16x32_bf16 v[140:143], a[156:159], a[112:115], v[140:143]\n"
    "ds_read_b128 a[104:107], v14 offset:4096\n"
    "v_mfma_f32_16x16x32_bf16 v[156:159], a[156:159], a[116:119], v[156:159]\n"
    "v_mfma_f32_16x16x32_bf16 v[152:155], a[152:155], a[116:119], v[152:155]\n"
    "ds_read_b128 a[108:111], v14 offset:6144\n"
    "v_mfma_f32_16x16x32_bf16 v[148:151], a[148:151], a[116:119], v[148:151]\n"
    "ds_read_b128 a[68:71], v12 offset:2048\n"
    "buffer_load_dwordx4 v9, s[60:63], s77 offen offset:1024 sc1 lds\n"
    "v_mfma_f32_16x16x32_bf16 v[144:147], a[144:147], a[116:119], v[144:147]\n"
    "v_mfma_f32_16x16x32_bf16 v[160:163], a[144:147], a[120:123], v[160:163]\n"
    "ds_read_b128 a[72:75], v12 offset:4096\n"
    "v_mfma_f32_16x16x32_bf16 v[164:167], a[148:151], a[120:123], v[164:167]\n"
    "ds_read_b128 a[76:79], v12 offset:6144\n"
    "v_mfma_f32_16x16x32_bf16 v[168:171], a[152:155], a[120:123], v[168:171]\n"
    "v_mfma_f32_16x16x32_bf16 v[172:175], a[156:159], a[120:123], v[172:175]\n"
    "ds_read_b128 a[80:83], v12 offset:8192\n"
    "buffer_load_dwordx4 v8, s[60:63], s78 offen offset:2048 sc1 lds\n"
    "v_mfma_f32_16x16x32_bf16 v[188:191], a[156:159], a[124:127], v[188:191]\n"
    "ds_read_b128 a[84:87], v12 offset:10240\n"
    "v_mfma_f32_16x16x32_bf16 v[184:187], a[152:155], a[124:127], v[184:187]\n"
    "v_mfma_f32_16x16x32_bf16 v[180:183], a[148:151], a[124:127], v[180:183]\n"
    "ds_read_b128 a[88:91], v12 offset:12288\n"
    "v_mfma_f32_16x16x32_bf16 v[176:179], a[144:147], a[124:127], v[176:179]\n"
    "ds_read_b128 a[92:95], v12 offset:14336\n"
    "v_mfma_f32_16x16x32_bf16 v[192:195], a[144:147], a[128:131], v[192:195]\n"
    "buffer_load_dwordx4 v9, s[60:63], s79 offen offset:3072 sc1 lds\n"
    "v_mfma_f32_16x16x32_bf16 v[196:199], a[148:151], a[128:131], v[196:199]\n"
    "v_mfma_f32_16x16x32_bf16 v[200:203], a[152:155], a[128:131], v[200:203]\n"
    "v_mfma_f32_16x16x32_bf16 v[204:207], a[156:159], a[128:131], v[204:207]\n"
    "v_mfma_f32_16x16x32_bf16 v[220:223], a[156:159], a[132:135], v[220:223]\n"
    "s_add_u32 m0, s92, 53248\n"
    "v_mfma_f32_16x16x32_bf16 v[216:219], a[152:155], a[132:135], v[216:219]\n"
    "buffer_load_dwordx4 v8, s[60:63], s80 offen offset:0 sc1 lds\n"
    "v_mfma_f32_16x16x32_bf16 v[212:215], a[148:151], a[132:135], v[212:215]\n"
    "v_mfma_f32_16x16x32_bf16 v[208:211], a[144:147], a[132:135], v[208:211]\n"
    "v_mfma_f32_16x16x32_bf16 v[224:227], a[144:147], a[136:139], v[224:227]\n"
    "v_mfma_f32_16x16x32_bf16 v[228:231], a[148:151], a[136:139], v[228:231]\n"
    "v_mfma_f32_16x16x32_bf16 v[232:235], a[152:155], a[136:139], v[232:235]\n"
    "buffer_load_dwordx4 v9, s[60:63], s81 offen offset:1024 sc1 lds\n"
    "v_mfma_f32_16x16x32_bf16 v[236:239], a[156:159], a[136:139], v[236:239]\n"
    "v_mfma_f32_16x16x32_bf16 v[252:255], a[156:159], a[140:143], v[252:255]\n"
    "v_mfma_f32_16x16x32_bf16 v[248:251], a[152:155], a[140:143], v[248:251]\n"
    "v_mfma_f32_16x16x32_bf16 v[244:247], a[148:151], a[140:143], v[244:247]\n"
    "v_mfma_f32_16x16x32_bf16 v[240:243], a[144:147], a[140:143], v[240:243]\n"
    "s_waitcnt lgkmcnt(0)\n"
    "buffer_load_dwordx4 v8, s[60:63], s82 offen offset:2048 sc1 lds\n"
    "v_mfma_f32_16x16x32_bf16 v[128:131], a[96:99], a[64:67], v[128:131]\n"
    "ds_read_b128 a[112:115], v13 offset:0\n"
    "v_mfma_f32_16x16x32_bf16 v[132:135], a[100:103], a[64:67], v[132:135]\n"
    "ds_read_b128 a[144:147], v14 offset:1024\n"
    "v_mfma_f32_16x16x32_bf16 v[136:139], a[104:107], a[64:67], v[136:139]\n"
    "v_mfma_f32_16x16x32_bf16 v[140:143], a[108:111], a[64:67], v[140:143]\n"
    "ds_read_b128 a[148:151], v14 offset:3072\n"
    "v_mfma_f32_16x16x32_bf16 v[156:159], a[108:111], a[68:71], v[156:159]\n"
    "ds_read_b128 a[152:155], v14 offset:5120\n"
    "buffer_load_dwordx4 v9, s[60:63], s83 offen offset:3072 sc1 lds\n"
    "v_mfma_f32_16x16x32_bf16 v[152:155], a[104:107], a[68:71], v[152:155]\n"
    "v_mfma_f32_16x16x32_bf16 v[148:151], a[100:103], a[68:71], v[148:151]\n"
    "ds_read_b128 a[156:159], v14 offset:7168\n"
    "v_mfma_f32_16x16x32_bf16 v[144:147], a[96:99], a[68:71], v[144:147]\n"
    "ds_read_b128 a[116:119], v13 offset:2048\n"
    "v_mfma_f32_16x16x32_bf16 v[160:163], a[96:99], a[72:75], v[160:163]\n"
    "s_add_u32 m0, s98, 81920\n"
    "v_mfma_f32_16x16x32_bf16 v[164:167], a[100:103], a[72:75], v[164:167]\n"
    "ds_read_b128 a[120:123], v13 offset:4096\n"
    "buffer_load_dwordx4 v10, s[64:67], 0 offen offset:0 sc1 lds\n"
    "v_mfma_f32_16x16x32_bf16 v[168:171], a[104:107], a[72:75], v[168:171]\n"
    "ds_read_b128 a[124:127], v13 offset:6144\n"
    "v_mfma_f32_16x16x32_bf16 v[172:175], a[108:111], a[72:75], v[172:175]\n"
    "v_mfma_f32_16x16x32_bf16 v[188:191], a[108:111], a[76:79], v[188:191]\n"
    "ds_read_b128 a[128:131], v13 offset:8192\n"
    "v_mfma_f32_16x16x32_bf16 v[184:187], a[104:107], a[76:79], v[184:187]\n"
    "ds_read_b128 a[132:135], v13 offset:10240\n"
    "v_mfma_f32_16x16x32_bf16 v[180:183], a[100:103], a[76:79], v[180:183]\n"
    "buffer_load_dwordx4 v10, s[64:67], 0 offen offset:1024 sc1 lds\n"
    "v_mfma_f32_16x16x32_bf16 v[176:179], a[96:99], a[76:79], v[176:179]\n"
    "ds_read_b128 a[136:139], v13 offset:12288\n"
    "v_mfma_f32_16x16x32_bf16 v[192:195], a[96:99], a[80:83], v[192:195]\n"
    "ds_read_b128 a[140:143], v13 offset:14336\n"
    "v_mfma_f32_16x16x32_bf16 v[196:199], a[100:103], a[80:83], v[196:199]\n"
    "v_add_u32 v12, s97, v12\n"
    "v_mfma_f32_16x16x32_bf16 v[200:203], a[104:107], a[80:83], v[200:203]\n"
    "v_add_u32 v13, s97, v13\n"
    "v_mfma_f32_16x16x32_bf16 v[204:207], a[108:111], a[80:83], v[204:207]\n"
    "buffer_load_dwordx4 v10, s[64:67], 0 offen offset:2048 sc1 lds\n"
    "v_mfma_f32_16x16x32_bf16 v[220:223], a[108:111], a[84:87], v[220:223]\n"
    "v_add_u32 v14, s97, v14\n"
    "v_mfma_f32_16x16x32_bf16 v[216:219], a[104:107], a[84:87], v[216:219]\n"
    "v_mfma_f32_16x16x32_bf16 v[212:215], a[100:103], a[84:87], v[212:215]\n"
    "v_mfma_f32_16x16x32_bf16 v[208:211], a[96:99], a[84:87], v[208:211]\n"
    "v_mfma_f32_16x16x32_bf16 v[224:227], a[96:99], a[88:91], v[224:227]\n"
    "buffer_load_dwordx4 v10, s[64:67], 0 offen offset:3072 sc1 lds\n"
    "v_mfma_f32_16x16x32_bf16 v[228:231], a[100:103], a[88:91], v[228:231]\n"
    "v_mfma_f32_16x16x32_bf16 v[232:235], a[104:107], a[88:91], v[232:235]\n"
    "v_mfma_f32_16x16x32_bf16 v[236:239], a[108:111], a[88:91], v[236:239]\n"
    "v_mfma_f32_16x16x32_bf16 v[252:255], a[108:111], a[92:95], v[252:255]\n"
    "s_add_u32 s60, s60, 256\n"
    "s_addc_u32 s61, s61, 0\n"
    "v_mfma_f32_16x16x32_bf16 v[248:251], a[104:107], a[92:95], v[248:251]\n"
    "s_add_u32 s64, s64, 32768\n"
    "s_addc_u32 s65, s65, 0\n"
    "v_mfma_f32_16x16x32_bf16 v[244:247], a[100:103], a[92:95], v[244:247]\n"
    "v_mfma_f32_16x16x32_bf16 v[240:243], a[96:99], a[92:95], v[240:243]\n"
    "s_waitcnt lgkmcnt(0) vmcnt(12)\n"
    "s_barrier\n"
    "ds_read_b128 a[64:67], v12 offset:0\n"
    "v_mfma_f32_16x16x32_bf16 v[128:131], a[144:147], a[112:115], v[128:131]\n"
    "ds_read_b128 a[96:99], v14 offset:0\n"
    "s_add_u32 m0, s92, 98304\n"
    "v_mfma_f32_16x16x32_bf16 v[132:135], a[148:151], a[112:115], v[132:135]\n"
    "buffer_load_dwordx4 v8, s[60:63], s76 offen offset:0 sc1 lds\n"
    "v_mfma_f32_16x16x32_bf16 v[136:139], a[152:155], a[112:115], v[136:139]\n"
    "ds_read_b128 a[100:103], v14 offset:2048\n"
    "s_mul_i32 s94, s56, 1835008\n"
    "s_lshl_b32 s95, s58, 8\n"
    "v_mfma_f32_16x16x32_bf16 v[140:143], a[156:159], a[112:115], v[140:143]\n"
    "ds_read_b128 a[104:107], v14 offset:4096\n"
    "s_add_u32 s94, s94, s95\n"
    "s_add_u32 s72, s46, s94\n"
    "s_addc_u32 s73, s47, 0\n"
    "v_mfma_f32_16x16x32_bf16 v[156:159], a[156:159], a[116:119], v[156:159]\n"
    "s_and_b32 s73, s73, 0xffff\n"
    "s_mov_b32 s74, 129280\n"
    "s_mov_b32 s75, 0x20000\n"
    "v_mfma_f32_16x16x32_bf16 v[152:155], a[152:155], a[116:119], v[152:155]\n"
    "ds_read_b128 a[108:111], v14 offset:6144\n"
    "v_mfma_f32_16x16x32_bf16 v[148:151], a[148:151], a[116:119], v[148:151]\n"
    "ds_read_b128 a[68:71], v12 offset:2048\n"
    "buffer_load_dwordx4 v9, s[60:63], s77 offen offset:1024 sc1 lds\n"
    "v_mfma_f32_16x16x32_bf16 v[144:147], a[144:147], a[116:119], v[144:147]\n"
    "v_mfma_f32_16x16x32_bf16 v[160:163], a[144:147], a[120:123], v[160:163]\n"
    "ds_read_b128 a[72:75], v12 offset:4096\n"
    "v_mfma_f32_16x16x32_bf16 v[164:167], a[148:151], a[120:123], v[164:167]\n"
    "ds_read_b128 a[76:79], v12 offset:6144\n"
    "TC8:\n"
    "s_cbranch_execz TC12\n"
    "v_mfma_f32_16x16x32_bf16 v[168:171], a[152:155], a[120:123], v[168:171]\n"
    "v_mfma_f32_16x16x32_bf16 v[172:175], a[156:159], a[120:123], v[172:175]\n"
    "ds_read_b128 a[80:83], v12 offset:8192\n"
    "buffer_load_dwordx4 v8, s[60:63], s78 offen offset:2048 sc1 lds\n"
    "v_mfma_f32_16x16x32_bf16 v[188:191], a[156:159], a[124:127], v[188:191]\n"
    "ds_read_b128 a[84:87], v12 offset:10240\n"
    "v_mfma_f32_16x16x32_bf16 v[184:187], a[152:155], a[124:127], v[184:187]\n"
    "v_mfma_f32_16x16x32_bf16 v[180:183], a[148:151], a[124:127], v[180:183]\n"
    "ds_read_b128 a[88:91], v12 offset:12288\n"
    "v_mfma_f32_16x16x32_bf16 v[176:179], a[144:147], a[124:127], v[176:179]\n"
    "ds_read_b128 a[92:95], v12 offset:14336\n"
    "v_mfma_f32_16x16x32_bf16 v[192:195], a[144:147], a[128:131], v[192:195]\n"
    "buffer_load_dwordx4 v9, s[60:63], s79 offen offset:3072 sc1 lds\n"
    "v_mfma_f32_16x16x32_bf16 v[196:199], a[148:151], a[128:131], v[196:199]\n"
    "v_mfma_f32_16x16x32_bf16 v[200:203], a[152:155], a[128:131], v[200:203]\n"
    "v_mfma_f32_16x16x32_bf16 v[204:207], a[156:159], a[128:131], v[204:207]\n"
    "v_mfma_f32_16x16x32_bf16 v[220:223], a[156:159], a[132:135], v[220:223]\n"
    "s_add_u32 m0, s92, 102400\n"
    "v_mfma_f32_16x16x32_bf16 v[216:219], a[152:155], a[132:135], v[216:219]\n"
    "buffer_load_dwordx4 v8, s[60:63], s80 offen offset:0 sc1 lds\n"
    "v_mfma_f32_16x16x32_bf16 v[212:215], a[148:151], a[132:135], v[212:215]\n"
    "v_mfma_f32_16x16x32_bf16 v[208:211], a[144:147], a[132:135], v[208:211]\n"
    "v_mfma_f32_16x16x32_bf16 v[224:227], a[144:147], a[136:139], v[224:227]\n"
    "v_mfma_f32_16x16x32_bf16 v[228:231], a[148:151], a[136:139], v[228:231]\n"
    "v_mfma_f32_16x16x32_bf16 v[232:235], a[152:155], a[136:139], v[232:235]\n"
    "buffer_load_dwordx4 v9, s[60:63], s81 offen offset:1024 sc1 lds\n"
    "v_mfma_f32_16x16x32_bf16 v[236:239], a[156:159], a[136:139], v[236:239]\n"
    "v_mfma_f32_16x16x32_bf16 v[252:255], a[156:159], a[140:143], v[252:255]\n"
    "v_mfma_f32_16x16x32_bf16 v[248:251], a[152:155], a[140:143], v[248:251]\n"
    "v_mfma_f32_16x16x32_bf16 v[244:247], a[148:151], a[140:143], v[244:247]\n"
    "v_mfma_f32_16x16x32_bf16 v[240:243], a[144:147], a[140:143], v[240:243]\n"
    "s_waitcnt lgkmcnt(0)\n"
    "buffer_load_dwordx4 v8, s[60:63], s82 offen offset:2048 sc1 lds\n"
    "v_mfma_f32_16x16x32_bf16 v[128:131], a[96:99], a[64:67], v[128:131]\n"
    "ds_read_b128 a[112:115], v13 offset:0\n"
    "v_mfma_f32_16x16x32_bf16 v[132:135], a[100:103], a[64:67], v[132:135]\n"
    "ds_read_b128 a[144:147], v14 offset:1024\n"
    "v_mfma_f32_16x16x32_bf16 v[136:139], a[104:107], a[64:67], v[136:139]\n"
    "v_mfma_f32_16x16x32_bf16 v[140:143], a[108:111], a[64:67], v[140:143]\n"
    "ds_read_b128 a[148:151], v14 offset:3072\n"
    "v_mfma_f32_16x16x32_bf16 v[156:159], a[108:111], a[68:71], v[156:159]\n"
    "ds_read_b128 a[152:155], v14 offset:5120\n"
    "buffer_load_dwordx4 v9, s[60:63], s83 offen offset:3072 sc1 lds\n"
    "v_mfma_f32_16x16x32_bf16 v[152:155], a[104:107], a[68:71], v[152:155]\n"
    "v_mfma_f32_16x16x32_bf16 v[148:151], a[100:103], a[68:71], v[148:151]\n"
    "ds_read_b128 a[156:159], v14 offset:7168\n"
    "v_mfma_f32_16x16x32_bf16 v[144:147], a[96:99], a[68:71], v[144:147]\n"
    "ds_read_b128 a[116:119], v13 offset:2048\n"
    "v_mfma_f32_16x16x32_bf16 v[160:163], a[96:99], a[72:75], v[160:163]\n"
    "s_add_u32 m0, s98, 131072\n"
    "v_mfma_f32_16x16x32_bf16 v[164:167], a[100:103], a[72:75], v[164:167]\n"
    "ds_read_b128 a[120:123], v13 offset:4096\n"
    "buffer_load_dwordx4 v10, s[64:67], 0 offen offset:0 sc1 lds\n"
    "v_mfma_f32_16x16x32_bf16 v[168:171], a[104:107], a[72:75], v[168:171]\n"
    "ds_read_b128 a[124:127], v13 offset:6144\n"
    "v_mfma_f32_16x16x32_bf16 v[172:175], a[108:111], a[72:75], v[172:175]\n"
    "v_mfma_f32_16x16x32_bf16 v[188:191], a[108:111], a[76:79], v[188:191]\n"
    "ds_read_b128 a[128:131], v13 offset:8192\n"
    "v_mfma_f32_16x16x32_bf16 v[184:187], a[104:107], a[76:79], v[184:187]\n"
    "ds_read_b128 a[132:135], v13 offset:10240\n"
    "v_mfma_f32_16x16x32_bf16 v[180:183], a[100:103], a[76:79], v[180:183]\n"
    "buffer_load_dwordx4 v10, s[64:67], 0 offen offset:1024 sc1 lds\n"
    "v_mfma_f32_16x16x32_bf16 v[176:179], a[96:99], a[76:79], v[176:179]\n"
    "ds_read_b128 a[136:139], v13 offset:12288\n"
    "v_mfma_f32_16x16x32_bf16 v[192:195], a[96:99], a[80:83], v[192:195]\n"
    "ds_read_b128 a[140:143], v13 offset:14336\n"
    "v_mfma_f32_16x16x32_bf16 v[196:199], a[100:103], a[80:83], v[196:199]\n"
    "v_add_u32 v12, s96, v12\n"
    "v_mfma_f32_16x16x32_bf16 v[200:203], a[104:107], a[80:83], v[200:203]\n"
    "v_add_u32 v13, s96, v13\n"
    "v_mfma_f32_16x16x32_bf16 v[204:207], a[108:111], a[80:83], v[204:207]\n"
    "buffer_load_dwordx4 v10, s[64:67], 0 offen offset:2048 sc1 lds\n"
    "v_mfma_f32_16x16x32_bf16 v[220:223], a[108:111], a[84:87], v[220:223]\n"
    "v_add_u32 v14, s96, v14\n"
    "v_mfma_f32_16x16x32_bf16 v[216:219], a[104:107], a[84:87], v[216:219]\n"
    "v_mfma_f32_16x16x32_bf16 v[212:215], a[100:103], a[84:87], v[212:215]\n"
    "v_mfma_f32_16x16x32_bf16 v[208:211], a[96:99], a[84:87], v[208:211]\n"
    "v_mfma_f32_16x16x32_bf16 v[224:227], a[96:99], a[88:91], v[224:227]\n"
    "buffer_load_dwordx4 v10, s[64:67], 0 offen offset:3072 sc1 lds\n"
    "v_mfma_f32_16x16x32_bf16 v[228:231], a[100:103], a[88:91], v[228:231]\n"
    "v_mfma_f32_16x16x32_bf16 v[232:235], a[104:107], a[88:91], v[232:235]\n"
    "global_load_dwordx2 v[100:101], v98, s[52:53] sc1\n"
    "v_mfma_f32_16x16x32_bf16 v[236:239], a[108:111], a[88:91], v[236:239]\n"
    "v_mfma_f32_16x16x32_bf16 v[252:255], a[108:111], a[92:95], v[252:255]\n"
    "s_add_u32 s60, s60, 256\n"
    "s_addc_u32 s61, s61, 0\n"
    "v_mfma_f32_16x16x32_bf16 v[248:251], a[104:107], a[92:95], v[248:251]\n"
    "s_add_u32 s64, s64, 32768\n"
    "s_addc_u32 s65, s65, 0\n"
    "v_mfma_f32_16x16x32_bf16 v[244:247], a[100:103], a[92:95], v[244:247]\n"
    "v_mfma_f32_16x16x32_bf16 v[240:243], a[96:99], a[92:95], v[240:243]\n"
    "s_waitcnt lgkmcnt(0) vmcnt(13)\n"
    "s_barrier\n"
    "ds_read_b128 a[64:67], v12 offset:0\n"
    "v_mfma_f32_16x16x32_bf16 v[128:131], a[144:147], a[112:115], v[128:131]\n"
    "ds_read_b128 a[96:99], v14 offset:0\n"
    "s_add_u32 m0, s92, 0\n"
    "v_mfma_f32_16x16x32_bf16 v[132:135], a[148:151], a[112:115], v[132:135]\n"
    "buffer_load_dwordx4 v8, s[60:63], s76 offen offset:0 sc1 lds\n"
    "v_mfma_f32_16x16x32_bf16 v[136:139], a[152:155], a[112:115], v[136:139]\n"
    "ds_read_b128 a[100:103], v14 offset:2048\n"
    "v_mfma_f32_16x16x32_bf16 v[140:143], a[156:159], a[112:115], v[140:143]\n"
    "ds_read_b128 a[104:107], v14 offset:4096\n"
    "v_mfma_f32_16x16x32_bf16 v[156:159], a[156:159], a[116:119], v[156:159]\n"
    "v_mfma_f32_16x16x32_bf16 v[152:155], a[152:155], a[116:119], v[152:155]\n"
    "ds_read_b128 a[108:111], v14 offset:6144\n"
    "v_mfma_f32_16x16x32_bf16 v[148:151], a[148:151], a[116:119], v[148:151]\n"
    "ds_read_b128 a[68:71], v12 offset:2048\n"
    "buffer_load_dwordx4 v9, s[60:63], s77 offen offset:1024 sc1 lds\n"
    "v_mfma_f32_16x16x32_bf16 v[144:147], a[144:147], a[116:119], v[144:147]\n"
    "v_mfma_f32_16x16x32_bf16 v[160:163], a[144:147], a[120:123], v[160:163]\n"
    "ds_read_b128 a[72:75], v12 offset:4096\n"
    "v_mfma_f32_16x16x32_bf16 v[164:167], a[148:151], a[120:123], v[164:167]\n"
    "ds_read_b128 a[76:79], v12 offset:6144\n"
    "v_mfma_f32_16x16x32_bf16 v[168:171], a[152:155], a[120:123], v[168:171]\n"
    "v_mfma_f32_16x16x32_bf16 v[172:175], a[156:159], a[120:123], v[172:175]\n"
    "ds_read_b128 a[80:83], v12 offset:8192\n"
    "buffer_load_dwordx4 v8, s[60:63], s78 offen offset:2048 sc1 lds\n"
    "v_mfma_f32_16x16x32_bf16 v[188:191], a[156:159], a[124:127], v[188:191]\n"
    "ds_read_b128 a[84:87], v12 offset:10240\n"
    "v_mfma_f32_16x16x32_bf16 v[184:187], a[152:155], a[124:127], v[184:187]\n"
    "v_mfma_f32_16x16x32_bf16 v[180:183], a[148:151], a[124:127], v[180:183]\n"
    "ds_read_b128 a[88:91], v12 offset:12288\n"
    "v_mfma_f32_16x16x32_bf16 v[176:179], a[144:147], a[124:127], v[176:179]\n"
    "ds_read_b128 a[92:95], v12 offset:14336\n"
    "v_mfma_f32_16x16x32_bf16 v[192:195], a[144:147], a[128:131], v[192:195]\n"
    "buffer_load_dwordx4 v9, s[60:63], s79 offen offset:3072 sc1 lds\n"
    "v_mfma_f32_16x16x32_bf16 v[196:199], a[148:151], a[128:131], v[196:199]\n"
    "v_mfma_f32_16x16x32_bf16 v[200:203], a[152:155], a[128:131], v[200:203]\n"
    "v_mfma_f32_16x16x32_bf16 v[204:207], a[156:159], a[128:131], v[204:207]\n"
    "v_mfma_f32_16x16x32_bf16 v[220:223], a[156:159], a[132:135], v[220:223]\n"
    "s_add_u32 m0, s92, 4096\n"
    "v_mfma_f32_16x16x32_bf16 v[216:219], a[152:155], a[132:135], v[216:219]\n"
    "buffer_load_dwordx4 v8, s[60:63], s80 offen offset:0 sc1 lds\n"
    "v_mfma_f32_16x16x32_bf16 v[212:215], a[148:151], a[132:135], v[212:215]\n"
    "v_mfma_f32_16x16x32_bf16 v[208:211], a[144:147], a[132:135], v[208:211]\n"
    "v_mfma_f32_16x16x32_bf16 v[224:227], a[144:147], a[136:139], v[224:227]\n"
    "v_mfma_f32_16x16x32_bf16 v[228:231], a[148:151], a[136:139], v[228:231]\n"
    "v_mfma_f32_16x16x32_bf16 v[232:235], a[152:155], a[136:139], v[232:235]\n"
    "buffer_load_dwordx4 v9, s[60:63], s81 offen offset:1024 sc1 lds\n"
    "v_mfma_f32_16x16x32_bf16 v[236:239], a[156:159], a[136:139], v[236:239]\n"
    "v_mfma_f32_16x16x32_bf16 v[252:255], a[156:159], a[140:143], v[252:255]\n"
    "v_mfma_f32_16x16x32_bf16 v[248:251], a[152:155], a[140:143], v[248:251]\n"
    "v_mfma_f32_16x16x32_bf16 v[244:247], a[148:151], a[140:143], v[244:247]\n"
    "v_mfma_f32_16x16x32_bf16 v[240:243], a[144:147], a[140:143], v[240:243]\n"
    "s_waitcnt lgkmcnt(0)\n"
    "buffer_load_dwordx4 v8, s[60:63], s82 offen offset:2048 sc1 lds\n"
    "v_mfma_f32_16x16x32_bf16 v[128:131], a[96:99], a[64:67], v[128:131]\n"
    "ds_read_b128 a[112:115], v13 offset:0\n"
    "v_mfma_f32_16x16x32_bf16 v[132:135], a[100:103], a[64:67], v[132:135]\n"
    "ds_read_b128 a[144:147], v14 offset:1024\n"
    "v_mfma_f32_16x16x32_bf16 v[136:139], a[104:107], a[64:67], v[136:139]\n"
    "v_mfma_f32_16x16x32_bf16 v[140:143], a[108:111], a[64:67], v[140:143]\n"
    "ds_read_b128 a[148:151], v14 offset:3072\n"
    "v_mfma_f32_16x16x32_bf16 v[156:159], a[108:111], a[68:71], v[156:159]\n"
    "ds_read_b128 a[152:155], v14 offset:5120\n"
    "buffer_load_dwordx4 v9, s[60:63], s83 offen offset:3072 sc1 lds\n"
    "v_mfma_f32_16x16x32_bf16 v[152:155], a[104:107], a[68:71], v[152:155]\n"
    "v_mfma_f32_16x16x32_bf16 v[148:151], a[100:103], a[68:71], v[148:151]\n"
    "ds_read_b128 a[156:159], v14 offset:7168\n"
    "v_mfma_f32_16x16x32_bf16 v[144:147], a[96:99], a[68:71], v[144:147]\n"
    "ds_read_b128 a[116:119], v13 offset:2048\n"
    "v_mfma_f32_16x16x32_bf16 v[160:163], a[96:99], a[72:75], v[160:163]\n"
    "s_add_u32 m0, s98, 32768\n"
    "v_mfma_f32_16x16x32_bf16 v[164:167], a[100:103], a[72:75], v[164:167]\n"
    "ds_read_b128 a[120:123], v13 offset:4096\n"
    "buffer_load_dwordx4 v10, s[64:67], 0 offen offset:0 sc1 lds\n"
    "v_mfma_f32_16x16x32_bf16 v[168:171], a[104:107], a[72:75], v[168:171]\n"
    "ds_read_b128 a[124:127], v13 offset:6144\n"
    "v_mfma_f32_16x16x32_bf16 v[172:175], a[108:111], a[72:75], v[172:175]\n"
    "v_mfma_f32_16x16x32_bf16 v[188:191], a[108:111], a[76:79], v[188:191]\n"
    "ds_read_b128 a[128:131], v13 offset:8192\n"
    "v_mfma_f32_16x16x32_bf16 v[184:187], a[104:107], a[76:79], v[184:187]\n"
    "ds_read_b128 a[132:135], v13 offset:10240\n"
    "v_mfma_f32_16x16x32_bf16 v[180:183], a[100:103], a[76:79], v[180:183]\n"
    "buffer_load_dwordx4 v10, s[64:67], 0 offen offset:1024 sc1 lds\n"
    "v_mfma_f32_16x16x32_bf16 v[176:179], a[96:99], a[76:79], v[176:179]\n"
    "ds_read_b128 a[136:139], v13 offset:12288\n"
    "v_mfma_f32_16x16x32_bf16 v[192:195], a[96:99], a[80:83], v[192:195]\n"
    "ds_read_b128 a[140:143], v13 offset:14336\n"
    "v_mfma_f32_16x16x32_bf16 v[196:199], a[100:103], a[80:83], v[196:199]\n"
    "v_add_u32 v12, s96, v12\n"
    "v_mfma_f32_16x16x32_bf16 v[200:203], a[104:107], a[80:83], v[200:203]\n"
    "v_add_u32 v13, s96, v13\n"
    "v_mfma_f32_16x16x32_bf16 v[204:207], a[108:111], a[80:83], v[204:207]\n"
    "buffer_load_dwordx4 v10, s[64:67], 0 offen offset:2048 sc1 lds\n"
    "v_mfma_f32_16x16x32_bf16 v[220:223], a[108:111], a[84:87], v[220:223]\n"
    "v_add_u32 v14, s96, v14\n"
    "v_mfma_f32_16x16x32_bf16 v[216:219], a[104:107], a[84:87], v[216:219]\n"
    "v_mfma_f32_16x16x32_bf16 v[212:215], a[100:103], a[84:87], v[212:215]\n"
    "v_mfma_f32_16x16x32_bf16 v[208:211], a[96:99], a[84:87], v[208:211]\n"
    "v_mfma_f32_16x16x32_bf16 v[224:227], a[96:99], a[88:91], v[224:227]\n"
    "buffer_load_dwordx4 v10, s[64:67], 0 offen offset:3072 sc1 lds\n"
    "v_mfma_f32_16x16x32_bf16 v[228:231], a[100:103], a[88:91], v[228:231]\n"
    "v_mfma_f32_16x16x32_bf16 v[232:235], a[104:107], a[88:91], v[232:235]\n"
    "s_waitcnt vmcnt(12)\n"
    "v_mfma_f32_16x16x32_bf16 v[236:239], a[108:111], a[88:91], v[236:239]\n"
    "v_cmp_ne_u64 vcc, s[8:9], v[100:101]\n"
    "v_mfma_f32_16x16x32_bf16 v[252:255], a[108:111], a[92:95], v[252:255]\n"
    "s_cbranch_vccnz HP_slow\n"
    "HP_back:\n"
    "v_mfma_f32_16x16x32_bf16 v[248:251], a[104:107], a[92:95], v[248:251]\n"
    "v_mfma_f32_16x16x32_bf16 v[244:247], a[100:103], a[92:95], v[244:247]\n"
    "v_mfma_f32_16x16x32_bf16 v[240:243], a[96:99], a[92:95], v[240:243]\n"
    "s_waitcnt lgkmcnt(0) vmcnt(12)\n"
    "s_barrier\n"
    "ds_read_b128 a[64:67], v12 offset:0\n"
    "v_mfma_f32_16x16x32_bf16 v[128:131], a[144:147], a[112:115], v[128:131]\n"
    "ds_read_b128 a[96:99], v14 offset:0\n"
    "s_add_u32 m0, s92, 49152\n"
    "v_mfma_f32_16x16x32_bf16 v[132:135], a[148:151], a[112:115], v[132:135]\n"
    "buffer_load_dwordx4 v21, s[12:15], s84 offen offset:0 sc1 lds\n"
    "v_mfma_f32_16x16x32_bf16 v[136:139], a[152:155], a[112:115], v[136:139]\n"
    "ds_read_b128 a[100:103], v14 offset:2048\n"
    "s_cmp_lt_u32 s56, 2\n"
    "s_cselect_b32 s94, 0, 0x80000000\n"
    "v_mfma_f32_16x16x32_bf16 v[140:143], a[156:159], a[112:115], v[140:143]\n"
    "ds_read_b128 a[104:107], v14 offset:4096\n"
    "v_mfma_f32_16x16x32_bf16 v[156:159], a[156:159], a[116:119], v[156:159]\n"
    "v_rcp_f32 v22, v22\n"
    "v_mfma_f32_16x16x32_bf16 v[152:155], a[152:155], a[116:119], v[152:155]\n"
    "ds_read_b128 a[108:111], v14 offset:6144\n"
    "v_accvgpr_read_b32 v1, a200\n"
    "v_mfma_f32_16x16x32_bf16 v[148:151], a[148:151], a[116:119], v[148:151]\n"
    "ds_read_b128 a[68:71], v12 offset:2048\n"
    "buffer_load_dwordx4 v21, s[12:15], s84 offen offset:1024 sc1 lds\n"
    "v_mfma_f32_16x16x32_bf16 v[144:147], a[144:147], a[116:119], v[144:147]\n"
    "v_xor_b32 v1, s94, v1\n"
    "v_mul_f32 v22, v1, v22\n"
    "v_mfma_f32_16x16x32_bf16 v[160:163], a[144:147], a[120:123], v[160:163]\n"
    "ds_read_b128 a[72:75], v12 offset:4096\n"
    "v_mfma_f32_16x16x32_bf16 v[164:167], a[148:151], a[120:123], v[164:167]\n"
    "ds_read_b128 a[76:79], v12 offset:6144\n"
    "v_mfma_f32_16x16x32_bf16 v[168:171], a[152:155], a[120:123], v[168:171]\n"
    "v_rcp_f32 v23, v23\n"
    "v_mfma_f32_16x16x32_bf16 v[172:175], a[156:159], a[120:123], v[172:175]\n"
    "ds_read_b128 a[80:83], v12 offset:8192\n"
    "buffer_load_dwordx4 v21, s[12:15], s84 offen offset:2048 sc1 lds\n"
    "v_mfma_f32_16x16x32_bf16 v[188:191], a[156:159], a[124:127], v[188:191]\n"
    "ds_read_b128 a[84:87], v12 offset:10240\n"
    "v_accvgpr_read_b32 v1, a202\n"
    "v_mfma_f32_16x16x32_bf16 v[184:187], a[152:155], a[124:127], v[184:187]\n"
    "v_xor_b32 v1, s94, v1\n"
    "v_mul_f32 v23, v1, v23\n"
    "v_mfma_f32_16x16x32_bf16 v[180:183], a[148:151], a[124:127], v[180:183]\n"
    "ds_read_b128 a[88:91], v12 offset:12288\n"
    "v_mfma_f32_16x16x32_bf16 v[176:179], a[144:147], a[124:127], v[176:179]\n"
    "ds_read_b128 a[92:95], v12 offset:14336\n"
    "v_mfma_f32_16x16x32_bf16 v[192:195], a[144:147], a[128:131], v[192:195]\n"
    "buffer_load_dwordx4 v21, s[12:15], s84 offen offset:3072 sc1 lds\n"
    "v_mfma_f32_16x16x32_bf16 v[196:199], a[148:151], a[128:131], v[196:199]\n"
    "v_rcp_f32 v24, v24\n"
    "v_mfma_f32_16x16x32_bf16 v[200:203], a[152:155], a[128:131], v[200:203]\n"
    "v_accvgpr_read_b32 v1, a204\n"
    "v_xor_b32 v1, s94, v1\n"
    "v_mfma_f32_16x16x32_bf16 v[204:207], a[156:159], a[128:131], v[204:207]\n"
    "v_mul_f32 v24, v1, v24\n"
    "v_mfma_f32_16x16x32_bf16 v[220:223], a[156:159], a[132:135], v[220:223]\n"
    "s_add_u32 m0, s92, 53248\n"
    "v_mfma_f32_16x16x32_bf16 v[216:219], a[152:155], a[132:135], v[216:219]\n"
    "buffer_load_dwordx4 v21, s[12:15], s85 offen offset:0 sc1 lds\n"
    "v_mfma_f32_16x16x32_bf16 v[212:215], a[148:151], a[132:135], v[212:215]\n"
    "v_rcp_f32 v25, v25\n"
    "v_mfma_f32_16x16x32_bf16 v[208:211], a[144:147], a[132:135], v[208:211]\n"
    "v_accvgpr_read_b32 v1, a206\n"
    "v_xor_b32 v1, s94, v1\n"
    "v_mfma_f32_16x16x32_bf16 v[224:227], a[144:147], a[136:139], v[224:227]\n"
    "v_mul_f32 v25, v1, v25\n"
    "v_mfma_f32_16x16x32_bf16 v[228:231], a[148:151], a[136:139], v[228:231]\n"
    "v_mfma_f32_16x16x32_bf16 v[232:235], a[152:155], a[136:139], v[232:235]\n"
    "buffer_load_dwordx4 v21, s[12:15], s85 offen offset:1024 sc1 lds\n"
    "v_mfma_f32_16x16x32_bf16 v[236:239], a[156:159], a[136:139], v[236:239]\n"
    "v_mfma_f32_16x16x32_bf16 v[252:255], a[156:159], a[140:143], v[252:255]\n"
    "v_mfma_f32_16x16x32_bf16 v[248:251], a[152:155], a[140:143], v[248:251]\n"
    "v_mfma_f32_16x16x32_bf16 v[244:247], a[148:151], a[140:143], v[244:247]\n"
    "v_mfma_f32_16x16x32_bf16 v[240:243], a[144:147], a[140:143], v[240:243]\n"
    "s_waitcnt lgkmcnt(0)\n"
    "buffer_load_dwordx4 v21, s[12:15], s85 offen offset:2048 sc1 lds\n"
    "v_mfma_f32_16x16x32_bf16 v[128:131], a[96:99], a[64:67], v[128:131]\n"
    "ds_read_b128 a[112:115], v13 offset:0\n"
    "v_mfma_f32_16x16x32_bf16 v[132:135], a[100:103], a[64:67], v[132:135]\n"
    "ds_read_b128 a[144:147], v14 offset:1024\n"
    "v_mfma_f32_16x16x32_bf16 v[136:139], a[104:107], a[64:67], v[136:139]\n"
    "v_mfma_f32_16x16x32_bf16 v[140:143], a[108:111], a[64:67], v[140:143]\n"
    "ds_read_b128 a[148:151], v14 offset:3072\n"
    "v_mfma_f32_16x16x32_bf16 v[156:159], a[108:111], a[68:71], v[156:159]\n"
    "ds_read_b128 a[152:155], v14 offset:5120\n"
    "buffer_load_dwordx4 v21, s[12:15], s85 offen offset:3072 sc1 lds\n"
    "v_mfma_f32_16x16x32_bf16 v[152:155], a[104:107], a[68:71], v[152:155]\n"
    "v_mfma_f32_16x16x32_bf16 v[148:151], a[100:103], a[68:71], v[148:151]\n"
    "ds_read_b128 a[156:159], v14 offset:7168\n"
    "v_mfma_f32_16x16x32_bf16 v[144:147], a[96:99], a[68:71], v[144:147]\n"
    "ds_read_b128 a[116:119], v13 offset:2048\n"
    "v_mfma_f32_16x16x32_bf16 v[160:163], a[96:99], a[72:75], v[160:163]\n"
    "s_add_u32 m0, s98, 81920\n"
    "v_mfma_f32_16x16x32_bf16 v[164:167], a[100:103], a[72:75], v[164:167]\n"
    "ds_read_b128 a[120:123], v13 offset:4096\n"
    "buffer_load_dwordx4 v96, s[72:75], 0 offen offset:0 sc1 lds\n"
    "v_mfma_f32_16x16x32_bf16 v[168:171], a[104:107], a[72:75], v[168:171]\n"
    "ds_read_b128 a[124:127], v13 offset:6144\n"
    "v_mfma_f32_16x16x32_bf16 v[172:175], a[108:111], a[72:75], v[172:175]\n"
    "v_mfma_f32_16x16x32_bf16 v[188:191], a[108:111], a[76:79], v[188:191]\n"
    "ds_read_b128 a[128:131], v13 offset:8192\n"
    "v_mfma_f32_16x16x32_bf16 v[184:187], a[104:107], a[76:79], v[184:187]\n"
    "ds_read_b128 a[132:135], v13 offset:10240\n"
    "v_mfma_f32_16x16x32_bf16 v[180:183], a[100:103], a[76:79], v[180:183]\n"
    "buffer_load_dwordx4 v97, s[72:75], s86 offen offset:1024 sc1 lds\n"
    "v_mfma_f32_16x16x32_bf16 v[176:179], a[96:99], a[76:79], v[176:179]\n"
    "ds_read_b128 a[136:139], v13 offset:12288\n"
    "v_mfma_f32_16x16x32_bf16 v[192:195], a[96:99], a[80:83], v[192:195]\n"
    "ds_read_b128 a[140:143], v13 offset:14336\n"
    "v_mfma_f32_16x16x32_bf16 v[196:199], a[100:103], a[80:83], v[196:199]\n"
    "v_add_u32 v12, s97, v12\n"
    "v_mfma_f32_16x16x32_bf16 v[200:203], a[104:107], a[80:83], v[200:203]\n"
    "v_add_u32 v13, s97, v13\n"
    "v_mfma_f32_16x16x32_bf16 v[204:207], a[108:111], a[80:83], v[204:207]\n"
    "buffer_load_dwordx4 v96, s[72:75], s87 offen offset:2048 sc1 lds\n"
    "v_mfma_f32_16x16x32_bf16 v[220:223], a[108:111], a[84:87], v[220:223]\n"
    "v_add_u32 v14, s97, v14\n"
    "v_mfma_f32_16x16x32_bf16 v[216:219], a[104:107], a[84:87], v[216:219]\n"
    "v_mfma_f32_16x16x32_bf16 v[212:215], a[100:103], a[84:87], v[212:215]\n"
    "v_mfma_f32_16x16x32_bf16 v[208:211], a[96:99], a[84:87], v[208:211]\n"
    "v_mfma_f32_16x16x32_bf16 v[224:227], a[96:99], a[88:91], v[224:227]\n"
    "buffer_load_dwordx4 v97, s[72:75], s88 offen offset:3072 sc1 lds\n"
    "v_mfma_f32_16x16x32_bf16 v[228:231], a[100:103], a[88:91], v[228:231]\n"
    "v_mfma_f32_16x16x32_bf16 v[232:235], a[104:107], a[88:91], v[232:235]\n"
    "v_mfma_f32_16x16x32_bf16 v[236:239], a[108:111], a[88:91], v[236:239]\n"
    "v_mfma_f32_16x16x32_bf16 v[252:255], a[108:111], a[92:95], v[252:255]\n"
    "s_add_u32 s12, s12, 32768\n"
    "s_addc_u32 s13, s13, 0\n"
    "v_mfma_f32_16x16x32_bf16 v[248:251], a[104:107], a[92:95], v[248:251]\n"
    "s_add_u32 s72, s72, 131072\n"
    "s_addc_u32 s73, s73, 0\n"
    "v_mfma_f32_16x16x32_bf16 v[244:247], a[100:103], a[92:95], v[244:247]\n"
    "v_mfma_f32_16x16x32_bf16 v[240:243], a[96:99], a[92:95], v[240:243]\n"
    "s_waitcnt lgkmcnt(0) vmcnt(12)\n"
    "s_barrier\n"
    "ds_read_b128 a[64:67], v12 offset:0\n"
    "v_mfma_f32_16x16x32_bf16 v[128:131], a[144:147], a[112:115], v[128:131]\n"
    "ds_read_b128 a[96:99], v14 offset:0\n"
    "s_add_u32 m0, s92, 98304\n"
    "v_mfma_f32_16x16x32_bf16 v[132:135], a[148:151], a[112:115], v[132:135]\n"
    "buffer_load_dwordx4 v21, s[12:15], s84 offen offset:0 sc1 lds\n"
    "v_mfma_f32_16x16x32_bf16 v[136:139], a[152:155], a[112:115], v[136:139]\n"
    "ds_read_b128 a[100:103], v14 offset:2048\n"
    "s_cmp_lt_u32 s56, 2\n"
    "s_cselect_b32 s94, 0, 0x80000000\n"
    "v_mfma_f32_16x16x32_bf16 v[140:143], a[156:159], a[112:115], v[140:143]\n"
    "ds_read_b128 a[104:107], v14 offset:4096\n"
    "v_mfma_f32_16x16x32_bf16 v[156:159], a[156:159], a[116:119], v[156:159]\n"
    "v_rcp_f32 v26, v26\n"
    "v_mfma_f32_16x16x32_bf16 v[152:155], a[152:155], a[116:119], v[152:155]\n"
    "ds_read_b128 a[108:111], v14 offset:6144\n"
    "v_accvgpr_read_b32 v1, a208\n"
    "v_mfma_f32_16x16x32_bf16 v[148:151], a[148:151], a[116:119], v[148:151]\n"
    "ds_read_b128 a[68:71], v12 offset:2048\n"
    "buffer_load_dwordx4 v21, s[12:15], s84 offen offset:1024 sc1 lds\n"
    "v_mfma_f32_16x16x32_bf16 v[144:147], a[144:147], a[116:119], v[144:147]\n"
    "v_xor_b32 v1, s94, v1\n"
    "v_mul_f32 v26, v1, v26\n"
    "v_mfma_f32_16x16x32_bf16 v[160:163], a[144:147], a[120:123], v[160:163]\n"
    "ds_read_b128 a[72:75], v12 offset:4096\n"
    "v_mfma_f32_16x16x32_bf16 v[164:167], a[148:151], a[120:123], v[164:167]\n"
    "ds_read_b128 a[76:79], v12 offset:6144\n"
    "v_mfma_f32_16x16x32_bf16 v[168:171], a[152:155], a[120:123], v[168:171]\n"
    "v_rcp_f32 v27, v27\n"
    "v_mfma_f32_16x16x32_bf16 v[172:175], a[156:159], a[120:123], v[172:175]\n"
    "ds_read_b128 a[80:83], v12 offset:8192\n"
    "buffer_load_dwordx4 v21, s[12:15], s84 offen offset:2048 sc1 lds\n"
    "v_mfma_f32_16x16x32_bf16 v[188:191], a[156:159], a[124:127], v[188:191]\n"
    "ds_read_b128 a[84:87], v12 offset:10240\n"
    "v_accvgpr_read_b32 v1, a210\n"
    "v_mfma_f32_16x16x32_bf16 v[184:187], a[152:155], a[124:127], v[184:187]\n"
    "v_xor_b32 v1, s94, v1\n"
    "v_mul_f32 v27, v1, v27\n"
    "v_mfma_f32_16x16x32_bf16 v[180:183], a[148:151], a[124:127], v[180:183]\n"
    "ds_read_b128 a[88:91], v12 offset:12288\n"
    "v_mfma_f32_16x16x32_bf16 v[176:179], a[144:147], a[124:127], v[176:179]\n"
    "ds_read_b128 a[92:95], v12 offset:14336\n"
    "v_mfma_f32_16x16x32_bf16 v[192:195], a[144:147], a[128:131], v[192:195]\n"
    "buffer_load_dwordx4 v21, s[12:15], s84 offen offset:3072 sc1 lds\n"
    "v_mfma_f32_16x16x32_bf16 v[196:199], a[148:151], a[128:131], v[196:199]\n"
    "v_rcp_f32 v28, v28\n"
    "v_mfma_f32_16x16x32_bf16 v[200:203], a[152:155], a[128:131], v[200:203]\n"
    "v_accvgpr_read_b32 v1, a212\n"
    "v_xor_b32 v1, s94, v1\n"
    "v_mfma_f32_16x16x32_bf16 v[204:207], a[156:159], a[128:131], v[204:207]\n"
    "v_mul_f32 v28, v1, v28\n"
    "v_mfma_f32_16x16x32_bf16 v[220:223], a[156:159], a[132:135], v[220:223]\n"
    "s_add_u32 m0, s92, 102400\n"
    "v_mfma_f32_16x16x32_bf16 v[216:219], a[152:155], a[132:135], v[216:219]\n"
    "buffer_load_dwordx4 v21, s[12:15], s85 offen offset:0 sc1 lds\n"
    "v_mfma_f32_16x16x32_bf16 v[212:215], a[148:151], a[132:135], v[212:215]\n"
    "v_rcp_f32 v29, v29\n"
    "v_mfma_f32_16x16x32_bf16 v[208:211], a[144:147], a[132:135], v[208:211]\n"
    "v_accvgpr_read_b32 v1, a214\n"
    "v_xor_b32 v1, s94, v1\n"
    "v_mfma_f32_16x16x32_bf16 v[224:227], a[144:147], a[136:139], v[224:227]\n"
    "v_mul_f32 v29, v1, v29\n"
    "v_mfma_f32_16x16x32_bf16 v[228:231], a[148:151], a[136:139], v[228:231]\n"
    "v_mfma_f32_16x16x32_bf16 v[232:235], a[152:155], a[136:139], v[232:235]\n"
    "buffer_load_dwordx4 v21, s[12:15], s85 offen offset:1024 sc1 lds\n"
    "v_mfma_f32_16x16x32_bf16 v[236:239], a[156:159], a[136:139], v[236:239]\n"
    "v_mfma_f32_16x16x32_bf16 v[252:255], a[156:159], a[140:143], v[252:255]\n"
    "v_mfma_f32_16x16x32_bf16 v[248:251], a[152:155], a[140:143], v[248:251]\n"
    "v_mfma_f32_16x16x32_bf16 v[244:247], a[148:151], a[140:143], v[244:247]\n"
    "v_mfma_f32_16x16x32_bf16 v[240:243], a[144:147], a[140:143], v[240:243]\n"
    "s_waitcnt lgkmcnt(0)\n"
    "buffer_load_dwordx4 v21, s[12:15], s85 offen offset:2048 sc1 lds\n"
    "v_mfma_f32_16x16x32_bf16 v[128:131], a[96:99], a[64:67], v[128:131]\n"
    "ds_read_b128 a[112:115], v13 offset:0\n"
    "v_mfma_f32_16x16x32_bf16 v[132:135], a[100:103], a[64:67], v[132:135]\n"
    "ds_read_b128 a[144:147], v14 offset:1024\n"
    "v_mfma_f32_16x16x32_bf16 v[136:139], a[104:107], a[64:67], v[136:139]\n"
    "v_mfma_f32_16x16x32_bf16 v[140:143], a[108:111], a[64:67], v[140:143]\n"
    "ds_read_b128 a[148:151], v14 offset:3072\n"
    "v_mfma_f32_16x16x32_bf16 v[156:159], a[108:111], a[68:71], v[156:159]\n"
    "ds_read_b128 a[152:155], v14 offset:5120\n"
    "buffer_load_dwordx4 v21, s[12:15], s85 offen offset:3072 sc1 lds\n"
    "v_mfma_f32_16x16x32_bf16 v[152:155], a[104:107], a[68:71], v[152:155]\n"
    "v_mfma_f32_16x16x32_bf16 v[148:151], a[100:103], a[68:71], v[148:151]\n"
    "ds_read_b128 a[156:159], v14 offset:7168\n"
    "v_mfma_f32_16x16x32_bf16 v[144:147], a[96:99], a[68:71], v[144:147]\n"
    "ds_read_b128 a[116:119], v13 offset:2048\n"
    "v_mfma_f32_16x16x32_bf16 v[160:163], a[96:99], a[72:75], v[160:163]\n"
    "s_add_u32 m0, s98, 131072\n"
    "v_mfma_f32_16x16x32_bf16 v[164:167], a[100:103], a[72:75], v[164:167]\n"
    "ds_read_b128 a[120:123], v13 offset:4096\n"
    "buffer_load_dwordx4 v96, s[72:75], 0 offen offset:0 sc1 lds\n"
    "v_mfma_f32_16x16x32_bf16 v[168:171], a[104:107], a[72:75], v[168:171]\n"
    "ds_read_b128 a[124:127], v13 offset:6144\n"
    "v_mfma_f32_16x16x32_bf16 v[172:175], a[108:111], a[72:75], v[172:175]\n"
    "v_mfma_f32_16x16x32_bf16 v[188:191], a[108:111], a[76:79], v[188:191]\n"
    "ds_read_b128 a[128:131], v13 offset:8192\n"
    "v_mfma_f32_16x16x32_bf16 v[184:187], a[104:107], a[76:79], v[184:187]\n"
    "ds_read_b128 a[132:135], v13 offset:10240\n"
    "v_mfma_f32_16x16x32_bf16 v[180:183], a[100:103], a[76:79], v[180:183]\n"
    "buffer_load_dwordx4 v97, s[72:75], s86 offen offset:1024 sc1 lds\n"
    "v_mfma_f32_16x16x32_bf16 v[176:179], a[96:99], a[76:79], v[176:179]\n"
    "ds_read_b128 a[136:139], v13 offset:12288\n"
    "v_mfma_f32_16x16x32_bf16 v[192:195], a[96:99], a[80:83], v[192:195]\n"
    "ds_read_b128 a[140:143], v13 offset:14336\n"
    "v_mfma_f32_16x16x32_bf16 v[196:199], a[100:103], a[80:83], v[196:199]\n"
    "v_mfma_f32_16x16x32_bf16 v[200:203], a[104:107], a[80:83], v[200:203]\n"
    "v_mfma_f32_16x16x32_bf16 v[204:207], a[108:111], a[80:83], v[204:207]\n"
    "buffer_load_dwordx4 v96, s[72:75], s87 offen offset:2048 sc1 lds\n"
    "v_mfma_f32_16x16x32_bf16 v[220:223], a[108:111], a[84:87], v[220:223]\n"
    "v_mfma_f32_16x16x32_bf16 v[216:219], a[104:107], a[84:87], v[216:219]\n"
    "v_mfma_f32_16x16x32_bf16 v[212:215], a[100:103], a[84:87], v[212:215]\n"
    "v_mfma_f32_16x16x32_bf16 v[208:211], a[96:99], a[84:87], v[208:211]\n"
    "v_mfma_f32_16x16x32_bf16 v[224:227], a[96:99], a[88:91], v[224:227]\n"
    "buffer_load_dwordx4 v97, s[72:75], s88 offen offset:3072 sc1 lds\n"
    "v_mfma_f32_16x16x32_bf16 v[228:231], a[100:103], a[88:91], v[228:231]\n"
    "v_mfma_f32_16x16x32_bf16 v[232:235], a[104:107], a[88:91], v[232:235]\n"
    "v_mfma_f32_16x16x32_bf16 v[236:239], a[108:111], a[88:91], v[236:239]\n"
    "v_mfma_f32_16x16x32_bf16 v[252:255], a[108:111], a[92:95], v[252:255]\n"
    "s_add_u32 s12, s12, 32768\n"
    "s_addc_u32 s13, s13, 0\n"
    "v_mfma_f32_16x16x32_bf16 v[248:251], a[104:107], a[92:95], v[248:251]\n"
    "s_add_u32 s72, s72, 131072\n"
    "s_addc_u32 s73, s73, 0\n"
    "v_mfma_f32_16x16x32_bf16 v[244:247], a[100:103], a[92:95], v[244:247]\n"
    "v_mfma_f32_16x16x32_bf16 v[240:243], a[96:99], a[92:95], v[240:243]\n"
    "s_waitcnt lgkmcnt(0) vmcnt(12)\n"
    "s_barrier\n"
    "ds_read_b128 a[64:67], v15 offset:0\n"
    "v_mfma_f32_16x16x32_bf16 v[128:131], a[144:147], a[112:115], v[128:131]\n"
    "ds_read_b64_tr_b16 a[96:97], v16 offset:0\n"
    "s_add_u32 m0, s92, 0\n"
    "v_mfma_f32_16x16x32_bf16 v[132:135], a[148:151], a[112:115], v[132:135]\n"
    "buffer_load_dwordx4 v21, s[12:15], s84 offen offset:0 sc1 lds\n"
    "v_mfma_f32_16x16x32_bf16 v[136:139], a[152:155], a[112:115], v[136:139]\n"
    "ds_read_b64_tr_b16 a[98:99], v16 offset:4096\n"
    "v_mfma_f32_16x16x32_bf16 v[140:143], a[156:159], a[112:115], v[140:143]\n"
    "ds_read_b64_tr_b16 a[100:101], v17 offset:0\n"
    "v_mfma_f32_16x16x32_bf16 v[156:159], a[156:159], a[116:119], v[156:159]\n"
    "v_mfma_f32_16x16x32_bf16 v[152:155], a[152:155], a[116:119], v[152:155]\n"
    "ds_read_b64_tr_b16 a[102:103], v17 offset:4096\n"
    "v_mfma_f32_16x16x32_bf16 v[148:151], a[148:151], a[116:119], v[148:151]\n"
    "ds_read_b64_tr_b16 a[104:105], v18 offset:0\n"
    "buffer_load_dwordx4 v21, s[12:15], s84 offen offset:1024 sc1 lds\n"
    "v_mfma_f32_16x16x32_bf16 v[144:147], a[144:147], a[116:119], v[144:147]\n"
    "v_mfma_f32_16x16x32_bf16 v[160:163], a[144:147], a[120:123], v[160:163]\n"
    "ds_read_b64_tr_b16 a[106:107], v18 offset:4096\n"
    "v_mul_f32 v128, v22, v128\n"
    "v_mul_f32 v129, v22, v129\n"
    "v_mul_f32 v130, v22, v130\n"
    "v_mul_f32 v131, v22, v131\n"
    "v_mfma_f32_16x16x32_bf16 v[164:167], a[148:151], a[120:123], v[164:167]\n"
    "ds_read_b64_tr_b16 a[108:109], v19 offset:0\n"
    "v_mul_f32 v132, v22, v132\n"
    "v_mul_f32 v133, v22, v133\n"
    "v_mfma_f32_16x16x32_bf16 v[168:171], a[152:155], a[120:123], v[168:171]\n"
    "v_mul_f32 v134, v22, v134\n"
    "v_mul_f32 v135, v22, v135\n"
    "v_mfma_f32_16x16x32_bf16 v[172:175], a[156:159], a[120:123], v[172:175]\n"
    "ds_read_b64_tr_b16 a[110:111], v19 offset:4096\n"
    "buffer_load_dwordx4 v21, s[12:15], s84 offen offset:2048 sc1 lds\n"
    "v_mul_f32 v136, v22, v136\n"
    "v_mul_f32 v137, v22, v137\n"
    "v_mfma_f32_16x16x32_bf16 v[188:191], a[156:159], a[124:127], v[188:191]\n"
    "ds_read_b128 a[68:71], v15 offset:2048\n"
    "v_mul_f32 v138, v22, v138\n"
    "v_mul_f32 v139, v22, v139\n"
    "v_mul_f32 v140, v22, v140\n"
    "v_mul_f32 v141, v22, v141\n"
    "v_mfma_f32_16x16x32_bf16 v[184:187], a[152:155], a[124:127], v[184:187]\n"
    "v_mul_f32 v142, v22, v142\n"
    "v_mul_f32 v143, v22, v143\n"
    "v_mfma_f32_16x16x32_bf16 v[180:183], a[148:151], a[124:127], v[180:183]\n"
    "ds_read_b128 a[72:75], v15 offset:4096\n"
    "v_mul_f32 v156, v23, v156\n"
    "v_mul_f32 v157, v23, v157\n"
    "v_mfma_f32_16x16x32_bf16 v[176:179], a[144:147], a[124:127], v[176:179]\n"
    "ds_read_b128 a[76:79], v15 offset:6144\n"
    "v_mul_f32 v158, v23, v158\n"
    "v_mul_f32 v159, v23, v159\n"
    "v_mfma_f32_16x16x32_bf16 v[192:195], a[144:147], a[128:131], v[192:195]\n"
    "buffer_load_dwordx4 v21, s[12:15], s84 offen offset:3072 sc1 lds\n"
    "v_mul_f32 v152, v23, v152\n"
    "v_mul_f32 v153, v23, v153\n"
    "v_mul_f32 v154, v23, v154\n"
    "v_mul_f32 v155, v23, v155\n"
    "v_mfma_f32_16x16x32_bf16 v[196:199], a[148:151], a[128:131], v[196:199]\n"
    "ds_read_b128 a[80:83], v15 offset:8192\n"
    "v_mul_f32 v148, v23, v148\n"
    "v_mul_f32 v149, v23, v149\n"
    "v_mfma_f32_16x16x32_bf16 v[200:203], a[152:155], a[128:131], v[200:203]\n"
    "ds_read_b128 a[84:87], v15 offset:10240\n"
    "v_mul_f32 v150, v23, v150\n"
    "v_mul_f32 v151, v23, v151\n"
    "v_mfma_f32_16x16x32_bf16 v[204:207], a[156:159], a[128:131], v[204:207]\n"
    "v_mul_f32 v144, v23, v144\n"
    "v_mul_f32 v145, v23, v145\n"
    "v_mfma_f32_16x16x32_bf16 v[220:223], a[156:159], a[132:135], v[220:223]\n"
    "ds_read_b128 a[88:91], v15 offset:12288\n"
    "s_add_u32 m0, s92, 4096\n"
    "v_mul_f32 v146, v23, v146\n"
    "v_mul_f32 v147, v23, v147\n"
    "v_mul_f32 v160, v24, v160\n"
    "v_mul_f32 v161, v24, v161\n"
    "v_mfma_f32_16x16x32_bf16 v[216:219], a[152:155], a[132:135], v[216:219]\n"
    "ds_read_b128 a[92:95], v15 offset:14336\n"
    "buffer_load_dwordx4 v21, s[12:15], s85 offen offset:0 sc1 lds\n"
    "v_mul_f32 v162, v24, v162\n"
    "v_mul_f32 v163, v24, v163\n"
    "v_mfma_f32_16x16x32_bf16 v[212:215], a[148:151], a[132:135], v[212:215]\n"
    "v_mul_f32 v164, v24, v164\n"
    "v_mul_f32 v165, v24, v165\n"
    "v_mfma_f32_16x16x32_bf16 v[208:211], a[144:147], a[132:135], v[208:211]\n"
    "v_mul_f32 v166, v24, v166\n"
    "v_mul_f32 v167, v24, v167\n"
    "v_mfma_f32_16x16x32_bf16 v[224:227], a[144:147], a[136:139], v[224:227]\n"
    "v_mul_f32 v168, v24, v168\n"
    "v_mul_f32 v169, v24, v169\n"
    "v_mfma_f32_16x16x32_bf16 v[228:231], a[148:151], a[136:139], v[228:231]\n"
    "v_mul_f32 v170, v24, v170\n"
    "v_mul_f32 v171, v24, v171\n"
    "v_mul_f32 v172, v24, v172\n"
    "v_mul_f32 v173, v24, v173\n"
    "v_mfma_f32_16x16x32_bf16 v[232:235], a[152:155], a[136:139], v[232:235]\n"
    "buffer_load_dwordx4 v21, s[12:15], s85 offen offset:1024 sc1 lds\n"
    "v_mul_f32 v174, v24, v174\n"
    "v_mul_f32 v175, v24, v175\n"
    "v_mfma_f32_16x16x32_bf16 v[236:239], a[156:159], a[136:139], v[236:239]\n"
    "v_mul_f32 v188, v25, v188\n"
    "TC9:\n"
    "s_cbranch_execz TC13\n"
    "v_mul_f32 v189, v25, v189\n"
    "v_mfma_f32_16x16x32_bf16 v[252:255], a[156:159], a[140:143], v[252:255]\n"
    "v_mul_f32 v190, v25, v190\n"
    "v_mul_f32 v191, v25, v191\n"
    "v_mfma_f32_16x16x32_bf16 v[248:251], a[152:155], a[140:143], v[248:251]\n"
    "v_mul_f32 v184, v25, v184\n"
    "v_mul_f32 v185, v25, v185\n"
    "v_mul_f32 v186, v25, v186\n"
    "v_mul_f32 v187, v25, v187\n"
    "v_mfma_f32_16x16x32_bf16 v[244:247], a[148:151], a[140:143], v[244:247]\n"
    "v_mul_f32 v180, v25, v180\n"
    "v_mul_f32 v181, v25, v181\n"
    "v_mfma_f32_16x16x32_bf16 v[240:243], a[144:147], a[140:143], v[240:243]\n"
    "s_waitcnt lgkmcnt(0)\n"
    "buffer_load_dwordx4 v21, s[12:15], s85 offen offset:2048 sc1 lds\n"
    "v_mul_f32 v182, v25, v182\n"
    "v_mul_f32 v183, v25, v183\n"
    "v_mfma_f32_16x16x32_bf16 v[128:131], a[96:99], a[64:67], v[128:131]\n"
    "ds_read_b128 a[112:115], v15 offset:1024\n"
    "v_mul_f32 v176, v25, v176\n"
    "v_mul_f32 v177, v25, v177\n"
    "v_mfma_f32_16x16x32_bf16 v[132:135], a[100:103], a[64:67], v[132:135]\n"
    "ds_read_b64_tr_b16 a[144:145], v16 offset:8192\n"
    "v_mul_f32 v178, v25, v178\n"
    "v_mul_f32 v179, v25, v179\n"
    "v_mul_f32 v192, v26, v192\n"
    "v_mul_f32 v193, v26, v193\n"
    "v_mfma_f32_16x16x32_bf16 v[136:139], a[104:107], a[64:67], v[136:139]\n"
    "v_mul_f32 v194, v26, v194\n"
    "v_mul_f32 v195, v26, v195\n"
    "v_mfma_f32_16x16x32_bf16 v[140:143], a[108:111], a[64:67], v[140:143]\n"
    "ds_read_b64_tr_b16 a[146:147], v16 offset:12288\n"
    "v_mul_f32 v196, v26, v196\n"
    "v_mul_f32 v197, v26, v197\n"
    "v_mfma_f32_16x16x32_bf16 v[156:159], a[108:111], a[68:71], v[156:159]\n"
    "ds_read_b64_tr_b16 a[148:149], v17 offset:8192\n"
    "buffer_load_dwordx4 v21, s[12:15], s85 offen offset:3072 sc1 lds\n"
    "v_mul_f32 v198, v26, v198\n"
    "v_mul_f32 v199, v26, v199\n"
    "v_mfma_f32_16x16x32_bf16 v[152:155], a[104:107], a[68:71], v[152:155]\n"
    "v_mul_f32 v200, v26, v200\n"
    "v_mul_f32 v201, v26, v201\n"
    "v_mul_f32 v202, v26, v202\n"
    "v_mul_f32 v203, v26, v203\n"
    "v_mfma_f32_16x16x32_bf16 v[148:151], a[100:103], a[68:71], v[148:151]\n"
    "ds_read_b64_tr_b16 a[150:151], v17 offset:12288\n"
    "v_mul_f32 v204, v26, v204\n"
    "v_mul_f32 v205, v26, v205\n"
    "v_mfma_f32_16x16x32_bf16 v[144:147], a[96:99], a[68:71], v[144:147]\n"
    "ds_read_b64_tr_b16 a[152:153], v18 offset:8192\n"
    "v_mul_f32 v206, v26, v206\n"
    "v_mul_f32 v207, v26, v207\n"
    "v_mfma_f32_16x16x32_bf16 v[160:163], a[96:99], a[72:75], v[160:163]\n"
    "s_add_u32 m0, s98, 32768\n"
    "v_mul_f32 v220, v27, v220\n"
    "v_mul_f32 v221, v27, v221\n"
    "v_mfma_f32_16x16x32_bf16 v[164:167], a[100:103], a[72:75], v[164:167]\n"
    "ds_read_b64_tr_b16 a[154:155], v18 offset:12288\n"
    "buffer_load_dwordx4 v96, s[72:75], 0 offen offset:0 sc1 lds\n"
    "v_mul_f32 v222, v27, v222\n"
    "v_mul_f32 v223, v27, v223\n"
    "v_mfma_f32_16x16x32_bf16 v[168:171], a[104:107], a[72:75], v[168:171]\n"
    "ds_read_b64_tr_b16 a[156:157], v19 offset:8192\n"
    "v_mul_f32 v216, v27, v216\n"
    "v_mul_f32 v217, v27, v217\n"
    "v_mul_f32 v218, v27, v218\n"
    "v_mul_f32 v219, v27, v219\n"
    "v_mfma_f32_16x16x32_bf16 v[172:175], a[108:111], a[72:75], v[172:175]\n"
    "v_mul_f32 v212, v27, v212\n"
    "v_mul_f32 v213, v27, v213\n"
    "v_mfma_f32_16x16x32_bf16 v[188:191], a[108:111], a[76:79], v[188:191]\n"
    "ds_read_b64_tr_b16 a[158:159], v19 offset:12288\n"
    "v_mul_f32 v214, v27, v214\n"
    "v_mul_f32 v215, v27, v215\n"
    "v_mfma_f32_16x16x32_bf16 v[184:187], a[104:107], a[76:79], v[184:187]\n"
    "ds_read_b128 a[116:119], v15 offset:3072\n"
    "v_mul_f32 v208, v27, v208\n"
    "v_mul_f32 v209, v27, v209\n"
    "v_mfma_f32_16x16x32_bf16 v[180:183], a[100:103], a[76:79], v[180:183]\n"
    "buffer_load_dwordx4 v97, s[72:75], s86 offen offset:1024 sc1 lds\n"
    "v_mul_f32 v210, v27, v210\n"
    "v_mul_f32 v211, v27, v211\n"
    "v_mul_f32 v224, v28, v224\n"
    "v_mul_f32 v225, v28, v225\n"
    "v_mfma_f32_16x16x32_bf16 v[176:179], a[96:99], a[76:79], v[176:179]\n"
    "ds_read_b128 a[120:123], v15 offset:5120\n"
    "v_mul_f32 v226, v28, v226\n"
    "v_mul_f32 v227, v28, v227\n"
    "v_mfma_f32_16x16x32_bf16 v[192:195], a[96:99], a[80:83], v[192:195]\n"
    "ds_read_b128 a[124:127], v15 offset:7168\n"
    "v_mul_f32 v228, v28, v228\n"
    "v_mul_f32 v229, v28, v229\n"
    "v_mfma_f32_16x16x32_bf16 v[196:199], a[100:103], a[80:83], v[196:199]\n"
    "v_mul_f32 v230, v28, v230\n"
    "v_mul_f32 v231, v28, v231\n"
    "v_mfma_f32_16x16x32_bf16 v[200:203], a[104:107], a[80:83], v[200:203]\n"
    "ds_read_b128 a[128:131], v15 offset:9216\n"
    "v_mul_f32 v232, v28, v232\n"
    "v_mul_f32 v233, v28, v233\n"
    "v_mul_f32 v234, v28, v234\n"
    "v_mul_f32 v235, v28, v235\n"
    "v_mfma_f32_16x16x32_bf16 v[204:207], a[108:111], a[80:83], v[204:207]\n"
    "ds_read_b128 a[132:135], v15 offset:11264\n"
    "buffer_load_dwordx4 v96, s[72:75], s87 offen offset:2048 sc1 lds\n"
    "v_mul_f32 v236, v28, v236\n"
    "v_mul_f32 v237, v28, v237\n"
    "v_mfma_f32_16x16x32_bf16 v[220:223], a[108:111], a[84:87], v[220:223]\n"
    "v_mul_f32 v238, v28, v238\n"
    "v_mul_f32 v239, v28, v239\n"
    "v_mfma_f32_16x16x32_bf16 v[216:219], a[104:107], a[84:87], v[216:219]\n"
    "ds_read_b128 a[136:139], v15 offset:13312\n"
    "v_mul_f32 v252, v29, v252\n"
    "v_mul_f32 v253, v29, v253\n"
    "v_mfma_f32_16x16x32_bf16 v[212:215], a[100:103], a[84:87], v[212:215]\n"
    "ds_read_b128 a[140:143], v15 offset:15360\n"
    "v_mul_f32 v254, v29, v254\n"
    "v_mul_f32 v255, v29, v255\n"
    "v_mul_f32 v248, v29, v248\n"
    "v_mul_f32 v249, v29, v249\n"
    "v_mfma_f32_16x16x32_bf16 v[208:211], a[96:99], a[84:87], v[208:211]\n"
    "v_add_u32 v15, s96, v15\n"
    "v_mul_f32 v250, v29, v250\n"
    "v_mul_f32 v251, v29, v251\n"
    "v_mfma_f32_16x16x32_bf16 v[224:227], a[96:99], a[88:91], v[224:227]\n"
    "buffer_load_dwordx4 v97, s[72:75], s88 offen offset:3072 sc1 lds\n"
    "v_mul_f32 v244, v29, v244\n"
    "v_mul_f32 v245, v29, v245\n"
    "v_mfma_f32_16x16x32_bf16 v[228:231], a[100:103], a[88:91], v[228:231]\n"
    "v_add_u32 v16, s96, v16\n"
    "v_mul_f32 v246, v29, v246\n"
    "v_mul_f32 v247, v29, v247\n"
    "v_mfma_f32_16x16x32_bf16 v[232:235], a[104:107], a[88:91], v[232:235]\n"
    "v_add_u32 v17, s96, v17\n"
    "v_mul_f32 v240, v29, v240\n"
    "v_mul_f32 v241, v29, v241\n"
    "v_mfma_f32_16x16x32_bf16 v[236:239], a[108:111], a[88:91], v[236:239]\n"
    "v_add_u32 v18, s96, v18\n"
    "v_mul_f32 v242, v29, v242\n"
    "v_mul_f32 v243, v29, v243\n"
    "v_mfma_f32_16x16x32_bf16 v[252:255], a[108:111], a[92:95], v[252:255]\n"
    "v_add_u32 v19, s96, v19\n"
    "s_add_u32 s12, s12, 32768\n"
    "s_addc_u32 s13, s13, 0\n"
    "v_mfma_f32_16x16x32_bf16 v[248:251], a[104:107], a[92:95], v[248:251]\n"
    "s_add_u32 s72, s72, 131072\n"
    "s_addc_u32 s73, s73, 0\n"
    "v_mfma_f32_16x16x32_bf16 v[244:247], a[100:103], a[92:95], v[244:247]\n"
    "v_mfma_f32_16x16x32_bf16 v[240:243], a[96:99], a[92:95], v[240:243]\n"
    "s_waitcnt lgkmcnt(0) vmcnt(12)\n"
    "s_barrier\n"
    "ds_read_b128 a[64:67], v15 offset:0\n"
    "v_mfma_f32_16x16x32_bf16 v[128:131], a[144:147], a[112:115], v[128:131]\n"
    "ds_read_b64_tr_b16 a[96:97], v16 offset:0\n"
    "s_add_u32 m0, s92, 49152\n"
    "v_mfma_f32_16x16x32_bf16 v[132:135], a[148:151], a[112:115], v[132:135]\n"
    "buffer_load_dwordx4 v21, s[12:15], s84 offen offset:0 sc1 lds\n"
    "v_mfma_f32_16x16x32_bf16 v[136:139], a[152:155], a[112:115], v[136:139]\n"
    "ds_read_b64_tr_b16 a[98:99], v16 offset:4096\n"
    "s_lshl_b32 s69, s57, 3\n"
    "s_add_u32 s69, s69, s58\n"
    "v_mfma_f32_16x16x32_bf16 v[140:143], a[156:159], a[112:115], v[140:143]\n"
    "ds_read_b64_tr_b16 a[100:101], v17 offset:0\n"
    "s_mul_i32 s94, s69, 196608\n"
    "s_lshl_b32 s95, s55, 12\n"
    "v_mfma_f32_16x16x32_bf16 v[156:159], a[156:159], a[116:119], v[156:159]\n"
    "s_add_u32 s94, s94, s95\n"
    "s_add_u32 s94, s94, 25165824\n"
    "s_add_u32 s60, s52, s94\n"
    "s_addc_u32 s61, s53, 0\n"
    "v_mfma_f32_16x16x32_bf16 v[152:155], a[152:155], a[116:119], v[152:155]\n"
    "ds_read_b64_tr_b16 a[102:103], v17 offset:4096\n"
    "s_and_b32 s61, s61, 0xffff\n"
    "s_mov_b32 s62, 196608\n"
    "v_mfma_f32_16x16x32_bf16 v[148:151], a[148:151], a[116:119], v[148:151]\n"
    "ds_read_b64_tr_b16 a[104:105], v18 offset:0\n"
    "buffer_load_dwordx4 v21, s[12:15], s84 offen offset:1024 sc1 lds\n"
    "v_mfma_f32_16x16x32_bf16 v[144:147], a[144:147], a[116:119], v[144:147]\n"
    "s_mov_b32 s63, 0x20000\n"
    "s_mul_i32 s95, s56, 49152\n"
    "s_add_u32 s94, s56, 1\n"
    "v_mfma_f32_16x16x32_bf16 v[160:163], a[144:147], a[120:123], v[160:163]\n"
    "ds_read_b64_tr_b16 a[106:107], v18 offset:4096\n"
    "s_and_b32 s94, s94, 3\n"
    "s_mul_i32 s94, s94, 49152\n"
    "v_mfma_f32_16x16x32_bf16 v[164:167], a[148:151], a[120:123], v[164:167]\n"
    "ds_read_b64_tr_b16 a[108:109], v19 offset:0\n"
    "s_add_u32 s77, s94, 0\n"
    "s_add_u32 s81, s95, 0\n"
    "v_mfma_f32_16x16x32_bf16 v[168:171], a[152:155], a[120:123], v[168:171]\n"
    "s_add_u32 s94, s56, 2\n"
    "s_and_b32 s94, s94, 3\n"
    "s_mul_i32 s94, s94, 49152\n"
    "v_mfma_f32_16x16x32_bf16 v[172:175], a[156:159], a[120:123], v[172:175]\n"
    "ds_read_b64_tr_b16 a[110:111], v19 offset:4096\n"
    "buffer_load_dwordx4 v21, s[12:15], s84 offen offset:2048 sc1 lds\n"
    "v_mfma_f32_16x16x32_bf16 v[188:191], a[156:159], a[124:127], v[188:191]\n"
    "ds_read_b128 a[68:71], v15 offset:2048\n"
    "s_add_u32 s78, s94, 16384\n"
    "s_add_u32 s82, s95, 16384\n"
    "v_mfma_f32_16x16x32_bf16 v[184:187], a[152:155], a[124:127], v[184:187]\n"
    "s_add_u32 s94, s56, 3\n"
    "s_and_b32 s94, s94, 3\n"
    "s_mul_i32 s94, s94, 49152\n"
    "v_mfma_f32_16x16x32_bf16 v[180:183], a[148:151], a[124:127], v[180:183]\n"
    "ds_read_b128 a[72:75], v15 offset:4096\n"
    "s_add_u32 s79, s94, 32768\n"
    "s_add_u32 s83, s95, 32768\n"
    "v_mfma_f32_16x16x32_bf16 v[176:179], a[144:147], a[124:127], v[176:179]\n"
    "ds_read_b128 a[76:79], v15 offset:6144\n"
    "s_lshl_b32 s94, s69, 4\n"
    "s_add_u32 s94, s94, s55\n"
    "v_mfma_f32_16x16x32_bf16 v[192:195], a[144:147], a[128:131], v[192:195]\n"
    "buffer_load_dwordx4 v21, s[12:15], s84 offen offset:3072 sc1 lds\n"
    "v_mfma_f32_16x16x32_bf16 v[196:199], a[148:151], a[128:131], v[196:199]\n"
    "ds_read_b128 a[80:83], v15 offset:8192\n"
    "s_lshl_b32 s94, s94, 6\n"
    "s_add_u32 s94, s94, 37814272\n"
    "v_mfma_f32_16x16x32_bf16 v[200:203], a[152:155], a[128:131], v[200:203]\n"
    "ds_read_b128 a[84:87], v15 offset:10240\n"
    "v_lshl_add_u32 v7, v0, 3, s94\n"
    "v_mfma_f32_16x16x32_bf16 v[204:207], a[156:159], a[128:131], v[204:207]\n"
    "v_add3_u32 v1, v0, s56, 1\n"
    "v_and_b32 v1, 3, v1\n"
    "v_mfma_f32_16x16x32_bf16 v[220:223], a[156:159], a[132:135], v[220:223]\n"
    "ds_read_b128 a[88:91], v15 offset:12288\n"
    "s_add_u32 m0, s92, 53248\n"
    "v_mfma_f32_16x16x32_bf16 v[216:219], a[152:155], a[132:135], v[216:219]\n"
    "ds_read_b128 a[92:95], v15 offset:14336\n"
    "buffer_load_dwordx4 v21, s[12:15], s85 offen offset:0 sc1 lds\n"
    "v_mfma_f32_16x16x32_bf16 v[212:215], a[148:151], a[132:135], v[212:215]\n"
    "v_lshl_add_u32 v6, v1, 8, v7\n"
    "s_lshl_b32 s95, s56, 8\n"
    "v_mfma_f32_16x16x32_bf16 v[208:211], a[144:147], a[132:135], v[208:211]\n"
    "v_add_u32 v7, s95, v7\n"
    "v_mov_b32 v4, s8\n"
    "v_mfma_f32_16x16x32_bf16 v[224:227], a[144:147], a[136:139], v[224:227]\n"
    "v_mov_b32 v5, s9\n"
    "s_lshl_b32 s94, s56, 5\n"
    "v_mfma_f32_16x16x32_bf16 v[228:231], a[148:151], a[136:139], v[228:231]\n"
    "v_add_u32 v1, s94, v31\n"
    "v_bfe_u32 v2, v0, 4, 1\n"
    "v_mfma_f32_16x16x32_bf16 v[232:235], a[152:155], a[136:139], v[232:235]\n"
    "buffer_load_dwordx4 v21, s[12:15], s85 offen offset:1024 sc1 lds\n"
    "v_mfma_f32_16x16x32_bf16 v[236:239], a[156:159], a[136:139], v[236:239]\n"
    "v_lshrrev_b32 v3, 5, v0\n"
    "v_lshl_add_u32 v2, v2, 1, v3\n"
    "v_mfma_f32_16x16x32_bf16 v[252:255], a[156:159], a[140:143], v[252:255]\n"
    "v_lshlrev_b32 v2, 4, v2\n"
    "v_lshl_add_u32 v1, v1, 11, v2\n"
    "v_mfma_f32_16x16x32_bf16 v[248:251], a[152:155], a[140:143], v[248:251]\n"
    "s_lshl_b32 s94, s58, 1\n"
    "s_and_b32 s95, s55, 1\n"
    "s_add_u32 s94, s94, s95\n"
    "v_mfma_f32_16x16x32_bf16 v[244:247], a[148:151], a[140:143], v[244:247]\n"
    "s_lshl_b32 s94, s94, 7\n"
    "v_add_u32 v34, s94, v1\n"
    "v_mfma_f32_16x16x32_bf16 v[240:243], a[144:147], a[140:143], v[240:243]\n"
    "s_waitcnt lgkmcnt(0)\n"
    "buffer_load_dwordx4 v21, s[12:15], s85 offen offset:2048 sc1 lds\n"
    "v_mfma_f32_16x16x32_bf16 v[128:131], a[96:99], a[64:67], v[128:131]\n"
    "ds_read_b128 a[112:115], v15 offset:1024\n"
    "v_add_u32 v35, 8192, v34\n"
    "v_mfma_f32_16x16x32_bf16 v[132:135], a[100:103], a[64:67], v[132:135]\n"
    "ds_read_b64_tr_b16 a[144:145], v16 offset:8192\n"
    "v_lshlrev_b32 v8, 2, v21\n"
    "v_mfma_f32_16x16x32_bf16 v[136:139], a[104:107], a[64:67], v[136:139]\n"
    "v_mfma_f32_16x16x32_bf16 v[140:143], a[108:111], a[64:67], v[140:143]\n"
    "ds_read_b64_tr_b16 a[146:147], v16 offset:12288\n"
    "v_mfma_f32_16x16x32_bf16 v[156:159], a[108:111], a[68:71], v[156:159]\n"
    "ds_read_b64_tr_b16 a[148:149], v17 offset:8192\n"
    "buffer_load_dwordx4 v21, s[12:15], s85 offen offset:3072 sc1 lds\n"
    "v_mfma_f32_16x16x32_bf16 v[152:155], a[104:107], a[68:71], v[152:155]\n"
    "v_mfma_f32_16x16x32_bf16 v[148:151], a[100:103], a[68:71], v[148:151]\n"
    "ds_read_b64_tr_b16 a[150:151], v17 offset:12288\n"
    "v_mfma_f32_16x16x32_bf16 v[144:147], a[96:99], a[68:71], v[144:147]\n"
    "ds_read_b64_tr_b16 a[152:153], v18 offset:8192\n"
    "v_mfma_f32_16x16x32_bf16 v[160:163], a[96:99], a[72:75], v[160:163]\n"
    "s_add_u32 m0, s98, 81920\n"
    "v_mfma_f32_16x16x32_bf16 v[164:167], a[100:103], a[72:75], v[164:167]\n"
    "ds_read_b64_tr_b16 a[154:155], v18 offset:12288\n"
    "buffer_load_dwordx4 v96, s[72:75], 0 offen offset:0 sc1 lds\n"
    "v_mfma_f32_16x16x32_bf16 v[168:171], a[104:107], a[72:75], v[168:171]\n"
    "ds_read_b64_tr_b16 a[156:157], v19 offset:8192\n"
    "v_mfma_f32_16x16x32_bf16 v[172:175], a[108:111], a[72:75], v[172:175]\n"
    "v_mfma_f32_16x16x32_bf16 v[188:191], a[108:111], a[76:79], v[188:191]\n"
    "ds_read_b64_tr_b16 a[158:159], v19 offset:12288\n"
    "v_mfma_f32_16x16x32_bf16 v[184:187], a[104:107], a[76:79], v[184:187]\n"
    "ds_read_b128 a[116:119], v15 offset:3072\n"
    "v_mfma_f32_16x16x32_bf16 v[180:183], a[100:103], a[76:79], v[180:183]\n"
    "buffer_load_dwordx4 v97, s[72:75], s86 offen offset:1024 sc1 lds\n"
    "v_mfma_f32_16x16x32_bf16 v[176:179], a[96:99], a[76:79], v[176:179]\n"
    "ds_read_b128 a[120:123], v15 offset:5120\n"
    "v_mfma_f32_16x16x32_bf16 v[192:195], a[96:99], a[80:83], v[192:195]\n"
    "ds_read_b128 a[124:127], v15 offset:7168\n"
    "v_mfma_f32_16x16x32_bf16 v[196:199], a[100:103], a[80:83], v[196:199]\n"
    "v_mfma_f32_16x16x32_bf16 v[200:203], a[104:107], a[80:83], v[200:203]\n"
    "ds_read_b128 a[128:131], v15 offset:9216\n"
    "v_mfma_f32_16x16x32_bf16 v[204:207], a[108:111], a[80:83], v[204:207]\n"
    "ds_read_b128 a[132:135], v15 offset:11264\n"
    "buffer_load_dwordx4 v96, s[72:75], s87 offen offset:2048 sc1 lds\n"
    "v_mfma_f32_16x16x32_bf16 v[220:223], a[108:111], a[84:87], v[220:223]\n"
    "v_mfma_f32_16x16x32_bf16 v[216:219], a[104:107], a[84:87], v[216:219]\n"
    "ds_read_b128 a[136:139], v15 offset:13312\n"
    "v_mfma_f32_16x16x32_bf16 v[212:215], a[100:103], a[84:87], v[212:215]\n"
    "ds_read_b128 a[140:143], v15 offset:15360\n"
    "v_mfma_f32_16x16x32_bf16 v[208:211], a[96:99], a[84:87], v[208:211]\n"
    "v_add_u32 v15, s97, v15\n"
    "v_mfma_f32_16x16x32_bf16 v[224:227], a[96:99], a[88:91], v[224:227]\n"
    "buffer_load_dwordx4 v97, s[72:75], s88 offen offset:3072 sc1 lds\n"
    "v_mfma_f32_16x16x32_bf16 v[228:231], a[100:103], a[88:91], v[228:231]\n"
    "v_add_u32 v16, s97, v16\n"
    "v_mfma_f32_16x16x32_bf16 v[232:235], a[104:107], a[88:91], v[232:235]\n"
    "v_add_u32 v17, s97, v17\n"
    "v_mfma_f32_16x16x32_bf16 v[236:239], a[108:111], a[88:91], v[236:239]\n"
    "v_add_u32 v18, s97, v18\n"
    "v_mfma_f32_16x16x32_bf16 v[252:255], a[108:111], a[92:95], v[252:255]\n"
    "v_add_u32 v19, s97, v19\n"
    "s_add_u32 s12, s12, 32768\n"
    "s_addc_u32 s13, s13, 0\n"
    "v_mfma_f32_16x16x32_bf16 v[248:251], a[104:107], a[92:95], v[248:251]\n"
    "s_add_u32 s72, s72, 131072\n"
    "s_addc_u32 s73, s73, 0\n"
    "v_mfma_f32_16x16x32_bf16 v[244:247], a[100:103], a[92:95], v[244:247]\n"
    "v_mfma_f32_16x16x32_bf16 v[240:243], a[96:99], a[92:95], v[240:243]\n"
    "s_waitcnt lgkmcnt(0) vmcnt(12)\n"
    "s_barrier\n"
    "ds_read_b128 a[64:67], v15 offset:0\n"
    "v_mfma_f32_16x16x32_bf16 v[128:131], a[144:147], a[112:115], v[128:131]\n"
    "ds_read_b64_tr_b16 a[96:97], v16 offset:0\n"
    "s_add_u32 m0, s92, 98304\n"
    "v_mfma_f32_16x16x32_bf16 v[132:135], a[148:151], a[112:115], v[132:135]\n"
    "buffer_load_dwordx4 v21, s[12:15], s84 offen offset:0 sc1 lds\n"
    "v_mfma_f32_16x16x32_bf16 v[136:139], a[152:155], a[112:115], v[136:139]\n"
    "ds_read_b64_tr_b16 a[98:99], v16 offset:4096\n"
    "v_mfma_f32_16x16x32_bf16 v[140:143], a[156:159], a[112:115], v[140:143]\n"
    "ds_read_b64_tr_b16 a[100:101], v17 offset:0\n"
    "v_mfma_f32_16x16x32_bf16 v[156:159], a[156:159], a[116:119], v[156:159]\n"
    "v_mfma_f32_16x16x32_bf16 v[152:155], a[152:155], a[116:119], v[152:155]\n"
    "ds_read_b64_tr_b16 a[102:103], v17 offset:4096\n"
    "v_mfma_f32_16x16x32_bf16 v[148:151], a[148:151], a[116:119], v[148:151]\n"
    "ds_read_b64_tr_b16 a[104:105], v18 offset:0\n"
    "buffer_load_dwordx4 v21, s[12:15], s84 offen offset:1024 sc1 lds\n"
    "v_mfma_f32_16x16x32_bf16 v[144:147], a[144:147], a[116:119], v[144:147]\n"
    "v_mfma_f32_16x16x32_bf16 v[160:163], a[144:147], a[120:123], v[160:163]\n"
    "ds_read_b64_tr_b16 a[106:107], v18 offset:4096\n"
    "v_mfma_f32_16x16x32_bf16 v[164:167], a[148:151], a[120:123], v[164:167]\n"
    "ds_read_b64_tr_b16 a[108:109], v19 offset:0\n"
    "v_mfma_f32_16x16x32_bf16 v[168:171], a[152:155], a[120:123], v[168:171]\n"
    "v_mfma_f32_16x16x32_bf16 v[172:175], a[156:159], a[120:123], v[172:175]\n"
    "ds_read_b64_tr_b16 a[110:111], v19 offset:4096\n"
    "buffer_load_dwordx4 v21, s[12:15], s84 offen offset:2048 sc1 lds\n"
    "v_mfma_f32_16x16x32_bf16 v[188:191], a[156:159], a[124:127], v[188:191]\n"
    "ds_read_b128 a[68:71], v15 offset:2048\n"
    "v_mfma_f32_16x16x32_bf16 v[184:187], a[152:155], a[124:127], v[184:187]\n"
    "v_mfma_f32_16x16x32_bf16 v[180:183], a[148:151], a[124:127], v[180:183]\n"
    "ds_read_b128 a[72:75], v15 offset:4096\n"
    "v_mfma_f32_16x16x32_bf16 v[176:179], a[144:147], a[124:127], v[176:179]\n"
    "ds_read_b128 a[76:79], v15 offset:6144\n"
    "v_mfma_f32_16x16x32_bf16 v[192:195], a[144:147], a[128:131], v[192:195]\n"
    "buffer_load_dwordx4 v21, s[12:15], s84 offen offset:3072 sc1 lds\n"
    "v_mfma_f32_16x16x32_bf16 v[196:199], a[148:151], a[128:131], v[196:199]\n"
    "ds_read_b128 a[80:83], v15 offset:8192\n"
    "v_mfma_f32_16x16x32_bf16 v[200:203], a[152:155], a[128:131], v[200:203]\n"
    "ds_read_b128 a[84:87], v15 offset:10240\n"
    "v_mfma_f32_16x16x32_bf16 v[204:207], a[156:159], a[128:131], v[204:207]\n"
    "v_mfma_f32_16x16x32_bf16 v[220:223], a[156:159], a[132:135], v[220:223]\n"
    "ds_read_b128 a[88:91], v15 offset:12288\n"
    "s_add_u32 m0, s92, 102400\n"
    "v_mfma_f32_16x16x32_bf16 v[216:219], a[152:155], a[132:135], v[216:219]\n"
    "ds_read_b128 a[92:95], v15 offset:14336\n"
    "buffer_load_dwordx4 v21, s[12:15], s85 offen offset:0 sc1 lds\n"
    "v_mfma_f32_16x16x32_bf16 v[212:215], a[148:151], a[132:135], v[212:215]\n"
    "v_mfma_f32_16x16x32_bf16 v[208:211], a[144:147], a[132:135], v[208:211]\n"
    "v_mfma_f32_16x16x32_bf16 v[224:227], a[144:147], a[136:139], v[224:227]\n"
    "v_mfma_f32_16x16x32_bf16 v[228:231], a[148:151], a[136:139], v[228:231]\n"
    "v_mfma_f32_16x16x32_bf16 v[232:235], a[152:155], a[136:139], v[232:235]\n"
    "buffer_load_dwordx4 v21, s[12:15], s85 offen offset:1024 sc1 lds\n"
    "v_mfma_f32_16x16x32_bf16 v[236:239], a[156:159], a[136:139], v[236:239]\n"
    "v_mfma_f32_16x16x32_bf16 v[252:255], a[156:159], a[140:143], v[252:255]\n"
    "v_mfma_f32_16x16x32_bf16 v[248:251], a[152:155], a[140:143], v[248:251]\n"
    "v_mfma_f32_16x16x32_bf16 v[244:247], a[148:151], a[140:143], v[244:247]\n"
    "v_mfma_f32_16x16x32_bf16 v[240:243], a[144:147], a[140:143], v[240:243]\n"
    "s_waitcnt lgkmcnt(0)\n"
    "buffer_load_dwordx4 v21, s[12:15], s85 offen offset:2048 sc1 lds\n"
    "v_mfma_f32_16x16x32_bf16 v[128:131], a[96:99], a[64:67], v[128:131]\n"
    "ds_read_b128 a[112:115], v15 offset:1024\n"
    "v_mfma_f32_16x16x32_bf16 v[132:135], a[100:103], a[64:67], v[132:135]\n"
    "ds_read_b64_tr_b16 a[144:145], v16 offset:8192\n"
    "v_mfma_f32_16x16x32_bf16 v[136:139], a[104:107], a[64:67], v[136:139]\n"
    "v_mfma_f32_16x16x32_bf16 v[140:143], a[108:111], a[64:67], v[140:143]\n"
    "ds_read_b64_tr_b16 a[146:147], v16 offset:12288\n"
    "v_mfma_f32_16x16x32_bf16 v[156:159], a[108:111], a[68:71], v[156:159]\n"
    "ds_read_b64_tr_b16 a[148:149], v17 offset:8192\n"
    "buffer_load_dwordx4 v21, s[12:15], s85 offen offset:3072 sc1 lds\n"
    "v_mfma_f32_16x16x32_bf16 v[152:155], a[104:107], a[68:71], v[152:155]\n"
    "v_mfma_f32_16x16x32_bf16 v[148:151], a[100:103], a[68:71], v[148:151]\n"
    "ds_read_b64_tr_b16 a[150:151], v17 offset:12288\n"
    "v_mfma_f32_16x16x32_bf16 v[144:147], a[96:99], a[68:71], v[144:147]\n"
    "ds_read_b64_tr_b16 a[152:153], v18 offset:8192\n"
    "v_mfma_f32_16x16x32_bf16 v[160:163], a[96:99], a[72:75], v[160:163]\n"
    "s_add_u32 m0, s98, 131072\n"
    "v_mfma_f32_16x16x32_bf16 v[164:167], a[100:103], a[72:75], v[164:167]\n"
    "ds_read_b64_tr_b16 a[154:155], v18 offset:12288\n"
    "buffer_load_dwordx4 v96, s[72:75], 0 offen offset:0 sc1 lds\n"
    "v_mfma_f32_16x16x32_bf16 v[168:171], a[104:107], a[72:75], v[168:171]\n"
    "ds_read_b64_tr_b16 a[156:157], v19 offset:8192\n"
    "v_mfma_f32_16x16x32_bf16 v[172:175], a[108:111], a[72:75], v[172:175]\n"
    "v_mfma_f32_16x16x32_bf16 v[188:191], a[108:111], a[76:79], v[188:191]\n"
    "ds_read_b64_tr_b16 a[158:159], v19 offset:12288\n"
    "v_mfma_f32_16x16x32_bf16 v[184:187], a[104:107], a[76:79], v[184:187]\n"
    "ds_read_b128 a[116:119], v15 offset:3072\n"
    "v_mfma_f32_16x16x32_bf16 v[180:183], a[100:103], a[76:79], v[180:183]\n"
    "buffer_load_dwordx4 v97, s[72:75], s86 offen offset:1024 sc1 lds\n"
    "v_mfma_f32_16x16x32_bf16 v[176:179], a[96:99], a[76:79], v[176:179]\n"
    "ds_read_b128 a[120:123], v15 offset:5120\n"
    "v_mfma_f32_16x16x32_bf16 v[192:195], a[96:99], a[80:83], v[192:195]\n"
    "ds_read_b128 a[124:127], v15 offset:7168\n"
    "v_mfma_f32_16x16x32_bf16 v[196:199], a[100:103], a[80:83], v[196:199]\n"
    "v_mfma_f32_16x16x32_bf16 v[200:203], a[104:107], a[80:83], v[200:203]\n"
    "ds_read_b128 a[128:131], v15 offset:9216\n"
    "v_mfma_f32_16x16x32_bf16 v[204:207], a[108:111], a[80:83], v[204:207]\n"
    "ds_read_b128 a[132:135], v15 offset:11264\n"
    "buffer_load_dwordx4 v96, s[72:75], s87 offen offset:2048 sc1 lds\n"
    "v_mfma_f32_16x16x32_bf16 v[220:223], a[108:111], a[84:87], v[220:223]\n"
    "v_mfma_f32_16x16x32_bf16 v[216:219], a[104:107], a[84:87], v[216:219]\n"
    "ds_read_b128 a[136:139], v15 offset:13312\n"
    "v_mfma_f32_16x16x32_bf16 v[212:215], a[100:103], a[84:87], v[212:215]\n"
    "ds_read_b128 a[140:143], v15 offset:15360\n"
    "v_mfma_f32_16x16x32_bf16 v[208:211], a[96:99], a[84:87], v[208:211]\n"
    "v_add_u32 v15, s96, v15\n"
    "v_mfma_f32_16x16x32_bf16 v[224:227], a[96:99], a[88:91], v[224:227]\n"
    "buffer_load_dwordx4 v97, s[72:75], s88 offen offset:3072 sc1 lds\n"
    "v_mfma_f32_16x16x32_bf16 v[228:231], a[100:103], a[88:91], v[228:231]\n"
    "v_add_u32 v16, s96, v16\n"
    "v_mfma_f32_16x16x32_bf16 v[232:235], a[104:107], a[88:91], v[232:235]\n"
    "v_add_u32 v17, s96, v17\n"
    "v_mfma_f32_16x16x32_bf16 v[236:239], a[108:111], a[88:91], v[236:239]\n"
    "v_add_u32 v18, s96, v18\n"
    "v_mfma_f32_16x16x32_bf16 v[252:255], a[108:111], a[92:95], v[252:255]\n"
    "v_add_u32 v19, s96, v19\n"
    "s_add_u32 s12, s12, 32768\n"
    "s_addc_u32 s13, s13, 0\n"
    "v_mfma_f32_16x16x32_bf16 v[248:251], a[104:107], a[92:95], v[248:251]\n"
    "s_add_u32 s72, s72, 131072\n"
    "s_addc_u32 s73, s73, 0\n"
    "v_mfma_f32_16x16x32_bf16 v[244:247], a[100:103], a[92:95], v[244:247]\n"
    "v_mfma_f32_16x16x32_bf16 v[240:243], a[96:99], a[92:95], v[240:243]\n"
    "s_waitcnt lgkmcnt(0) vmcnt(12)\n"
    "s_barrier\n"
    "ds_read_b128 a[64:67], v15 offset:0\n"
    "v_mfma_f32_16x16x32_bf16 v[128:131], a[144:147], a[112:115], v[128:131]\n"
    "ds_read_b64_tr_b16 a[96:97], v16 offset:0\n"
    "s_add_u32 m0, s92, 0\n"
    "v_mfma_f32_16x16x32_bf16 v[132:135], a[148:151], a[112:115], v[132:135]\n"
    "buffer_load_dwordx4 v21, s[12:15], s84 offen offset:0 sc1 lds\n"
    "v_mfma_f32_16x16x32_bf16 v[136:139], a[152:155], a[112:115], v[136:139]\n"
    "ds_read_b64_tr_b16 a[98:99], v16 offset:4096\n"
    "v_lshrrev_b32 v1, 4, v0\n"
    "v_mfma_f32_16x16x32_bf16 v[140:143], a[156:159], a[112:115], v[140:143]\n"
    "ds_read_b64_tr_b16 a[100:101], v17 offset:0\n"
    "v_bfe_u32 v2, v0, 2, 2\n"
    "v_mfma_f32_16x16x32_bf16 v[156:159], a[156:159], a[116:119], v[156:159]\n"
    "v_and_b32 v3, 1, v0\n"
    "v_lshlrev_b32 v3, 4, v3\n"
    "v_mfma_f32_16x16x32_bf16 v[152:155], a[152:155], a[116:119], v[152:155]\n"
    "ds_read_b64_tr_b16 a[102:103], v17 offset:4096\n"
    "v_mov_b32 v22, 0x3f80\n"
    "v_mfma_f32_16x16x32_bf16 v[148:151], a[148:151], a[116:119], v[148:151]\n"
    "ds_read_b64_tr_b16 a[104:105], v18 offset:0\n"
    "buffer_load_dwordx4 v21, s[12:15], s84 offen offset:1024 sc1 lds\n"
    "v_mfma_f32_16x16x32_bf16 v[144:147], a[144:147], a[116:119], v[144:147]\n"
    "v_lshlrev_b32 v3, v3, v22\n"
    "v_cmp_eq_u32 vcc, v1, v2\n"
    "v_mfma_f32_16x16x32_bf16 v[160:163], a[144:147], a[120:123], v[160:163]\n"
    "ds_read_b64_tr_b16 a[106:107], v18 offset:4096\n"
    "v_cndmask_b32 v3, 0, v3, vcc\n"
    "v_mfma_f32_16x16x32_bf16 v[164:167], a[148:151], a[120:123], v[164:167]\n"
    "ds_read_b64_tr_b16 a[108:109], v19 offset:0\n"
    "v_and_b32 v1, 2, v0\n"
    "v_mfma_f32_16x16x32_bf16 v[168:171], a[152:155], a[120:123], v[168:171]\n"
    "v_cmp_eq_u32 vcc, 0, v1\n"
    "v_cndmask_b32 v24, 0, v3, vcc\n"
    "v_mfma_f32_16x16x32_bf16 v[172:175], a[156:159], a[120:123], v[172:175]\n"
    "ds_read_b64_tr_b16 a[110:111], v19 offset:4096\n"
    "buffer_load_dwordx4 v21, s[12:15], s84 offen offset:2048 sc1 lds\n"
    "v_mfma_f32_16x16x32_bf16 v[188:191], a[156:159], a[124:127], v[188:191]\n"
    "ds_read_b128 a[68:71], v15 offset:2048\n"
    "v_xor_b32 v25, v24, v3\n"
    "v_mfma_f32_16x16x32_bf16 v[184:187], a[152:155], a[124:127], v[184:187]\n"
    "v_mov_b32 v22, 0\n"
    "v_mov_b32 v23, 0\n"
    "v_mfma_f32_16x16x32_bf16 v[180:183], a[148:151], a[124:127], v[180:183]\n"
    "ds_read_b128 a[72:75], v15 offset:4096\n"
    "v_mov_b32 v26, 0\n"
    "v_mfma_f32_16x16x32_bf16 v[176:179], a[144:147], a[124:127], v[176:179]\n"
    "ds_read_b128 a[76:79], v15 offset:6144\n"
    "v_mov_b32 v27, 0\n"
    "v_mfma_f32_16x16x32_bf16 v[192:195], a[144:147], a[128:131], v[192:195]\n"
    "buffer_load_dwordx4 v21, s[12:15], s84 offen offset:3072 sc1 lds\n"
    "v_mfma_f32_16x16x32_bf16 v[196:199], a[148:151], a[128:131], v[196:199]\n"
    "ds_read_b128 a[80:83], v15 offset:8192\n"
    "v_mfma_f32_16x16x32_bf16 v[200:203], a[152:155], a[128:131], v[200:203]\n"
    "ds_read_b128 a[84:87], v15 offset:10240\n"
    "v_mfma_f32_16x16x32_bf16 v[204:207], a[156:159], a[128:131], v[204:207]\n"
    "v_mfma_f32_16x16x32_bf16 v[220:223], a[156:159], a[132:135], v[220:223]\n"
    "ds_read_b128 a[88:91], v15 offset:12288\n"
    "s_add_u32 m0, s92, 4096\n"
    "v_mfma_f32_16x16x32_bf16 v[216:219], a[152:155], a[132:135], v[216:219]\n"
    "ds_read_b128 a[92:95], v15 offset:14336\n"
    "buffer_load_dwordx4 v21, s[12:15], s85 offen offset:0 sc1 lds\n"
    "v_mfma_f32_16x16x32_bf16 v[212:215], a[148:151], a[132:135], v[212:215]\n"
    "v_mfma_f32_16x16x32_bf16 v[208:211], a[144:147], a[132:135], v[208:211]\n"
    "v_mfma_f32_16x16x32_bf16 v[224:227], a[144:147], a[136:139], v[224:227]\n"
    "v_mfma_f32_16x16x32_bf16 v[228:231], a[148:151], a[136:139], v[228:231]\n"
    "v_mfma_f32_16x16x32_bf16 v[232:235], a[152:155], a[136:139], v[232:235]\n"
    "buffer_load_dwordx4 v21, s[12:15], s85 offen offset:1024 sc1 lds\n"
    "v_mfma_f32_16x16x32_bf16 v[236:239], a[156:159], a[136:139], v[236:239]\n"
    "v_mfma_f32_16x16x32_bf16 v[252:255], a[156:159], a[140:143], v[252:255]\n"
    "v_mfma_f32_16x16x32_bf16 v[248:251], a[152:155], a[140:143], v[248:251]\n"
    "v_mfma_f32_16x16x32_bf16 v[244:247], a[148:151], a[140:143], v[244:247]\n"
    "v_mfma_f32_16x16x32_bf16 v[240:243], a[144:147], a[140:143], v[240:243]\n"
    "s_waitcnt lgkmcnt(0)\n"
    "buffer_load_dwordx4 v21, s[12:15], s85 offen offset:2048 sc1 lds\n"
    "v_mfma_f32_16x16x32_bf16 v[128:131], a[96:99], a[64:67], v[128:131]\n"
    "ds_read_b128 a[112:115], v15 offset:1024\n"
    "v_mfma_f32_16x16x32_bf16 v[132:135], a[100:103], a[64:67], v[132:135]\n"
    "ds_read_b64_tr_b16 a[144:145], v16 offset:8192\n"
    "v_mfma_f32_16x16x32_bf16 v[136:139], a[104:107], a[64:67], v[136:139]\n"
    "v_mfma_f32_16x16x32_bf16 v[140:143], a[108:111], a[64:67], v[140:143]\n"
    "ds_read_b64_tr_b16 a[146:147], v16 offset:12288\n"
    "v_mfma_f32_16x16x32_bf16 v[156:159], a[108:111], a[68:71], v[156:159]\n"
    "ds_read_b64_tr_b16 a[148:149], v17 offset:8192\n"
    "buffer_load_dwordx4 v21, s[12:15], s85 offen offset:3072 sc1 lds\n"
    "v_mfma_f32_16x16x32_bf16 v[152:155], a[104:107], a[68:71], v[152:155]\n"
    "v_mfma_f32_16x16x32_bf16 v[148:151], a[100:103], a[68:71], v[148:151]\n"
    "ds_read_b64_tr_b16 a[150:151], v17 offset:12288\n"
    "v_mfma_f32_16x16x32_bf16 v[144:147], a[96:99], a[68:71], v[144:147]\n"
    "ds_read_b64_tr_b16 a[152:153], v18 offset:8192\n"
    "v_mfma_f32_16x16x32_bf16 v[160:163], a[96:99], a[72:75], v[160:163]\n"
    "s_add_u32 m0, s98, 32768\n"
    "v_mfma_f32_16x16x32_bf16 v[164:167], a[100:103], a[72:75], v[164:167]\n"
    "ds_read_b64_tr_b16 a[154:155], v18 offset:12288\n"
    "buffer_load_dwordx4 v96, s[72:75], 0 offen offset:0 sc1 lds\n"
    "v_mfma_f32_16x16x32_bf16 v[168:171], a[104:107], a[72:75], v[168:171]\n"
    "ds_read_b64_tr_b16 a[156:157], v19 offset:8192\n"
    "v_mfma_f32_16x16x32_bf16 v[172:175], a[108:111], a[72:75], v[172:175]\n"
    "v_mfma_f32_16x16x32_bf16 v[188:191], a[108:111], a[76:79], v[188:191]\n"
    "ds_read_b64_tr_b16 a[158:159], v19 offset:12288\n"
    "v_mfma_f32_16x16x32_bf16 v[184:187], a[104:107], a[76:79], v[184:187]\n"
    "ds_read_b128 a[116:119], v15 offset:3072\n"
    "v_mfma_f32_16x16x32_bf16 v[180:183], a[100:103], a[76:79], v[180:183]\n"
    "buffer_load_dwordx4 v97, s[72:75], s86 offen offset:1024 sc1 lds\n"
    "v_mfma_f32_16x16x32_bf16 v[176:179], a[96:99], a[76:79], v[176:179]\n"
    "ds_read_b128 a[120:123], v15 offset:5120\n"
    "v_mfma_f32_16x16x32_bf16 v[192:195], a[96:99], a[80:83], v[192:195]\n"
    "ds_read_b128 a[124:127], v15 offset:7168\n"
    "v_mfma_f32_16x16x32_bf16 v[196:199], a[100:103], a[80:83], v[196:199]\n"
    "v_mfma_f32_16x16x32_bf16 v[200:203], a[104:107], a[80:83], v[200:203]\n"
    "ds_read_b128 a[128:131], v15 offset:9216\n"
    "v_mfma_f32_16x16x32_bf16 v[204:207], a[108:111], a[80:83], v[204:207]\n"
    "ds_read_b128 a[132:135], v15 offset:11264\n"
    "buffer_load_dwordx4 v96, s[72:75], s87 offen offset:2048 sc1 lds\n"
    "v_mfma_f32_16x16x32_bf16 v[220:223], a[108:111], a[84:87], v[220:223]\n"
    "v_mfma_f32_16x16x32_bf16 v[216:219], a[104:107], a[84:87], v[216:219]\n"
    "ds_read_b128 a[136:139], v15 offset:13312\n"
    "v_mfma_f32_16x16x32_bf16 v[212:215], a[100:103], a[84:87], v[212:215]\n"
    "ds_read_b128 a[140:143], v15 offset:15360\n"
    "v_mfma_f32_16x16x32_bf16 v[208:211], a[96:99], a[84:87], v[208:211]\n"
    "v_add_u32 v15, s96, v15\n"
    "v_mfma_f32_16x16x32_bf16 v[224:227], a[96:99], a[88:91], v[224:227]\n"
    "buffer_load_dwordx4 v97, s[72:75], s88 offen offset:3072 sc1 lds\n"
    "v_mfma_f32_16x16x32_bf16 v[228:231], a[100:103], a[88:91], v[228:231]\n"
    "v_add_u32 v16, s96, v16\n"
    "v_mfma_f32_16x16x32_bf16 v[232:235], a[104:107], a[88:91], v[232:235]\n"
    "v_add_u32 v17, s96, v17\n"
    "v_mfma_f32_16x16x32_bf16 v[236:239], a[108:111], a[88:91], v[236:239]\n"
    "v_add_u32 v18, s96, v18\n"
    "v_mfma_f32_16x16x32_bf16 v[252:255], a[108:111], a[92:95], v[252:255]\n"
    "v_add_u32 v19, s96, v19\n"
    "s_add_u32 s12, s12, 32768\n"
    "s_addc_u32 s13, s13, 0\n"
    "v_mfma_f32_16x16x32_bf16 v[248:251], a[104:107], a[92:95], v[248:251]\n"
    "s_add_u32 s72, s72, 131072\n"
    "s_addc_u32 s73, s73, 0\n"
    "v_mfma_f32_16x16x32_bf16 v[244:247], a[100:103], a[92:95], v[244:247]\n"
    "v_mfma_f32_16x16x32_bf16 v[240:243], a[96:99], a[92:95], v[240:243]\n"
    "s_waitcnt lgkmcnt(0) vmcnt(12)\n"
    "s_barrier\n"
    "ds_read_b128 a[64:67], v15 offset:0\n"
    "v_mfma_f32_16x16x32_bf16 v[128:131], a[144:147], a[112:115], v[128:131]\n"
    "ds_read_b64_tr_b16 a[96:97], v16 offset:0\n"
    "s_add_u32 m0, s92, 49152\n"
    "v_mfma_f32_16x16x32_bf16 v[132:135], a[148:151], a[112:115], v[132:135]\n"
    "buffer_load_dwordx4 v21, s[12:15], s84 offen offset:0 sc1 lds\n"
    "v_mfma_f32_16x16x32_bf16 v[136:139], a[152:155], a[112:115], v[136:139]\n"
    "TC10:\n"
    "s_cbranch_execz TC14\n"
    "ds_read_b64_tr_b16 a[98:99], v16 offset:4096\n"
    "v_mfma_f32_16x16x32_bf16 v[140:143], a[156:159], a[112:115], v[140:143]\n"
    "ds_read_b64_tr_b16 a[100:101], v17 offset:0\n"
    "v_mfma_f32_16x16x32_bf16 v[156:159], a[156:159], a[116:119], v[156:159]\n"
    "v_mfma_f32_16x16x32_bf16 v[152:155], a[152:155], a[116:119], v[152:155]\n"
    "ds_read_b64_tr_b16 a[102:103], v17 offset:4096\n"
    "v_mfma_f32_16x16x32_bf16 v[148:151], a[148:151], a[116:119], v[148:151]\n"
    "ds_read_b64_tr_b16 a[104:105], v18 offset:0\n"
    "buffer_load_dwordx4 v21, s[12:15], s84 offen offset:1024 sc1 lds\n"
    "v_mfma_f32_16x16x32_bf16 v[144:147], a[144:147], a[116:119], v[144:147]\n"
    "v_mfma_f32_16x16x32_bf16 v[160:163], a[144:147], a[120:123], v[160:163]\n"
    "ds_read_b64_tr_b16 a[106:107], v18 offset:4096\n"
    "v_mfma_f32_16x16x32_bf16 v[164:167], a[148:151], a[120:123], v[164:167]\n"
    "ds_read_b64_tr_b16 a[108:109], v19 offset:0\n"
    "v_mfma_f32_16x16x32_bf16 v[168:171], a[152:155], a[120:123], v[168:171]\n"
    "v_mfma_f32_16x16x32_bf16 v[172:175], a[156:159], a[120:123], v[172:175]\n"
    "ds_read_b64_tr_b16 a[110:111], v19 offset:4096\n"
    "buffer_load_dwordx4 v21, s[12:15], s84 offen offset:2048 sc1 lds\n"
    "v_mfma_f32_16x16x32_bf16 v[188:191], a[156:159], a[124:127], v[188:191]\n"
    "ds_read_b128 a[68:71], v15 offset:2048\n"
    "v_mfma_f32_16x16x32_bf16 v[184:187], a[152:155], a[124:127], v[184:187]\n"
    "v_mfma_f32_16x16x32_bf16 v[180:183], a[148:151], a[124:127], v[180:183]\n"
    "ds_read_b128 a[72:75], v15 offset:4096\n"
    "v_mfma_f32_16x16x32_bf16 v[176:179], a[144:147], a[124:127], v[176:179]\n"
    "ds_read_b128 a[76:79], v15 offset:6144\n"
    "v_mfma_f32_16x16x32_bf16 v[192:195], a[144:147], a[128:131], v[192:195]\n"
    "buffer_load_dwordx4 v21, s[12:15], s84 offen offset:3072 sc1 lds\n"
    "v_mfma_f32_16x16x32_bf16 v[196:199], a[148:151], a[128:131], v[196:199]\n"
    "ds_read_b128 a[80:83], v15 offset:8192\n"
    "v_mfma_f32_16x16x32_bf16 v[200:203], a[152:155], a[128:131], v[200:203]\n"
    "ds_read_b128 a[84:87], v15 offset:10240\n"
    "v_mfma_f32_16x16x32_bf16 v[204:207], a[156:159], a[128:131], v[204:207]\n"
    "v_mfma_f32_16x16x32_bf16 v[220:223], a[156:159], a[132:135], v[220:223]\n"
    "ds_read_b128 a[88:91], v15 offset:12288\n"
    "s_add_u32 m0, s92, 53248\n"
    "v_mfma_f32_16x16x32_bf16 v[216:219], a[152:155], a[132:135], v[216:219]\n"
    "ds_read_b128 a[92:95], v15 offset:14336\n"
    "buffer_load_dwordx4 v21, s[12:15], s85 offen offset:0 sc1 lds\n"
    "v_mfma_f32_16x16x32_bf16 v[212:215], a[148:151], a[132:135], v[212:215]\n"
    "v_mfma_f32_16x16x32_bf16 v[208:211], a[144:147], a[132:135], v[208:211]\n"
    "v_mfma_f32_16x16x32_bf16 v[224:227], a[144:147], a[136:139], v[224:227]\n"
    "v_mfma_f32_16x16x32_bf16 v[228:231], a[148:151], a[136:139], v[228:231]\n"
    "v_mfma_f32_16x16x32_bf16 v[232:235], a[152:155], a[136:139], v[232:235]\n"
    "buffer_load_dwordx4 v21, s[12:15], s85 offen offset:1024 sc1 lds\n"
    "v_mfma_f32_16x16x32_bf16 v[236:239], a[156:159], a[136:139], v[236:239]\n"
    "v_mfma_f32_16x16x32_bf16 v[252:255], a[156:159], a[140:143], v[252:255]\n"
    "v_mfma_f32_16x16x32_bf16 v[248:251], a[152:155], a[140:143], v[248:251]\n"
    "v_mfma_f32_16x16x32_bf16 v[244:247], a[148:151], a[140:143], v[244:247]\n"
    "v_mfma_f32_16x16x32_bf16 v[240:243], a[144:147], a[140:143], v[240:243]\n"
    "s_waitcnt lgkmcnt(0)\n"
    "buffer_load_dwordx4 v21, s[12:15], s85 offen offset:2048 sc1 lds\n"
    "v_mfma_f32_16x16x32_bf16 v[128:131], a[96:99], a[64:67], v[128:131]\n"
    "ds_read_b128 a[112:115], v15 offset:1024\n"
    "v_mfma_f32_16x16x32_bf16 v[132:135], a[100:103], a[64:67], v[132:135]\n"
    "ds_read_b64_tr_b16 a[144:145], v16 offset:8192\n"
    "v_mfma_f32_16x16x32_bf16 v[136:139], a[104:107], a[64:67], v[136:139]\n"
    "v_mfma_f32_16x16x32_bf16 v[140:143], a[108:111], a[64:67], v[140:143]\n"
    "ds_read_b64_tr_b16 a[146:147], v16 offset:12288\n"
    "v_mfma_f32_16x16x32_bf16 v[156:159], a[108:111], a[68:71], v[156:159]\n"
    "ds_read_b64_tr_b16 a[148:149], v17 offset:8192\n"
    "buffer_load_dwordx4 v21, s[12:15], s85 offen offset:3072 sc1 lds\n"
    "v_mfma_f32_16x16x32_bf16 v[152:155], a[104:107], a[68:71], v[152:155]\n"
    "v_mfma_f32_16x16x32_bf16 v[148:151], a[100:103], a[68:71], v[148:151]\n"
    "ds_read_b64_tr_b16 a[150:151], v17 offset:12288\n"
    "v_mfma_f32_16x16x32_bf16 v[144:147], a[96:99], a[68:71], v[144:147]\n"
    "ds_read_b64_tr_b16 a[152:153], v18 offset:8192\n"
    "v_mfma_f32_16x16x32_bf16 v[160:163], a[96:99], a[72:75], v[160:163]\n"
    "s_add_u32 m0, s98, 81920\n"
    "v_mfma_f32_16x16x32_bf16 v[164:167], a[100:103], a[72:75], v[164:167]\n"
    "ds_read_b64_tr_b16 a[154:155], v18 offset:12288\n"
    "buffer_load_dwordx4 v96, s[72:75], 0 offen offset:0 sc1 lds\n"
    "v_mfma_f32_16x16x32_bf16 v[168:171], a[104:107], a[72:75], v[168:171]\n"
    "ds_read_b64_tr_b16 a[156:157], v19 offset:8192\n"
    "v_mfma_f32_16x16x32_bf16 v[172:175], a[108:111], a[72:75], v[172:175]\n"
    "v_mfma_f32_16x16x32_bf16 v[188:191], a[108:111], a[76:79], v[188:191]\n"
    "ds_read_b64_tr_b16 a[158:159], v19 offset:12288\n"
    "v_mfma_f32_16x16x32_bf16 v[184:187], a[104:107], a[76:79], v[184:187]\n"
    "ds_read_b128 a[116:119], v15 offset:3072\n"
    "v_mfma_f32_16x16x32_bf16 v[180:183], a[100:103], a[76:79], v[180:183]\n"
    "buffer_load_dwordx4 v97, s[72:75], s86 offen offset:1024 sc1 lds\n"
    "v_mfma_f32_16x16x32_bf16 v[176:179], a[96:99], a[76:79], v[176:179]\n"
    "ds_read_b128 a[120:123], v15 offset:5120\n"
    "v_mfma_f32_16x16x32_bf16 v[192:195], a[96:99], a[80:83], v[192:195]\n"
    "ds_read_b128 a[124:127], v15 offset:7168\n"
    "v_mfma_f32_16x16x32_bf16 v[196:199], a[100:103], a[80:83], v[196:199]\n"
    "v_mfma_f32_16x16x32_bf16 v[200:203], a[104:107], a[80:83], v[200:203]\n"
    "ds_read_b128 a[128:131], v15 offset:9216\n"
    "v_mfma_f32_16x16x32_bf16 v[204:207], a[108:111], a[80:83], v[204:207]\n"
    "ds_read_b128 a[132:135], v15 offset:11264\n"
    "buffer_load_dwordx4 v96, s[72:75], s87 offen offset:2048 sc1 lds\n"
    "v_mfma_f32_16x16x32_bf16 v[220:223], a[108:111], a[84:87], v[220:223]\n"
    "v_mfma_f32_16x16x32_bf16 v[216:219], a[104:107], a[84:87], v[216:219]\n"
    "ds_read_b128 a[136:139], v15 offset:13312\n"
    "v_mfma_f32_16x16x32_bf16 v[212:215], a[100:103], a[84:87], v[212:215]\n"
    "ds_read_b128 a[140:143], v15 offset:15360\n"
    "v_mfma_f32_16x16x32_bf16 v[208:211], a[96:99], a[84:87], v[208:211]\n"
    "v_add_u32 v15, s97, v15\n"
    "v_mfma_f32_16x16x32_bf16 v[224:227], a[96:99], a[88:91], v[224:227]\n"
    "buffer_load_dwordx4 v97, s[72:75], s88 offen offset:3072 sc1 lds\n"
    "v_mfma_f32_16x16x32_bf16 v[228:231], a[100:103], a[88:91], v[228:231]\n"
    "v_add_u32 v16, s97, v16\n"
    "v_mfma_f32_16x16x32_bf16 v[232:235], a[104:107], a[88:91], v[232:235]\n"
    "v_add_u32 v17, s97, v17\n"
    "v_mfma_f32_16x16x32_bf16 v[236:239], a[108:111], a[88:91], v[236:239]\n"
    "v_add_u32 v18, s97, v18\n"
    "v_mfma_f32_16x16x32_bf16 v[252:255], a[108:111], a[92:95], v[252:255]\n"
    "v_add_u32 v19, s97, v19\n"
    "s_add_u32 s12, s12, 32768\n"
    "s_addc_u32 s13, s13, 0\n"
    "v_mfma_f32_16x16x32_bf16 v[248:251], a[104:107], a[92:95], v[248:251]\n"
    "s_add_u32 s72, s72, 131072\n"
    "s_addc_u32 s73, s73, 0\n"
    "v_mfma_f32_16x16x32_bf16 v[244:247], a[100:103], a[92:95], v[244:247]\n"
    "v_mfma_f32_16x16x32_bf16 v[240:243], a[96:99], a[92:95], v[240:243]\n"
    "s_waitcnt lgkmcnt(0) vmcnt(12)\n"
    "s_barrier\n"
    "ds_read_b128 a[64:67], v15 offset:0\n"
    "v_mfma_f32_16x16x32_bf16 v[128:131], a[144:147], a[112:115], v[128:131]\n"
    "ds_read_b64_tr_b16 a[96:97], v16 offset:0\n"
    "s_add_u32 m0, s92, 98304\n"
    "v_mfma_f32_16x16x32_bf16 v[132:135], a[148:151], a[112:115], v[132:135]\n"
    "buffer_load_dwordx4 v21, s[12:15], s84 offen offset:0 sc1 lds\n"
    "v_mfma_f32_16x16x32_bf16 v[136:139], a[152:155], a[112:115], v[136:139]\n"
    "ds_read_b64_tr_b16 a[98:99], v16 offset:4096\n"
    "v_mfma_f32_16x16x32_bf16 v[140:143], a[156:159], a[112:115], v[140:143]\n"
    "ds_read_b64_tr_b16 a[100:101], v17 offset:0\n"
    "v_mfma_f32_16x16x32_bf16 v[156:159], a[156:159], a[116:119], v[156:159]\n"
    "v_mfma_f32_16x16x32_bf16 v[152:155], a[152:155], a[116:119], v[152:155]\n"
    "ds_read_b64_tr_b16 a[102:103], v17 offset:4096\n"
    "v_mfma_f32_16x16x32_bf16 v[148:151], a[148:151], a[116:119], v[148:151]\n"
    "ds_read_b64_tr_b16 a[104:105], v18 offset:0\n"
    "buffer_load_dwordx4 v21, s[12:15], s84 offen offset:1024 sc1 lds\n"
    "v_mfma_f32_16x16x32_bf16 v[144:147], a[144:147], a[116:119], v[144:147]\n"
    "v_mfma_f32_16x16x32_bf16 v[160:163], a[144:147], a[120:123], v[160:163]\n"
    "ds_read_b64_tr_b16 a[106:107], v18 offset:4096\n"
    "v_mfma_f32_16x16x32_bf16 v[164:167], a[148:151], a[120:123], v[164:167]\n"
    "ds_read_b64_tr_b16 a[108:109], v19 offset:0\n"
    "v_mfma_f32_16x16x32_bf16 v[168:171], a[152:155], a[120:123], v[168:171]\n"
    "v_mfma_f32_16x16x32_bf16 v[172:175], a[156:159], a[120:123], v[172:175]\n"
    "ds_read_b64_tr_b16 a[110:111], v19 offset:4096\n"
    "buffer_load_dwordx4 v21, s[12:15], s84 offen offset:2048 sc1 lds\n"
    "v_mfma_f32_16x16x32_bf16 v[188:191], a[156:159], a[124:127], v[188:191]\n"
    "ds_read_b128 a[68:71], v15 offset:2048\n"
    "v_mfma_f32_16x16x32_bf16 v[184:187], a[152:155], a[124:127], v[184:187]\n"
    "v_mfma_f32_16x16x32_bf16 v[180:183], a[148:151], a[124:127], v[180:183]\n"
    "ds_read_b128 a[72:75], v15 offset:4096\n"
    "v_mfma_f32_16x16x32_bf16 v[176:179], a[144:147], a[124:127], v[176:179]\n"
    "ds_read_b128 a[76:79], v15 offset:6144\n"
    "v_mfma_f32_16x16x32_bf16 v[192:195], a[144:147], a[128:131], v[192:195]\n"
    "buffer_load_dwordx4 v21, s[12:15], s84 offen offset:3072 sc1 lds\n"
    "v_mfma_f32_16x16x32_bf16 v[196:199], a[148:151], a[128:131], v[196:199]\n"
    "ds_read_b128 a[80:83], v15 offset:8192\n"
    "v_mfma_f32_16x16x32_bf16 v[200:203], a[152:155], a[128:131], v[200:203]\n"
    "ds_read_b128 a[84:87], v15 offset:10240\n"
    "v_mfma_f32_16x16x32_bf16 v[204:207], a[156:159], a[128:131], v[204:207]\n"
    "v_mfma_f32_16x16x32_bf16 v[220:223], a[156:159], a[132:135], v[220:223]\n"
    "ds_read_b128 a[88:91], v15 offset:12288\n"
    "s_add_u32 m0, s92, 102400\n"
    "v_mfma_f32_16x16x32_bf16 v[216:219], a[152:155], a[132:135], v[216:219]\n"
    "ds_read_b128 a[92:95], v15 offset:14336\n"
    "buffer_load_dwordx4 v21, s[12:15], s85 offen offset:0 sc1 lds\n"
    "v_mfma_f32_16x16x32_bf16 v[212:215], a[148:151], a[132:135], v[212:215]\n"
    "v_mfma_f32_16x16x32_bf16 v[208:211], a[144:147], a[132:135], v[208:211]\n"
    "v_mfma_f32_16x16x32_bf16 v[224:227], a[144:147], a[136:139], v[224:227]\n"
    "v_mfma_f32_16x16x32_bf16 v[228:231], a[148:151], a[136:139], v[228:231]\n"
    "v_mfma_f32_16x16x32_bf16 v[232:235], a[152:155], a[136:139], v[232:235]\n"
    "buffer_load_dwordx4 v21, s[12:15], s85 offen offset:1024 sc1 lds\n"
    "v_mfma_f32_16x16x32_bf16 v[236:239], a[156:159], a[136:139], v[236:239]\n"
    "v_mfma_f32_16x16x32_bf16 v[252:255], a[156:159], a[140:143], v[252:255]\n"
    "v_mfma_f32_16x16x32_bf16 v[248:251], a[152:155], a[140:143], v[248:251]\n"
    "v_mfma_f32_16x16x32_bf16 v[244:247], a[148:151], a[140:143], v[244:247]\n"
    "v_mfma_f32_16x16x32_bf16 v[240:243], a[144:147], a[140:143], v[240:243]\n"
    "s_waitcnt lgkmcnt(0)\n"
    "buffer_load_dwordx4 v21, s[12:15], s85 offen offset:2048 sc1 lds\n"
    "v_mfma_f32_16x16x32_bf16 v[128:131], a[96:99], a[64:67], v[128:131]\n"
    "ds_read_b128 a[112:115], v15 offset:1024\n"
    "v_mfma_f32_16x16x32_bf16 v[132:135], a[100:103], a[64:67], v[132:135]\n"
    "ds_read_b64_tr_b16 a[144:145], v16 offset:8192\n"
    "v_mfma_f32_16x16x32_bf16 v[136:139], a[104:107], a[64:67], v[136:139]\n"
    "v_mfma_f32_16x16x32_bf16 v[140:143], a[108:111], a[64:67], v[140:143]\n"
    "ds_read_b64_tr_b16 a[146:147], v16 offset:12288\n"
    "v_mfma_f32_16x16x32_bf16 v[156:159], a[108:111], a[68:71], v[156:159]\n"
    "ds_read_b64_tr_b16 a[148:149], v17 offset:8192\n"
    "buffer_load_dwordx4 v21, s[12:15], s85 offen offset:3072 sc1 lds\n"
    "v_mfma_f32_16x16x32_bf16 v[152:155], a[104:107], a[68:71], v[152:155]\n"
    "v_mfma_f32_16x16x32_bf16 v[148:151], a[100:103], a[68:71], v[148:151]\n"
    "ds_read_b64_tr_b16 a[150:151], v17 offset:12288\n"
    "v_mfma_f32_16x16x32_bf16 v[144:147], a[96:99], a[68:71], v[144:147]\n"
    "ds_read_b64_tr_b16 a[152:153], v18 offset:8192\n"
    "v_mfma_f32_16x16x32_bf16 v[160:163], a[96:99], a[72:75], v[160:163]\n"
    "s_add_u32 m0, s98, 131072\n"
    "v_mfma_f32_16x16x32_bf16 v[164:167], a[100:103], a[72:75], v[164:167]\n"
    "ds_read_b64_tr_b16 a[154:155], v18 offset:12288\n"
    "buffer_load_dwordx4 v96, s[72:75], 0 offen offset:0 sc1 lds\n"
    "v_mfma_f32_16x16x32_bf16 v[168:171], a[104:107], a[72:75], v[168:171]\n"
    "ds_read_b64_tr_b16 a[156:157], v19 offset:8192\n"
    "v_mfma_f32_16x16x32_bf16 v[172:175], a[108:111], a[72:75], v[172:175]\n"
    "v_mfma_f32_16x16x32_bf16 v[188:191], a[108:111], a[76:79], v[188:191]\n"
    "ds_read_b64_tr_b16 a[158:159], v19 offset:12288\n"
    "v_mfma_f32_16x16x32_bf16 v[184:187], a[104:107], a[76:79], v[184:187]\n"
    "ds_read_b128 a[116:119], v15 offset:3072\n"
    "v_mfma_f32_16x16x32_bf16 v[180:183], a[100:103], a[76:79], v[180:183]\n"
    "buffer_load_dwordx4 v97, s[72:75], s86 offen offset:1024 sc1 lds\n"
    "v_mfma_f32_16x16x32_bf16 v[176:179], a[96:99], a[76:79], v[176:179]\n"
    "ds_read_b128 a[120:123], v15 offset:5120\n"
    "v_mfma_f32_16x16x32_bf16 v[192:195], a[96:99], a[80:83], v[192:195]\n"
    "ds_read_b128 a[124:127], v15 offset:7168\n"
    "v_mfma_f32_16x16x32_bf16 v[196:199], a[100:103], a[80:83], v[196:199]\n"
    "v_mfma_f32_16x16x32_bf16 v[200:203], a[104:107], a[80:83], v[200:203]\n"
    "ds_read_b128 a[128:131], v15 offset:9216\n"
    "v_mfma_f32_16x16x32_bf16 v[204:207], a[108:111], a[80:83], v[204:207]\n"
    "ds_read_b128 a[132:135], v15 offset:11264\n"
    "buffer_load_dwordx4 v96, s[72:75], s87 offen offset:2048 sc1 lds\n"
    "v_mfma_f32_16x16x32_bf16 v[220:223], a[108:111], a[84:87], v[220:223]\n"
    "v_mfma_f32_16x16x32_bf16 v[216:219], a[104:107], a[84:87], v[216:219]\n"
    "ds_read_b128 a[136:139], v15 offset:13312\n"
    "v_mfma_f32_16x16x32_bf16 v[212:215], a[100:103], a[84:87], v[212:215]\n"
    "ds_read_b128 a[140:143], v15 offset:15360\n"
    "v_mfma_f32_16x16x32_bf16 v[208:211], a[96:99], a[84:87], v[208:211]\n"
    "v_add_u32 v15, s96, v15\n"
    "v_mfma_f32_16x16x32_bf16 v[224:227], a[96:99], a[88:91], v[224:227]\n"
    "buffer_load_dwordx4 v97, s[72:75], s88 offen offset:3072 sc1 lds\n"
    "v_mfma_f32_16x16x32_bf16 v[228:231], a[100:103], a[88:91], v[228:231]\n"
    "v_add_u32 v16, s96, v16\n"
    "v_mfma_f32_16x16x32_bf16 v[232:235], a[104:107], a[88:91], v[232:235]\n"
    "v_add_u32 v17, s96, v17\n"
    "v_mfma_f32_16x16x32_bf16 v[236:239], a[108:111], a[88:91], v[236:239]\n"
    "v_add_u32 v18, s96, v18\n"
    "v_mfma_f32_16x16x32_bf16 v[252:255], a[108:111], a[92:95], v[252:255]\n"
    "v_add_u32 v19, s96, v19\n"
    "s_add_u32 s12, s12, 32768\n"
    "s_addc_u32 s13, s13, 0\n"
    "v_mfma_f32_16x16x32_bf16 v[248:251], a[104:107], a[92:95], v[248:251]\n"
    "s_add_u32 s72, s72, 131072\n"
    "s_addc_u32 s73, s73, 0\n"
    "v_mfma_f32_16x16x32_bf16 v[244:247], a[100:103], a[92:95], v[244:247]\n"
    "v_mfma_f32_16x16x32_bf16 v[240:243], a[96:99], a[92:95], v[240:243]\n"
    "s_waitcnt lgkmcnt(0) vmcnt(12)\n"
    "s_barrier\n"
    "ds_read_b128 a[64:67], v15 offset:0\n"
    "v_mfma_f32_16x16x32_bf16 v[128:131], a[144:147], a[112:115], v[128:131]\n"
    "ds_read_b64_tr_b16 a[96:97], v16 offset:0\n"
    "s_add_u32 m0, s92, 0\n"
    "v_mfma_f32_16x16x32_bf16 v[132:135], a[148:151], a[112:115], v[132:135]\n"
    "buffer_load_dwordx4 v21, s[12:15], s84 offen offset:0 sc1 lds\n"
    "v_mfma_f32_16x16x32_bf16 v[136:139], a[152:155], a[112:115], v[136:139]\n"
    "ds_read_b64_tr_b16 a[98:99], v16 offset:4096\n"
    "v_mfma_f32_16x16x32_bf16 v[140:143], a[156:159], a[112:115], v[140:143]\n"
    "ds_read_b64_tr_b16 a[100:101], v17 offset:0\n"
    "v_mfma_f32_16x16x32_bf16 v[156:159], a[156:159], a[116:119], v[156:159]\n"
    "v_mfma_f32_16x16x32_bf16 v[152:155], a[152:155], a[116:119], v[152:155]\n"
    "ds_read_b64_tr_b16 a[102:103], v17 offset:4096\n"
    "v_mfma_f32_16x16x32_bf16 v[148:151], a[148:151], a[116:119], v[148:151]\n"
    "ds_read_b64_tr_b16 a[104:105], v18 offset:0\n"
    "buffer_load_dwordx4 v21, s[12:15], s84 offen offset:1024 sc1 lds\n"
    "v_mfma_f32_16x16x32_bf16 v[144:147], a[144:147], a[116:119], v[144:147]\n"
    "v_mfma_f32_16x16x32_bf16 v[160:163], a[144:147], a[120:123], v[160:163]\n"
    "ds_read_b64_tr_b16 a[106:107], v18 offset:4096\n"
    "v_mfma_f32_16x16x32_bf16 v[164:167], a[148:151], a[120:123], v[164:167]\n"
    "ds_read_b64_tr_b16 a[108:109], v19 offset:0\n"
    "v_mfma_f32_16x16x32_bf16 v[168:171], a[152:155], a[120:123], v[168:171]\n"
    "v_mfma_f32_16x16x32_bf16 v[172:175], a[156:159], a[120:123], v[172:175]\n"
    "ds_read_b64_tr_b16 a[110:111], v19 offset:4096\n"
    "buffer_load_dwordx4 v21, s[12:15], s84 offen offset:2048 sc1 lds\n"
    "v_mfma_f32_16x16x32_bf16 v[188:191], a[156:159], a[124:127], v[188:191]\n"
    "ds_read_b128 a[68:71], v15 offset:2048\n"
    "v_mfma_f32_16x16x32_bf16 v[184:187], a[152:155], a[124:127], v[184:187]\n"
    "v_mfma_f32_16x16x32_bf16 v[180:183], a[148:151], a[124:127], v[180:183]\n"
    "ds_read_b128 a[72:75], v15 offset:4096\n"
    "v_mfma_f32_16x16x32_bf16 v[176:179], a[144:147], a[124:127], v[176:179]\n"
    "ds_read_b128 a[76:79], v15 offset:6144\n"
    "v_mfma_f32_16x16x32_bf16 v[192:195], a[144:147], a[128:131], v[192:195]\n"
    "buffer_load_dwordx4 v21, s[12:15], s84 offen offset:3072 sc1 lds\n"
    "v_mfma_f32_16x16x32_bf16 v[196:199], a[148:151], a[128:131], v[196:199]\n"
    "ds_read_b128 a[80:83], v15 offset:8192\n"
    "v_mfma_f32_16x16x32_bf16 v[200:203], a[152:155], a[128:131], v[200:203]\n"
    "ds_read_b128 a[84:87], v15 offset:10240\n"
    "v_mfma_f32_16x16x32_bf16 v[204:207], a[156:159], a[128:131], v[204:207]\n"
    "v_mfma_f32_16x16x32_bf16 v[220:223], a[156:159], a[132:135], v[220:223]\n"
    "ds_read_b128 a[88:91], v15 offset:12288\n"
    "s_add_u32 m0, s92, 4096\n"
    "v_mfma_f32_16x16x32_bf16 v[216:219], a[152:155], a[132:135], v[216:219]\n"
    "ds_read_b128 a[92:95], v15 offset:14336\n"
    "buffer_load_dwordx4 v21, s[12:15], s85 offen offset:0 sc1 lds\n"
    "v_mfma_f32_16x16x32_bf16 v[212:215], a[148:151], a[132:135], v[212:215]\n"
    "v_mfma_f32_16x16x32_bf16 v[208:211], a[144:147], a[132:135], v[208:211]\n"
    "v_mfma_f32_16x16x32_bf16 v[224:227], a[144:147], a[136:139], v[224:227]\n"
    "v_mfma_f32_16x16x32_bf16 v[228:231], a[148:151], a[136:139], v[228:231]\n"
    "v_mfma_f32_16x16x32_bf16 v[232:235], a[152:155], a[136:139], v[232:235]\n"
    "buffer_load_dwordx4 v21, s[12:15], s85 offen offset:1024 sc1 lds\n"
    "v_mfma_f32_16x16x32_bf16 v[236:239], a[156:159], a[136:139], v[236:239]\n"
    "v_mfma_f32_16x16x32_bf16 v[252:255], a[156:159], a[140:143], v[252:255]\n"
    "v_mfma_f32_16x16x32_bf16 v[248:251], a[152:155], a[140:143], v[248:251]\n"
    "v_mfma_f32_16x16x32_bf16 v[244:247], a[148:151], a[140:143], v[244:247]\n"
    "v_mfma_f32_16x16x32_bf16 v[240:243], a[144:147], a[140:143], v[240:243]\n"
    "s_waitcnt lgkmcnt(0)\n"
    "buffer_load_dwordx4 v21, s[12:15], s85 offen offset:2048 sc1 lds\n"
    "v_mfma_f32_16x16x32_bf16 v[128:131], a[96:99], a[64:67], v[128:131]\n"
    "ds_read_b128 a[112:115], v15 offset:1024\n"
    "v_mfma_f32_16x16x32_bf16 v[132:135], a[100:103], a[64:67], v[132:135]\n"
    "ds_read_b64_tr_b16 a[144:145], v16 offset:8192\n"
    "v_mfma_f32_16x16x32_bf16 v[136:139], a[104:107], a[64:67], v[136:139]\n"
    "v_mfma_f32_16x16x32_bf16 v[140:143], a[108:111], a[64:67], v[140:143]\n"
    "ds_read_b64_tr_b16 a[146:147], v16 offset:12288\n"
    "v_mfma_f32_16x16x32_bf16 v[156:159], a[108:111], a[68:71], v[156:159]\n"
    "ds_read_b64_tr_b16 a[148:149], v17 offset:8192\n"
    "buffer_load_dwordx4 v21, s[12:15], s85 offen offset:3072 sc1 lds\n"
    "v_mfma_f32_16x16x32_bf16 v[152:155], a[104:107], a[68:71], v[152:155]\n"
    "v_mfma_f32_16x16x32_bf16 v[148:151], a[100:103], a[68:71], v[148:151]\n"
    "ds_read_b64_tr_b16 a[150:151], v17 offset:12288\n"
    "v_mfma_f32_16x16x32_bf16 v[144:147], a[96:99], a[68:71], v[144:147]\n"
    "ds_read_b64_tr_b16 a[152:153], v18 offset:8192\n"
    "v_mfma_f32_16x16x32_bf16 v[160:163], a[96:99], a[72:75], v[160:163]\n"
    "s_add_u32 m0, s98, 32768\n"
    "v_mfma_f32_16x16x32_bf16 v[164:167], a[100:103], a[72:75], v[164:167]\n"
    "ds_read_b64_tr_b16 a[154:155], v18 offset:12288\n"
    "buffer_load_dwordx4 v96, s[72:75], 0 offen offset:0 sc1 lds\n"
    "v_mfma_f32_16x16x32_bf16 v[168:171], a[104:107], a[72:75], v[168:171]\n"
    "ds_read_b64_tr_b16 a[156:157], v19 offset:8192\n"
    "v_mfma_f32_16x16x32_bf16 v[172:175], a[108:111], a[72:75], v[172:175]\n"
    "v_mfma_f32_16x16x32_bf16 v[188:191], a[108:111], a[76:79], v[188:191]\n"
    "ds_read_b64_tr_b16 a[158:159], v19 offset:12288\n"
    "v_mfma_f32_16x16x32_bf16 v[184:187], a[104:107], a[76:79], v[184:187]\n"
    "ds_read_b128 a[116:119], v15 offset:3072\n"
    "v_mfma_f32_16x16x32_bf16 v[180:183], a[100:103], a[76:79], v[180:183]\n"
    "buffer_load_dwordx4 v97, s[72:75], s86 offen offset:1024 sc1 lds\n"
    "v_mfma_f32_16x16x32_bf16 v[176:179], a[96:99], a[76:79], v[176:179]\n"
    "ds_read_b128 a[120:123], v15 offset:5120\n"
    "v_mfma_f32_16x16x32_bf16 v[192:195], a[96:99], a[80:83], v[192:195]\n"
    "ds_read_b128 a[124:127], v15 offset:7168\n"
    "v_mfma_f32_16x16x32_bf16 v[196:199], a[100:103], a[80:83], v[196:199]\n"
    "v_mfma_f32_16x16x32_bf16 v[200:203], a[104:107], a[80:83], v[200:203]\n"
    "ds_read_b128 a[128:131], v15 offset:9216\n"
    "v_mfma_f32_16x16x32_bf16 v[204:207], a[108:111], a[80:83], v[204:207]\n"
    "ds_read_b128 a[132:135], v15 offset:11264\n"
    "buffer_load_dwordx4 v96, s[72:75], s87 offen offset:2048 sc1 lds\n"
    "v_mfma_f32_16x16x32_bf16 v[220:223], a[108:111], a[84:87], v[220:223]\n"
    "v_mfma_f32_16x16x32_bf16 v[216:219], a[104:107], a[84:87], v[216:219]\n"
    "ds_read_b128 a[136:139], v15 offset:13312\n"
    "v_mfma_f32_16x16x32_bf16 v[212:215], a[100:103], a[84:87], v[212:215]\n"
    "ds_read_b128 a[140:143], v15 offset:15360\n"
    "v_mfma_f32_16x16x32_bf16 v[208:211], a[96:99], a[84:87], v[208:211]\n"
    "v_add_u32 v15, s96, v15\n"
    "v_mfma_f32_16x16x32_bf16 v[224:227], a[96:99], a[88:91], v[224:227]\n"
    "buffer_load_dwordx4 v97, s[72:75], s88 offen offset:3072 sc1 lds\n"
    "v_mfma_f32_16x16x32_bf16 v[228:231], a[100:103], a[88:91], v[228:231]\n"
    "v_add_u32 v16, s96, v16\n"
    "v_mfma_f32_16x16x32_bf16 v[232:235], a[104:107], a[88:91], v[232:235]\n"
    "v_add_u32 v17, s96, v17\n"
    "v_mfma_f32_16x16x32_bf16 v[236:239], a[108:111], a[88:91], v[236:239]\n"
    "v_add_u32 v18, s96, v18\n"
    "v_mfma_f32_16x16x32_bf16 v[252:255], a[108:111], a[92:95], v[252:255]\n"
    "v_add_u32 v19, s96, v19\n"
    "s_add_u32 s12, s12, 32768\n"
    "s_addc_u32 s13, s13, 0\n"
    "v_mfma_f32_16x16x32_bf16 v[248:251], a[104:107], a[92:95], v[248:251]\n"
    "s_add_u32 s72, s72, 131072\n"
    "s_addc_u32 s73, s73, 0\n"
    "v_mfma_f32_16x16x32_bf16 v[244:247], a[100:103], a[92:95], v[244:247]\n"
    "v_mfma_f32_16x16x32_bf16 v[240:243], a[96:99], a[92:95], v[240:243]\n"
    "s_waitcnt lgkmcnt(0) vmcnt(12)\n"
    "s_barrier\n"
    "ds_read_b128 v[88:91], v15 offset:0\n"
    "v_mfma_f32_16x16x32_bf16 v[128:131], a[144:147], a[112:115], v[128:131]\n"
    "ds_read_b64_tr_b16 a[48:49], v16 offset:0\n"
    "s_add_u32 m0, s92, 49152\n"
    "v_mfma_f32_16x16x32_bf16 v[132:135], a[148:151], a[112:115], v[132:135]\n"
    "buffer_load_dwordx4 v21, s[12:15], s84 offen offset:0 sc1 lds\n"
    "v_mfma_f32_16x16x32_bf16 v[136:139], a[152:155], a[112:115], v[136:139]\n"
    "ds_read_b64_tr_b16 a[50:51], v16 offset:4096\n"
    "v_mfma_f32_16x16x32_bf16 v[140:143], a[156:159], a[112:115], v[140:143]\n"
    "ds_read_b64_tr_b16 a[52:53], v17 offset:0\n"
    "v_mfma_f32_16x16x32_bf16 v[156:159], a[156:159], a[116:119], v[156:159]\n"
    "v_mfma_f32_16x16x32_bf16 v[152:155], a[152:155], a[116:119], v[152:155]\n"
    "ds_read_b64_tr_b16 a[54:55], v17 offset:4096\n"
    "buffer_load_dwordx4 v21, s[12:15], s84 offen offset:1024 sc1 lds\n"
    "v_mfma_f32_16x16x32_bf16 v[148:151], a[148:151], a[116:119], v[148:151]\n"
    "ds_read_b64_tr_b16 a[56:57], v18 offset:0\n"
    "v_mfma_f32_16x16x32_bf16 v[144:147], a[144:147], a[116:119], v[144:147]\n"
    "v_mfma_f32_16x16x32_bf16 v[160:163], a[144:147], a[120:123], v[160:163]\n"
    "ds_read_b64_tr_b16 a[58:59], v18 offset:4096\n"
    "v_mfma_f32_16x16x32_bf16 v[164:167], a[148:151], a[120:123], v[164:167]\n"
    "ds_read_b64_tr_b16 a[60:61], v19 offset:0\n"
    "buffer_load_dwordx4 v21, s[12:15], s84 offen offset:2048 sc1 lds\n"
    "v_mfma_f32_16x16x32_bf16 v[168:171], a[152:155], a[120:123], v[168:171]\n"
    "v_mfma_f32_16x16x32_bf16 v[172:175], a[156:159], a[120:123], v[172:175]\n"
    "ds_read_b64_tr_b16 a[62:63], v19 offset:4096\n"
    "v_mfma_f32_16x16x32_bf16 v[188:191], a[156:159], a[124:127], v[188:191]\n"
    "ds_read_b128 v[92:95], v15 offset:2048\n"
    "v_mfma_f32_16x16x32_bf16 v[184:187], a[152:155], a[124:127], v[184:187]\n"
    "buffer_load_dwordx4 v21, s[12:15], s84 offen offset:3072 sc1 lds\n"
    "v_mfma_f32_16x16x32_bf16 v[180:183], a[148:151], a[124:127], v[180:183]\n"
    "ds_read_b128 a[72:75], v15 offset:4096\n"
    "v_mfma_f32_16x16x32_bf16 v[176:179], a[144:147], a[124:127], v[176:179]\n"
    "ds_read_b128 a[76:79], v15 offset:6144\n"
    "v_mfma_f32_16x16x32_bf16 v[192:195], a[144:147], a[128:131], v[192:195]\n"
    "v_mfma_f32_16x16x32_bf16 v[196:199], a[148:151], a[128:131], v[196:199]\n"
    "ds_read_b128 a[80:83], v15 offset:8192\n"
    "s_add_u32 m0, s92, 53248\n"
    "v_mfma_f32_16x16x32_bf16 v[200:203], a[152:155], a[128:131], v[200:203]\n"
    "ds_read_b128 a[84:87], v15 offset:10240\n"
    "buffer_load_dwordx4 v21, s[12:15], s85 offen offset:0 sc1 lds\n"
    "v_mfma_f32_16x16x32_bf16 v[204:207], a[156:159], a[128:131], v[204:207]\n"
    "v_mfma_f32_16x16x32_bf16 v[220:223], a[156:159], a[132:135], v[220:223]\n"
    "ds_read_b128 a[88:91], v15 offset:12288\n"
    "v_mfma_f32_16x16x32_bf16 v[216:219], a[152:155], a[132:135], v[216:219]\n"
    "ds_read_b128 a[92:95], v15 offset:14336\n"
    "v_mfma_f32_16x16x32_bf16 v[212:215], a[148:151], a[132:135], v[212:215]\n"
    "buffer_load_dwordx4 v21, s[12:15], s85 offen offset:1024 sc1 lds\n"
    "v_mfma_f32_16x16x32_bf16 v[208:211], a[144:147], a[132:135], v[208:211]\n"
    "v_mfma_f32_16x16x32_bf16 v[224:227], a[144:147], a[136:139], v[224:227]\n"
    "v_mfma_f32_16x16x32_bf16 v[228:231], a[148:151], a[136:139], v[228:231]\n"
    "v_mfma_f32_16x16x32_bf16 v[232:235], a[152:155], a[136:139], v[232:235]\n"
    "buffer_load_dwordx4 v21, s[12:15], s85 offen offset:2048 sc1 lds\n"
    "v_mfma_f32_16x16x32_bf16 v[236:239], a[156:159], a[136:139], v[236:239]\n"
    "v_mfma_f32_16x16x32_bf16 v[252:255], a[156:159], a[140:143], v[252:255]\n"
    "v_mfma_f32_16x16x32_bf16 v[248:251], a[152:155], a[140:143], v[248:251]\n"
    "v_mfma_f32_16x16x32_bf16 v[244:247], a[148:151], a[140:143], v[244:247]\n"
    "v_mfma_f32_16x16x32_bf16 v[240:243], a[144:147], a[140:143], v[240:243]\n"
    "s_waitcnt lgkmcnt(0)\n"
    "buffer_load_dwordx4 v21, s[12:15], s85 offen offset:3072 sc1 lds\n"
    "v_mfma_f32_16x16x32_bf16 v[160:163], a[48:51], a[72:75], v[160:163]\n"
    "ds_read_b128 v[80:83], v15 offset:1024\n"
    "v_mfma_f32_16x16x32_bf16 v[164:167], a[52:55], a[72:75], v[164:167]\n"
    "ds_read_b64_tr_b16 v[64:65], v16 offset:8192\n"
    "v_mfma_f32_16x16x32_bf16 v[168:171], a[56:59], a[72:75], v[168:171]\n"
    "ds_read_b64_tr_b16 v[66:67], v16 offset:12288\n"
    "s_add_u32 m0, s98, 81920\n"
    "v_mfma_f32_16x16x32_bf16 v[172:175], a[60:63], a[72:75], v[172:175]\n"
    "ds_read_b64_tr_b16 v[68:69], v17 offset:8192\n"
    "buffer_load_dwordx4 v96, s[72:75], 0 offen offset:0 sc1 lds\n"
    "v_mfma_f32_16x16x32_bf16 v[188:191], a[60:63], a[76:79], v[188:191]\n"
    "ds_read_b64_tr_b16 v[70:71], v17 offset:12288\n"
    "v_mfma_f32_16x16x32_bf16 v[184:187], a[56:59], a[76:79], v[184:187]\n"
    "ds_read_b64_tr_b16 v[72:73], v18 offset:8192\n"
    "v_mfma_f32_16x16x32_bf16 v[180:183], a[52:55], a[76:79], v[180:183]\n"
    "ds_read_b64_tr_b16 v[74:75], v18 offset:12288\n"
    "v_mfma_f32_16x16x32_bf16 v[176:179], a[48:51], a[76:79], v[176:179]\n"
    "ds_read_b64_tr_b16 v[76:77], v19 offset:8192\n"
    "buffer_load_dwordx4 v97, s[72:75], s86 offen offset:1024 sc1 lds\n"
    "v_mfma_f32_16x16x32_bf16 v[192:195], a[48:51], a[80:83], v[192:195]\n"
    "ds_read_b64_tr_b16 v[78:79], v19 offset:12288\n"
    "v_mfma_f32_16x16x32_bf16 v[196:199], a[52:55], a[80:83], v[196:199]\n"
    "ds_read_b128 v[84:87], v15 offset:3072\n"
    "v_mfma_f32_16x16x32_bf16 v[200:203], a[56:59], a[80:83], v[200:203]\n"
    "v_mfma_f32_16x16x32_bf16 v[204:207], a[60:63], a[80:83], v[204:207]\n"
    "ds_read_b128 a[120:123], v15 offset:5120\n"
    "buffer_load_dwordx4 v96, s[72:75], s87 offen offset:2048 sc1 lds\n"
    "v_mfma_f32_16x16x32_bf16 v[220:223], a[60:63], a[84:87], v[220:223]\n"
    "ds_read_b128 a[124:127], v15 offset:7168\n"
    "v_mfma_f32_16x16x32_bf16 v[216:219], a[56:59], a[84:87], v[216:219]\n"
    "ds_read_b128 a[128:131], v15 offset:9216\n"
    "v_mfma_f32_16x16x32_bf16 v[212:215], a[52:55], a[84:87], v[212:215]\n"
    "ds_read_b128 a[132:135], v15 offset:11264\n"
    "v_mfma_f32_16x16x32_bf16 v[208:211], a[48:51], a[84:87], v[208:211]\n"
    "ds_read_b128 a[136:139], v15 offset:13312\n"
    "v_mfma_f32_16x16x32_bf16 v[224:227], a[48:51], a[88:91], v[224:227]\n"
    "ds_read_b128 a[140:143], v15 offset:15360\n"
    "buffer_load_dwordx4 v97, s[72:75], s88 offen offset:3072 sc1 lds\n"
    "v_mfma_f32_16x16x32_bf16 v[228:231], a[52:55], a[88:91], v[228:231]\n"
    "v_add_u32 v15, s97, v15\n"
    "v_mfma_f32_16x16x32_bf16 v[232:235], a[56:59], a[88:91], v[232:235]\n"
    "v_add_u32 v16, s97, v16\n"
    "v_mfma_f32_16x16x32_bf16 v[236:239], a[60:63], a[88:91], v[236:239]\n"
    "v_add_u32 v17, s97, v17\n"
    "v_mfma_f32_16x16x32_bf16 v[252:255], a[60:63], a[92:95], v[252:255]\n"
    "v_add_u32 v18, s97, v18\n"
    "s_add_u32 s12, s12, 32768\n"
    "s_addc_u32 s13, s13, 0\n"
    "v_mfma_f32_16x16x32_bf16 v[248:251], a[56:59], a[92:95], v[248:251]\n"
    "v_add_u32 v19, s97, v19\n"
    "s_add_u32 s72, s72, 131072\n"
    "s_addc_u32 s73, s73, 0\n"
    "v_mfma_f32_16x16x32_bf16 v[244:247], a[52:55], a[92:95], v[244:247]\n"
    "v_mfma_f32_16x16x32_bf16 v[240:243], a[48:51], a[92:95], v[240:243]\n"
    "s_waitcnt lgkmcnt(0) vmcnt(12)\n"
    "s_barrier\n"
    "ds_read_b128 v[120:123], v15 offset:0\n"
    "v_mfma_f32_16x16x32_bf16 v[160:163], v[64:67], a[120:123], v[160:163]\n"
    "ds_read_b64_tr_b16 v[104:105], v16 offset:0\n"
    "s_add_u32 m0, s92, 98304\n"
    "v_mfma_f32_16x16x32_bf16 v[164:167], v[68:71], a[120:123], v[164:167]\n"
    "ds_read_b64_tr_b16 v[106:107], v16 offset:4096\n"
    "buffer_load_dwordx4 v21, s[12:15], s84 offen offset:0 sc1 lds\n"
    "v_mfma_f32_16x16x32_bf16 v[168:171], v[72:75], a[120:123], v[168:171]\n"
    "ds_read_b64_tr_b16 v[108:109], v17 offset:0\n"
    "v_mfma_f32_16x16x32_bf16 v[172:175], v[76:79], a[120:123], v[172:175]\n"
    "ds_read_b64_tr_b16 v[110:111], v17 offset:4096\n"
    "v_mfma_f32_16x16x32_bf16 v[188:191], v[76:79], a[124:127], v[188:191]\n"
    "ds_read_b64_tr_b16 v[112:113], v18 offset:0\n"
    "buffer_load_dwordx4 v21, s[12:15], s84 offen offset:1024 sc1 lds\n"
    "v_mfma_f32_16x16x32_bf16 v[184:187], v[72:75], a[124:127], v[184:187]\n"
    "ds_read_b64_tr_b16 v[114:115], v18 offset:4096\n"
    "v_mfma_f32_16x16x32_bf16 v[180:183], v[68:71], a[124:127], v[180:183]\n"
    "ds_read_b64_tr_b16 v[116:117], v19 offset:0\n"
    "v_mfma_f32_16x16x32_bf16 v[176:179], v[64:67], a[124:127], v[176:179]\n"
    "ds_read_b64_tr_b16 v[118:119], v19 offset:4096\n"
    "v_mfma_f32_16x16x32_bf16 v[192:195], v[64:67], a[128:131], v[192:195]\n"
    "ds_read_b128 v[124:127], v15 offset:2048\n"
    "buffer_load_dwordx4 v21, s[12:15], s84 offen offset:2048 sc1 lds\n"
    "v_mfma_f32_16x16x32_bf16 v[196:199], v[68:71], a[128:131], v[196:199]\n"
    "v_mfma_f32_16x16x32_bf16 v[200:203], v[72:75], a[128:131], v[200:203]\n"
    "ds_read_b128 a[72:75], v15 offset:4096\n"
    "v_mfma_f32_16x16x32_bf16 v[204:207], v[76:79], a[128:131], v[204:207]\n"
    "ds_read_b128 a[76:79], v15 offset:6144\n"
    "buffer_load_dwordx4 v21, s[12:15], s84 offen offset:3072 sc1 lds\n"
    "v_mfma_f32_16x16x32_bf16 v[220:223], v[76:79], a[132:135], v[220:223]\n"
    "ds_read_b128 a[80:83], v15 offset:8192\n"
    "v_mfma_f32_16x16x32_bf16 v[216:219], v[72:75], a[132:135], v[216:219]\n"
    "ds_read_b128 a[84:87], v15 offset:10240\n"
    "v_mfma_f32_16x16x32_bf16 v[212:215], v[68:71], a[132:135], v[212:215]\n"
    "ds_read_b128 a[88:91], v15 offset:12288\n"
    "s_add_u32 m0, s92, 102400\n"
    "v_mfma_f32_16x16x32_bf16 v[208:211], v[64:67], a[132:135], v[208:211]\n"
    "ds_read_b128 a[92:95], v15 offset:14336\n"
    "buffer_load_dwordx4 v21, s[12:15], s85 offen offset:0 sc1 lds\n"
    "v_mfma_f32_16x16x32_bf16 v[224:227], v[64:67], a[136:139], v[224:227]\n"
    "v_mfma_f32_16x16x32_bf16 v[228:231], v[68:71], a[136:139], v[228:231]\n"
    "v_mfma_f32_16x16x32_bf16 v[232:235], v[72:75], a[136:139], v[232:235]\n"
    "buffer_load_dwordx4 v21, s[12:15], s85 offen offset:1024 sc1 lds\n"
    "v_mfma_f32_16x16x32_bf16 v[236:239], v[76:79], a[136:139], v[236:239]\n"
    "v_mfma_f32_16x16x32_bf16 v[252:255], v[76:79], a[140:143], v[252:255]\n"
    "v_mfma_f32_16x16x32_bf16 v[248:251], v[72:75], a[140:143], v[248:251]\n"
    "v_mfma_f32_16x16x32_bf16 v[244:247], v[68:71], a[140:143], v[244:247]\n"
    "buffer_load_dwordx4 v21, s[12:15], s85 offen offset:2048 sc1 lds\n"
    "v_mfma_f32_16x16x32_bf16 v[240:243], v[64:67], a[140:143], v[240:243]\n"
    "s_waitcnt lgkmcnt(0)\n"
    "v_mfma_f32_16x16x32_bf16 v[160:163], v[104:107], a[72:75], v[160:163]\n"
    "ds_read_b128 v[56:59], v15 offset:1024\n"
    "v_mfma_f32_16x16x32_bf16 v[164:167], v[108:111], a[72:75], v[164:167]\n"
    "ds_read_b64_tr_b16 v[40:41], v16 offset:8192\n"
    "v_mfma_f32_16x16x32_bf16 v[168:171], v[112:115], a[72:75], v[168:171]\n"
    "TC11:\n"
    "s_cbranch_execz TC15\n"
    "ds_read_b64_tr_b16 v[42:43], v16 offset:12288\n"
    "buffer_load_dwordx4 v21, s[12:15], s85 offen offset:3072 sc1 lds\n"
    "v_mfma_f32_16x16x32_bf16 v[172:175], v[116:119], a[72:75], v[172:175]\n"
    "ds_read_b64_tr_b16 v[44:45], v17 offset:8192\n"
    "v_mfma_f32_16x16x32_bf16 v[188:191], v[116:119], a[76:79], v[188:191]\n"
    "ds_read_b64_tr_b16 v[46:47], v17 offset:12288\n"
    "s_add_u32 m0, s98, 131072\n"
    "v_mfma_f32_16x16x32_bf16 v[184:187], v[112:115], a[76:79], v[184:187]\n"
    "ds_read_b64_tr_b16 v[48:49], v18 offset:8192\n"
    "buffer_load_dwordx4 v96, s[72:75], 0 offen offset:0 sc1 lds\n"
    "v_mfma_f32_16x16x32_bf16 v[180:183], v[108:111], a[76:79], v[180:183]\n"
    "ds_read_b64_tr_b16 v[50:51], v18 offset:12288\n"
    "v_mfma_f32_16x16x32_bf16 v[176:179], v[104:107], a[76:79], v[176:179]\n"
    "ds_read_b64_tr_b16 v[52:53], v19 offset:8192\n"
    "v_mfma_f32_16x16x32_bf16 v[192:195], v[104:107], a[80:83], v[192:195]\n"
    "ds_read_b64_tr_b16 v[54:55], v19 offset:12288\n"
    "v_mfma_f32_16x16x32_bf16 v[196:199], v[108:111], a[80:83], v[196:199]\n"
    "ds_read_b128 v[60:63], v15 offset:3072\n"
    "buffer_load_dwordx4 v97, s[72:75], s86 offen offset:1024 sc1 lds\n"
    "v_mfma_f32_16x16x32_bf16 v[200:203], v[112:115], a[80:83], v[200:203]\n"
    "v_mfma_f32_16x16x32_bf16 v[204:207], v[116:119], a[80:83], v[204:207]\n"
    "ds_read_b128 a[120:123], v15 offset:5120\n"
    "v_mfma_f32_16x16x32_bf16 v[220:223], v[116:119], a[84:87], v[220:223]\n"
    "ds_read_b128 a[124:127], v15 offset:7168\n"
    "buffer_load_dwordx4 v96, s[72:75], s87 offen offset:2048 sc1 lds\n"
    "v_mfma_f32_16x16x32_bf16 v[216:219], v[112:115], a[84:87], v[216:219]\n"
    "ds_read_b128 a[128:131], v15 offset:9216\n"
    "v_mfma_f32_16x16x32_bf16 v[212:215], v[108:111], a[84:87], v[212:215]\n"
    "ds_read_b128 a[132:135], v15 offset:11264\n"
    "v_mfma_f32_16x16x32_bf16 v[208:211], v[104:107], a[84:87], v[208:211]\n"
    "ds_read_b128 a[136:139], v15 offset:13312\n"
    "v_mfma_f32_16x16x32_bf16 v[224:227], v[104:107], a[88:91], v[224:227]\n"
    "ds_read_b128 a[140:143], v15 offset:15360\n"
    "buffer_load_dwordx4 v97, s[72:75], s88 offen offset:3072 sc1 lds\n"
    "v_mfma_f32_16x16x32_bf16 v[228:231], v[108:111], a[88:91], v[228:231]\n"
    "v_add_u32 v15, s96, v15\n"
    "v_mfma_f32_16x16x32_bf16 v[232:235], v[112:115], a[88:91], v[232:235]\n"
    "v_add_u32 v16, s96, v16\n"
    "v_mfma_f32_16x16x32_bf16 v[236:239], v[116:119], a[88:91], v[236:239]\n"
    "v_add_u32 v17, s96, v17\n"
    "v_mfma_f32_16x16x32_bf16 v[252:255], v[116:119], a[92:95], v[252:255]\n"
    "v_add_u32 v18, s96, v18\n"
    "s_add_u32 s12, s12, 32768\n"
    "s_addc_u32 s13, s13, 0\n"
    "v_mfma_f32_16x16x32_bf16 v[248:251], v[112:115], a[92:95], v[248:251]\n"
    "v_add_u32 v19, s96, v19\n"
    "s_add_u32 s72, s72, 131072\n"
    "s_addc_u32 s73, s73, 0\n"
    "v_mfma_f32_16x16x32_bf16 v[244:247], v[108:111], a[92:95], v[244:247]\n"
    "v_mfma_f32_16x16x32_bf16 v[240:243], v[104:107], a[92:95], v[240:243]\n"
    "s_waitcnt lgkmcnt(0) vmcnt(12)\n"
    "s_barrier\n"
    "ds_read_b128 a[40:43], v15 offset:0\n"
    "v_mfma_f32_16x16x32_bf16 v[160:163], v[40:43], a[120:123], v[160:163]\n"
    "ds_read_b64_tr_b16 a[24:25], v16 offset:0\n"
    "s_add_u32 m0, s92, 0\n"
    "v_mfma_f32_16x16x32_bf16 v[164:167], v[44:47], a[120:123], v[164:167]\n"
    "ds_read_b64_tr_b16 a[26:27], v16 offset:4096\n"
    "buffer_load_dwordx4 v21, s[12:15], s84 offen offset:0 sc1 lds\n"
    "v_mfma_f32_16x16x32_bf16 v[168:171], v[48:51], a[120:123], v[168:171]\n"
    "ds_read_b64_tr_b16 a[28:29], v17 offset:0\n"
    "v_mfma_f32_16x16x32_bf16 v[172:175], v[52:55], a[120:123], v[172:175]\n"
    "ds_read_b64_tr_b16 a[30:31], v17 offset:4096\n"
    "v_mfma_f32_16x16x32_bf16 v[188:191], v[52:55], a[124:127], v[188:191]\n"
    "ds_read_b64_tr_b16 a[32:33], v18 offset:0\n"
    "buffer_load_dwordx4 v21, s[12:15], s84 offen offset:1024 sc1 lds\n"
    "v_mfma_f32_16x16x32_bf16 v[184:187], v[48:51], a[124:127], v[184:187]\n"
    "ds_read_b64_tr_b16 a[34:35], v18 offset:4096\n"
    "v_mfma_f32_16x16x32_bf16 v[180:183], v[44:47], a[124:127], v[180:183]\n"
    "ds_read_b64_tr_b16 a[36:37], v19 offset:0\n"
    "v_mfma_f32_16x16x32_bf16 v[176:179], v[40:43], a[124:127], v[176:179]\n"
    "ds_read_b64_tr_b16 a[38:39], v19 offset:4096\n"
    "v_mfma_f32_16x16x32_bf16 v[192:195], v[40:43], a[128:131], v[192:195]\n"
    "ds_read_b128 a[44:47], v15 offset:2048\n"
    "buffer_load_dwordx4 v21, s[12:15], s84 offen offset:2048 sc1 lds\n"
    "v_mfma_f32_16x16x32_bf16 v[196:199], v[44:47], a[128:131], v[196:199]\n"
    "v_mfma_f32_16x16x32_bf16 v[200:203], v[48:51], a[128:131], v[200:203]\n"
    "ds_read_b128 a[72:75], v15 offset:4096\n"
    "v_mfma_f32_16x16x32_bf16 v[204:207], v[52:55], a[128:131], v[204:207]\n"
    "ds_read_b128 a[76:79], v15 offset:6144\n"
    "buffer_load_dwordx4 v21, s[12:15], s84 offen offset:3072 sc1 lds\n"
    "v_mfma_f32_16x16x32_bf16 v[220:223], v[52:55], a[132:135], v[220:223]\n"
    "ds_read_b128 a[80:83], v15 offset:8192\n"
    "v_mfma_f32_16x16x32_bf16 v[216:219], v[48:51], a[132:135], v[216:219]\n"
    "ds_read_b128 a[84:87], v15 offset:10240\n"
    "v_mfma_f32_16x16x32_bf16 v[212:215], v[44:47], a[132:135], v[212:215]\n"
    "ds_read_b128 a[88:91], v15 offset:12288\n"
    "s_add_u32 m0, s92, 4096\n"
    "v_mfma_f32_16x16x32_bf16 v[208:211], v[40:43], a[132:135], v[208:211]\n"
    "ds_read_b128 a[92:95], v15 offset:14336\n"
    "buffer_load_dwordx4 v21, s[12:15], s85 offen offset:0 sc1 lds\n"
    "v_mfma_f32_16x16x32_bf16 v[224:227], v[40:43], a[136:139], v[224:227]\n"
    "v_mfma_f32_16x16x32_bf16 v[228:231], v[44:47], a[136:139], v[228:231]\n"
    "v_mfma_f32_16x16x32_bf16 v[232:235], v[48:51], a[136:139], v[232:235]\n"
    "buffer_load_dwordx4 v21, s[12:15], s85 offen offset:1024 sc1 lds\n"
    "v_mfma_f32_16x16x32_bf16 v[236:239], v[52:55], a[136:139], v[236:239]\n"
    "v_mfma_f32_16x16x32_bf16 v[252:255], v[52:55], a[140:143], v[252:255]\n"
    "v_mfma_f32_16x16x32_bf16 v[248:251], v[48:51], a[140:143], v[248:251]\n"
    "v_mfma_f32_16x16x32_bf16 v[244:247], v[44:47], a[140:143], v[244:247]\n"
    "buffer_load_dwordx4 v21, s[12:15], s85 offen offset:2048 sc1 lds\n"
    "v_mfma_f32_16x16x32_bf16 v[240:243], v[40:43], a[140:143], v[240:243]\n"
    "s_waitcnt lgkmcnt(0)\n"
    "v_mfma_f32_16x16x32_bf16 v[160:163], a[24:27], a[72:75], v[160:163]\n"
    "ds_read_b128 a[16:19], v15 offset:1024\n"
    "v_mfma_f32_16x16x32_bf16 v[164:167], a[28:31], a[72:75], v[164:167]\n"
    "ds_read_b64_tr_b16 a[0:1], v16 offset:8192\n"
    "v_mfma_f32_16x16x32_bf16 v[168:171], a[32:35], a[72:75], v[168:171]\n"
    "ds_read_b64_tr_b16 a[2:3], v16 offset:12288\n"
    "buffer_load_dwordx4 v21, s[12:15], s85 offen offset:3072 sc1 lds\n"
    "v_mfma_f32_16x16x32_bf16 v[172:175], a[36:39], a[72:75], v[172:175]\n"
    "ds_read_b64_tr_b16 a[4:5], v17 offset:8192\n"
    "v_mfma_f32_16x16x32_bf16 v[188:191], a[36:39], a[76:79], v[188:191]\n"
    "ds_read_b64_tr_b16 a[6:7], v17 offset:12288\n"
    "s_add_u32 m0, s98, 32768\n"
    "v_mfma_f32_16x16x32_bf16 v[184:187], a[32:35], a[76:79], v[184:187]\n"
    "ds_read_b64_tr_b16 a[8:9], v18 offset:8192\n"
    "buffer_load_dwordx4 v96, s[72:75], 0 offen offset:0 sc1 lds\n"
    "v_mfma_f32_16x16x32_bf16 v[180:183], a[28:31], a[76:79], v[180:183]\n"
    "ds_read_b64_tr_b16 a[10:11], v18 offset:12288\n"
    "v_mfma_f32_16x16x32_bf16 v[176:179], a[24:27], a[76:79], v[176:179]\n"
    "ds_read_b64_tr_b16 a[12:13], v19 offset:8192\n"
    "v_mfma_f32_16x16x32_bf16 v[192:195], a[24:27], a[80:83], v[192:195]\n"
    "ds_read_b64_tr_b16 a[14:15], v19 offset:12288\n"
    "v_mfma_f32_16x16x32_bf16 v[196:199], a[28:31], a[80:83], v[196:199]\n"
    "ds_read_b128 a[20:23], v15 offset:3072\n"
    "buffer_load_dwordx4 v97, s[72:75], s86 offen offset:1024 sc1 lds\n"
    "v_mfma_f32_16x16x32_bf16 v[200:203], a[32:35], a[80:83], v[200:203]\n"
    "v_mfma_f32_16x16x32_bf16 v[204:207], a[36:39], a[80:83], v[204:207]\n"
    "ds_read_b128 a[120:123], v15 offset:5120\n"
    "v_mfma_f32_16x16x32_bf16 v[220:223], a[36:39], a[84:87], v[220:223]\n"
    "ds_read_b128 a[124:127], v15 offset:7168\n"
    "buffer_load_dwordx4 v96, s[72:75], s87 offen offset:2048 sc1 lds\n"
    "v_mfma_f32_16x16x32_bf16 v[216:219], a[32:35], a[84:87], v[216:219]\n"
    "ds_read_b128 a[128:131], v15 offset:9216\n"
    "v_mfma_f32_16x16x32_bf16 v[212:215], a[28:31], a[84:87], v[212:215]\n"
    "ds_read_b128 a[132:135], v15 offset:11264\n"
    "v_mfma_f32_16x16x32_bf16 v[208:211], a[24:27], a[84:87], v[208:211]\n"
    "ds_read_b128 a[136:139], v15 offset:13312\n"
    "v_mfma_f32_16x16x32_bf16 v[224:227], a[24:27], a[88:91], v[224:227]\n"
    "ds_read_b128 a[140:143], v15 offset:15360\n"
    "buffer_load_dwordx4 v97, s[72:75], s88 offen offset:3072 sc1 lds\n"
    "v_mfma_f32_16x16x32_bf16 v[228:231], a[28:31], a[88:91], v[228:231]\n"
    "v_add_u32 v15, s96, v15\n"
    "v_mfma_f32_16x16x32_bf16 v[232:235], a[32:35], a[88:91], v[232:235]\n"
    "v_add_u32 v16, s96, v16\n"
    "v_mfma_f32_16x16x32_bf16 v[236:239], a[36:39], a[88:91], v[236:239]\n"
    "v_add_u32 v17, s96, v17\n"
    "v_mfma_f32_16x16x32_bf16 v[252:255], a[36:39], a[92:95], v[252:255]\n"
    "v_add_u32 v18, s96, v18\n"
    "s_add_u32 s12, s12, 32768\n"
    "s_addc_u32 s13, s13, 0\n"
    "v_mfma_f32_16x16x32_bf16 v[248:251], a[32:35], a[92:95], v[248:251]\n"
    "v_add_u32 v19, s96, v19\n"
    "s_add_u32 s72, s72, 131072\n"
    "s_addc_u32 s73, s73, 0\n"
    "v_mfma_f32_16x16x32_bf16 v[244:247], a[28:31], a[92:95], v[244:247]\n"
    "v_mfma_f32_16x16x32_bf16 v[240:243], a[24:27], a[92:95], v[240:243]\n"
    "s_waitcnt lgkmcnt(0) vmcnt(12)\n"
    "s_barrier\n"
    "ds_read_b128 a[64:67], v15 offset:0\n"
    "v_mfma_f32_16x16x32_bf16 v[160:163], a[0:3], a[120:123], v[160:163]\n"
    "ds_read_b64_tr_b16 a[96:97], v16 offset:0\n"
    "s_add_u32 m0, s92, 49152\n"
    "v_mfma_f32_16x16x32_bf16 v[164:167], a[4:7], a[120:123], v[164:167]\n"
    "ds_read_b64_tr_b16 a[98:99], v16 offset:4096\n"
    "buffer_load_dwordx4 v21, s[12:15], s84 offen offset:0 sc1 lds\n"
    "v_mfma_f32_16x16x32_bf16 v[168:171], a[8:11], a[120:123], v[168:171]\n"
    "ds_read_b64_tr_b16 a[100:101], v17 offset:0\n"
    "v_mfma_f32_16x16x32_bf16 v[172:175], a[12:15], a[120:123], v[172:175]\n"
    "ds_read_b64_tr_b16 a[102:103], v17 offset:4096\n"
    "v_mfma_f32_16x16x32_bf16 v[188:191], a[12:15], a[124:127], v[188:191]\n"
    "ds_read_b64_tr_b16 a[104:105], v18 offset:0\n"
    "buffer_load_dwordx4 v21, s[12:15], s84 offen offset:1024 sc1 lds\n"
    "v_mfma_f32_16x16x32_bf16 v[184:187], a[8:11], a[124:127], v[184:187]\n"
    "ds_read_b64_tr_b16 a[106:107], v18 offset:4096\n"
    "v_mfma_f32_16x16x32_bf16 v[180:183], a[4:7], a[124:127], v[180:183]\n"
    "ds_read_b64_tr_b16 a[108:109], v19 offset:0\n"
    "v_mfma_f32_16x16x32_bf16 v[176:179], a[0:3], a[124:127], v[176:179]\n"
    "ds_read_b64_tr_b16 a[110:111], v19 offset:4096\n"
    "v_mfma_f32_16x16x32_bf16 v[192:195], a[0:3], a[128:131], v[192:195]\n"
    "ds_read_b128 a[68:71], v15 offset:2048\n"
    "buffer_load_dwordx4 v21, s[12:15], s84 offen offset:2048 sc1 lds\n"
    "v_mfma_f32_16x16x32_bf16 v[196:199], a[4:7], a[128:131], v[196:199]\n"
    "v_mfma_f32_16x16x32_bf16 v[200:203], a[8:11], a[128:131], v[200:203]\n"
    "ds_read_b128 a[72:75], v15 offset:4096\n"
    "v_mfma_f32_16x16x32_bf16 v[204:207], a[12:15], a[128:131], v[204:207]\n"
    "ds_read_b128 a[76:79], v15 offset:6144\n"
    "buffer_load_dwordx4 v21, s[12:15], s84 offen offset:3072 sc1 lds\n"
    "v_mfma_f32_16x16x32_bf16 v[220:223], a[12:15], a[132:135], v[220:223]\n"
    "ds_read_b128 a[80:83], v15 offset:8192\n"
    "v_mfma_f32_16x16x32_bf16 v[216:219], a[8:11], a[132:135], v[216:219]\n"
    "ds_read_b128 a[84:87], v15 offset:10240\n"
    "v_mfma_f32_16x16x32_bf16 v[212:215], a[4:7], a[132:135], v[212:215]\n"
    "ds_read_b128 a[88:91], v15 offset:12288\n"
    "s_add_u32 m0, s92, 53248\n"
    "v_mfma_f32_16x16x32_bf16 v[208:211], a[0:3], a[132:135], v[208:211]\n"
    "ds_read_b128 a[92:95], v15 offset:14336\n"
    "buffer_load_dwordx4 v21, s[12:15], s85 offen offset:0 sc1 lds\n"
    "v_mfma_f32_16x16x32_bf16 v[224:227], a[0:3], a[136:139], v[224:227]\n"
    "v_mfma_f32_16x16x32_bf16 v[228:231], a[4:7], a[136:139], v[228:231]\n"
    "v_mfma_f32_16x16x32_bf16 v[232:235], a[8:11], a[136:139], v[232:235]\n"
    "buffer_load_dwordx4 v21, s[12:15], s85 offen offset:1024 sc1 lds\n"
    "v_mfma_f32_16x16x32_bf16 v[236:239], a[12:15], a[136:139], v[236:239]\n"
    "v_mfma_f32_16x16x32_bf16 v[252:255], a[12:15], a[140:143], v[252:255]\n"
    "v_mfma_f32_16x16x32_bf16 v[248:251], a[8:11], a[140:143], v[248:251]\n"
    "v_mfma_f32_16x16x32_bf16 v[244:247], a[4:7], a[140:143], v[244:247]\n"
    "buffer_load_dwordx4 v21, s[12:15], s85 offen offset:2048 sc1 lds\n"
    "v_mfma_f32_16x16x32_bf16 v[240:243], a[0:3], a[140:143], v[240:243]\n"
    "s_waitcnt lgkmcnt(0)\n"
    "v_mfma_f32_16x16x32_bf16 v[160:163], a[96:99], a[72:75], v[160:163]\n"
    "ds_read_b128 a[112:115], v15 offset:1024\n"
    "v_mfma_f32_16x16x32_bf16 v[164:167], a[100:103], a[72:75], v[164:167]\n"
    "ds_read_b64_tr_b16 a[144:145], v16 offset:8192\n"
    "v_mfma_f32_16x16x32_bf16 v[168:171], a[104:107], a[72:75], v[168:171]\n"
    "ds_read_b64_tr_b16 a[146:147], v16 offset:12288\n"
    "buffer_load_dwordx4 v21, s[12:15], s85 offen offset:3072 sc1 lds\n"
    "v_mfma_f32_16x16x32_bf16 v[172:175], a[108:111], a[72:75], v[172:175]\n"
    "ds_read_b64_tr_b16 a[148:149], v17 offset:8192\n"
    "v_mfma_f32_16x16x32_bf16 v[188:191], a[108:111], a[76:79], v[188:191]\n"
    "ds_read_b64_tr_b16 a[150:151], v17 offset:12288\n"
    "s_add_u32 m0, s98, 81920\n"
    "v_mfma_f32_16x16x32_bf16 v[184:187], a[104:107], a[76:79], v[184:187]\n"
    "ds_read_b64_tr_b16 a[152:153], v18 offset:8192\n"
    "buffer_load_dwordx4 v96, s[72:75], 0 offen offset:0 sc1 lds\n"
    "v_mfma_f32_16x16x32_bf16 v[180:183], a[100:103], a[76:79], v[180:183]\n"
    "ds_read_b64_tr_b16 a[154:155], v18 offset:12288\n"
    "v_mfma_f32_16x16x32_bf16 v[176:179], a[96:99], a[76:79], v[176:179]\n"
    "ds_read_b64_tr_b16 a[156:157], v19 offset:8192\n"
    "v_mfma_f32_16x16x32_bf16 v[192:195], a[96:99], a[80:83], v[192:195]\n"
    "ds_read_b64_tr_b16 a[158:159], v19 offset:12288\n"
    "v_mfma_f32_16x16x32_bf16 v[196:199], a[100:103], a[80:83], v[196:199]\n"
    "ds_read_b128 a[116:119], v15 offset:3072\n"
    "buffer_load_dwordx4 v97, s[72:75], s86 offen offset:1024 sc1 lds\n"
    "v_mfma_f32_16x16x32_bf16 v[200:203], a[104:107], a[80:83], v[200:203]\n"
    "v_mfma_f32_16x16x32_bf16 v[204:207], a[108:111], a[80:83], v[204:207]\n"
    "ds_read_b128 a[120:123], v15 offset:5120\n"
    "v_mfma_f32_16x16x32_bf16 v[220:223], a[108:111], a[84:87], v[220:223]\n"
    "ds_read_b128 a[124:127], v15 offset:7168\n"
    "buffer_load_dwordx4 v96, s[72:75], s87 offen offset:2048 sc1 lds\n"
    "v_mfma_f32_16x16x32_bf16 v[216:219], a[104:107], a[84:87], v[216:219]\n"
    "ds_read_b128 a[128:131], v15 offset:9216\n"
    "v_mfma_f32_16x16x32_bf16 v[212:215], a[100:103], a[84:87], v[212:215]\n"
    "ds_read_b128 a[132:135], v15 offset:11264\n"
    "v_mfma_f32_16x16x32_bf16 v[208:211], a[96:99], a[84:87], v[208:211]\n"
    "ds_read_b128 a[136:139], v15 offset:13312\n"
    "v_mfma_f32_16x16x32_bf16 v[224:227], a[96:99], a[88:91], v[224:227]\n"
    "ds_read_b128 a[140:143], v15 offset:15360\n"
    "buffer_load_dwordx4 v97, s[72:75], s88 offen offset:3072 sc1 lds\n"
    "v_mfma_f32_16x16x32_bf16 v[228:231], a[100:103], a[88:91], v[228:231]\n"
    "v_add_u32 v15, s97, v15\n"
    "v_mfma_f32_16x16x32_bf16 v[232:235], a[104:107], a[88:91], v[232:235]\n"
    "v_add_u32 v16, s97, v16\n"
    "v_mfma_f32_16x16x32_bf16 v[236:239], a[108:111], a[88:91], v[236:239]\n"
    "v_add_u32 v17, s97, v17\n"
    "v_mfma_f32_16x16x32_bf16 v[252:255], a[108:111], a[92:95], v[252:255]\n"
    "v_add_u32 v18, s97, v18\n"
    "s_add_u32 s12, s12, 32768\n"
    "s_addc_u32 s13, s13, 0\n"
    "v_mfma_f32_16x16x32_bf16 v[248:251], a[104:107], a[92:95], v[248:251]\n"
    "v_add_u32 v19, s97, v19\n"
    "s_add_u32 s72, s72, 131072\n"
    "s_addc_u32 s73, s73, 0\n"
    "v_mfma_f32_16x16x32_bf16 v[244:247], a[100:103], a[92:95], v[244:247]\n"
    "v_mfma_f32_16x16x32_bf16 v[240:243], a[96:99], a[92:95], v[240:243]\n"
    "s_waitcnt lgkmcnt(0) vmcnt(12)\n"
    "s_barrier\n"
    "ds_read_b128 a[72:75], v15 offset:4096\n"
    "v_mfma_f32_16x16x32_bf16 v[160:163], a[144:147], a[120:123], v[160:163]\n"
    "s_add_u32 m0, s92, 98304\n"
    "v_add_u32 v12, 98304, v15\n"
    "ds_read_b64_tr_b16 a[160:161], v16 offset:0\n"
    "v_mfma_f32_16x16x32_bf16 v[164:167], a[148:151], a[120:123], v[164:167]\n"
    "buffer_load_dwordx4 v21, s[12:15], s84 offen offset:0 sc1 lds\n"
    "ds_read_b64_tr_b16 a[162:163], v16 offset:4096\n"
    "v_mfma_f32_16x16x32_bf16 v[168:171], a[152:155], a[120:123], v[168:171]\n"
    "ds_read_b64_tr_b16 a[164:165], v17 offset:0\n"
    "v_mfma_f32_16x16x32_bf16 v[172:175], a[156:159], a[120:123], v[172:175]\n"
    "ds_read_b64_tr_b16 a[166:167], v17 offset:4096\n"
    "v_mfma_f32_16x16x32_bf16 v[188:191], a[156:159], a[124:127], v[188:191]\n"
    "buffer_load_dwordx4 v21, s[12:15], s84 offen offset:1024 sc1 lds\n"
    "ds_read_b64_tr_b16 a[168:169], v18 offset:0\n"
    "v_mfma_f32_16x16x32_bf16 v[184:187], a[152:155], a[124:127], v[184:187]\n"
    "ds_read_b64_tr_b16 a[170:171], v18 offset:4096\n"
    "v_mfma_f32_16x16x32_bf16 v[180:183], a[148:151], a[124:127], v[180:183]\n"
    "ds_read_b64_tr_b16 a[172:173], v19 offset:0\n"
    "v_mfma_f32_16x16x32_bf16 v[176:179], a[144:147], a[124:127], v[176:179]\n"
    "buffer_load_dwordx4 v21, s[12:15], s84 offen offset:2048 sc1 lds\n"
    "ds_read_b64_tr_b16 a[174:175], v19 offset:4096\n"
    "v_mfma_f32_16x16x32_bf16 v[192:195], a[144:147], a[128:131], v[192:195]\n"
    "ds_read_b128 a[76:79], v15 offset:6144\n"
    "v_mfma_f32_16x16x32_bf16 v[196:199], a[148:151], a[128:131], v[196:199]\n"
    "ds_read_b128 a[80:83], v15 offset:8192\n"
    "v_mfma_f32_16x16x32_bf16 v[200:203], a[152:155], a[128:131], v[200:203]\n"
    "buffer_load_dwordx4 v21, s[12:15], s84 offen offset:3072 sc1 lds\n"
    "ds_read_b128 a[84:87], v15 offset:10240\n"
    "v_mfma_f32_16x16x32_bf16 v[204:207], a[156:159], a[128:131], v[204:207]\n"
    "v_mfma_f32_16x16x32_bf16 v[220:223], a[156:159], a[132:135], v[220:223]\n"
    "s_add_u32 m0, s92, 102400\n"
    "v_mfma_f32_16x16x32_bf16 v[216:219], a[152:155], a[132:135], v[216:219]\n"
    "buffer_load_dwordx4 v21, s[12:15], s85 offen offset:0 sc1 lds\n"
    "v_mfma_f32_16x16x32_bf16 v[212:215], a[148:151], a[132:135], v[212:215]\n"
    "v_mfma_f32_16x16x32_bf16 v[208:211], a[144:147], a[132:135], v[208:211]\n"
    "ds_read_b128 a[88:91], v15 offset:5120\n"
    "v_mfma_f32_16x16x32_bf16 v[224:227], a[144:147], a[136:139], v[224:227]\n"
    "buffer_load_dwordx4 v21, s[12:15], s85 offen offset:1024 sc1 lds\n"
    "ds_read_b64_tr_b16 a[176:177], v16 offset:8192\n"
    "v_mfma_f32_16x16x32_bf16 v[228:231], a[148:151], a[136:139], v[228:231]\n"
    "ds_read_b64_tr_b16 a[178:179], v16 offset:12288\n"
    "v_mfma_f32_16x16x32_bf16 v[232:235], a[152:155], a[136:139], v[232:235]\n"
    "ds_read_b64_tr_b16 a[180:181], v17 offset:8192\n"
    "v_mfma_f32_16x16x32_bf16 v[236:239], a[156:159], a[136:139], v[236:239]\n"
    "buffer_load_dwordx4 v21, s[12:15], s85 offen offset:2048 sc1 lds\n"
    "ds_read_b64_tr_b16 a[182:183], v17 offset:12288\n"
    "v_mfma_f32_16x16x32_bf16 v[252:255], a[156:159], a[140:143], v[252:255]\n"
    "ds_read_b64_tr_b16 a[184:185], v18 offset:8192\n"
    "v_mfma_f32_16x16x32_bf16 v[248:251], a[152:155], a[140:143], v[248:251]\n"
    "ds_read_b64_tr_b16 a[186:187], v18 offset:12288\n"
    "v_mfma_f32_16x16x32_bf16 v[244:247], a[148:151], a[140:143], v[244:247]\n"
    "buffer_load_dwordx4 v21, s[12:15], s85 offen offset:3072 sc1 lds\n"
    "ds_read_b64_tr_b16 a[188:189], v19 offset:8192\n"
    "v_mfma_f32_16x16x32_bf16 v[240:243], a[144:147], a[140:143], v[240:243]\n"
    "ds_read_b64_tr_b16 a[190:191], v19 offset:12288\n"
    "s_waitcnt lgkmcnt(15)\n"
    "v_mfma_f32_16x16x32_bf16 v[160:163], a[160:163], a[72:75], v[160:163]\n"
    "s_add_u32 m0, s98, 131072\n"
    "ds_read_b128 a[92:95], v15 offset:7168\n"
    "v_mfma_f32_16x16x32_bf16 v[164:167], a[164:167], a[72:75], v[164:167]\n"
    "buffer_load_dwordx4 v96, s[72:75], 0 offen offset:0 sc1 lds\n"
    "ds_read_b128 a[120:123], v15 offset:9216\n"
    "s_waitcnt lgkmcnt(15)\n"
    "v_mfma_f32_16x16x32_bf16 v[168:171], a[168:171], a[72:75], v[168:171]\n"
    "ds_read_b128 a[124:127], v15 offset:11264\n"
    "s_waitcnt lgkmcnt(15)\n"
    "v_mfma_f32_16x16x32_bf16 v[172:175], a[172:175], a[72:75], v[172:175]\n"
    "s_waitcnt lgkmcnt(14)\n"
    "v_mfma_f32_16x16x32_bf16 v[188:191], a[172:175], a[76:79], v[188:191]\n"
    "buffer_load_dwordx4 v97, s[72:75], s86 offen offset:1024 sc1 lds\n"
    "v_mfma_f32_16x16x32_bf16 v[184:187], a[168:171], a[76:79], v[184:187]\n"
    "v_mfma_f32_16x16x32_bf16 v[180:183], a[164:167], a[76:79], v[180:183]\n"
    "v_mfma_f32_16x16x32_bf16 v[176:179], a[160:163], a[76:79], v[176:179]\n"
    "buffer_load_dwordx4 v96, s[72:75], s87 offen offset:2048 sc1 lds\n"
    "ds_read_b128 a[128:131], v15 offset:12288\n"
    "s_waitcnt lgkmcnt(14)\n"
    "v_mfma_f32_16x16x32_bf16 v[192:195], a[160:163], a[80:83], v[192:195]\n"
    "ds_read_b128 a[132:135], v15 offset:14336\n"
    "v_mfma_f32_16x16x32_bf16 v[196:199], a[164:167], a[80:83], v[196:199]\n"
    "v_mfma_f32_16x16x32_bf16 v[200:203], a[168:171], a[80:83], v[200:203]\n"
    "buffer_load_dwordx4 v97, s[72:75], s88 offen offset:3072 sc1 lds\n"
    "v_mfma_f32_16x16x32_bf16 v[204:207], a[172:175], a[80:83], v[204:207]\n"
    "s_waitcnt lgkmcnt(14)\n"
    "v_mfma_f32_16x16x32_bf16 v[220:223], a[172:175], a[84:87], v[220:223]\n"
    "v_mfma_f32_16x16x32_bf16 v[216:219], a[168:171], a[84:87], v[216:219]\n"
    "v_mfma_f32_16x16x32_bf16 v[212:215], a[164:167], a[84:87], v[212:215]\n"
    "v_mfma_f32_16x16x32_bf16 v[208:211], a[160:163], a[84:87], v[208:211]\n"
    "ds_read_b128 a[136:139], v15 offset:13312\n"
    "s_waitcnt lgkmcnt(12)\n"
    "v_mfma_f32_16x16x32_bf16 v[160:163], a[176:179], a[88:91], v[160:163]\n"
    "ds_read_b128 a[140:143], v15 offset:15360\n"
    "s_waitcnt lgkmcnt(11)\n"
    "v_mfma_f32_16x16x32_bf16 v[164:167], a[180:183], a[88:91], v[164:167]\n"
    "s_waitcnt lgkmcnt(9)\n"
    "v_mfma_f32_16x16x32_bf16 v[168:171], a[184:187], a[88:91], v[168:171]\n"
    "s_waitcnt lgkmcnt(7)\n"
    "v_mfma_f32_16x16x32_bf16 v[172:175], a[188:191], a[88:91], v[172:175]\n"
    "s_waitcnt lgkmcnt(6)\n"
    "v_mfma_f32_16x16x32_bf16 v[188:191], a[188:191], a[92:95], v[188:191]\n"
    "v_mfma_f32_16x16x32_bf16 v[184:187], a[184:187], a[92:95], v[184:187]\n"
    "v_mfma_f32_16x16x32_bf16 v[180:183], a[180:183], a[92:95], v[180:183]\n"
    "v_mfma_f32_16x16x32_bf16 v[176:179], a[176:179], a[92:95], v[176:179]\n"
    "s_waitcnt lgkmcnt(5)\n"
    "v_mfma_f32_16x16x32_bf16 v[192:195], a[176:179], a[120:123], v[192:195]\n"
    "v_mfma_f32_16x16x32_bf16 v[196:199], a[180:183], a[120:123], v[196:199]\n"
    "v_mfma_f32_16x16x32_bf16 v[200:203], a[184:187], a[120:123], v[200:203]\n"
    "v_mfma_f32_16x16x32_bf16 v[204:207], a[188:191], a[120:123], v[204:207]\n"
    "s_waitcnt lgkmcnt(4)\n"
    "v_mfma_f32_16x16x32_bf16 v[220:223], a[188:191], a[124:127], v[220:223]\n"
    "v_mfma_f32_16x16x32_bf16 v[216:219], a[184:187], a[124:127], v[216:219]\n"
    "v_mfma_f32_16x16x32_bf16 v[212:215], a[180:183], a[124:127], v[212:215]\n"
    "v_mfma_f32_16x16x32_bf16 v[208:211], a[176:179], a[124:127], v[208:211]\n"
    "s_waitcnt vmcnt(12)\n"
    "s_barrier\n"
    "ds_read_b128 a[72:75], v15 offset:53248\n"
    "s_waitcnt lgkmcnt(4)\n"
    "v_mfma_f32_16x16x32_bf16 v[224:227], a[160:163], a[128:131], v[224:227]\n"
    "ds_read_b64_tr_b16 a[192:193], v16 offset:49152\n"
    "v_mfma_f32_16x16x32_bf16 v[228:231], a[164:167], a[128:131], v[228:231]\n"
    "ds_read_b64_tr_b16 a[194:195], v16 offset:53248\n"
    "v_mfma_f32_16x16x32_bf16 v[232:235], a[168:171], a[128:131], v[232:235]\n"
    "ds_read_b64_tr_b16 a[196:197], v17 offset:49152\n"
    "v_mfma_f32_16x16x32_bf16 v[236:239], a[172:175], a[128:131], v[236:239]\n"
    "ds_read_b64_tr_b16 a[198:199], v17 offset:53248\n"
    "s_waitcnt lgkmcnt(7)\n"
    "v_mfma_f32_16x16x32_bf16 v[252:255], a[172:175], a[132:135], v[252:255]\n"
    "ds_read_b64_tr_b16 a[200:201], v18 offset:49152\n"
    "v_mfma_f32_16x16x32_bf16 v[248:251], a[168:171], a[132:135], v[248:251]\n"
    "ds_read_b64_tr_b16 a[202:203], v18 offset:53248\n"
    "v_mfma_f32_16x16x32_bf16 v[244:247], a[164:167], a[132:135], v[244:247]\n"
    "ds_read_b64_tr_b16 a[204:205], v19 offset:49152\n"
    "v_mfma_f32_16x16x32_bf16 v[240:243], a[160:163], a[132:135], v[240:243]\n"
    "ds_read_b64_tr_b16 a[206:207], v19 offset:53248\n"
    "s_waitcnt lgkmcnt(10)\n"
    "v_mfma_f32_16x16x32_bf16 v[224:227], a[176:179], a[136:139], v[224:227]\n"
    "ds_read_b128 a[76:79], v15 offset:55296\n"
    "v_mfma_f32_16x16x32_bf16 v[228:231], a[180:183], a[136:139], v[228:231]\n"
    "ds_read_b128 a[80:83], v15 offset:57344\n"
    "v_mfma_f32_16x16x32_bf16 v[232:235], a[184:187], a[136:139], v[232:235]\n"
    "ds_read_b128 a[84:87], v15 offset:59392\n"
    "v_mfma_f32_16x16x32_bf16 v[236:239], a[188:191], a[136:139], v[236:239]\n"
    "ds_read_b128 a[88:91], v15 offset:54272\n"
    "s_waitcnt lgkmcnt(13)\n"
    "v_mfma_f32_16x16x32_bf16 v[252:255], a[188:191], a[140:143], v[252:255]\n"
    "ds_read_b64_tr_b16 a[208:209], v16 offset:57344\n"
    "v_mfma_f32_16x16x32_bf16 v[248:251], a[184:187], a[140:143], v[248:251]\n"
    "ds_read_b64_tr_b16 a[210:211], v16 offset:61440\n"
    "v_mfma_f32_16x16x32_bf16 v[244:247], a[180:183], a[140:143], v[244:247]\n"
    "ds_read_b64_tr_b16 a[212:213], v17 offset:57344\n"
    "v_mfma_f32_16x16x32_bf16 v[240:243], a[176:179], a[140:143], v[240:243]\n"
    "ds_read_b64_tr_b16 a[214:215], v17 offset:61440\n"
    "s_waitcnt lgkmcnt(14)\n"
    "v_mfma_f32_16x16x32_bf16 v[160:163], a[192:195], a[72:75], v[160:163]\n"
    "ds_read_b64_tr_b16 a[216:217], v18 offset:57344\n"
    "s_waitcnt lgkmcnt(13)\n"
    "v_mfma_f32_16x16x32_bf16 v[164:167], a[196:199], a[72:75], v[164:167]\n"
    "ds_read_b64_tr_b16 a[218:219], v18 offset:61440\n"
    "s_waitcnt lgkmcnt(12)\n"
    "v_mfma_f32_16x16x32_bf16 v[168:171], a[200:203], a[72:75], v[168:171]\n"
    "ds_read_b64_tr_b16 a[220:221], v19 offset:57344\n"
    "s_waitcnt lgkmcnt(11)\n"
    "v_mfma_f32_16x16x32_bf16 v[172:175], a[204:207], a[72:75], v[172:175]\n"
    "ds_read_b64_tr_b16 a[222:223], v19 offset:61440\n"
    "s_waitcnt lgkmcnt(11)\n"
    "v_mfma_f32_16x16x32_bf16 v[188:191], a[204:207], a[76:79], v[188:191]\n"
    "ds_read_b128 a[92:95], v15 offset:56320\n"
    "v_mfma_f32_16x16x32_bf16 v[184:187], a[200:203], a[76:79], v[184:187]\n"
    "ds_read_b128 a[120:123], v15 offset:58368\n"
    "v_mfma_f32_16x16x32_bf16 v[180:183], a[196:199], a[76:79], v[180:183]\n"
    "ds_read_b128 a[124:127], v15 offset:60416\n"
    "v_mfma_f32_16x16x32_bf16 v[176:179], a[192:195], a[76:79], v[176:179]\n"
    "s_waitcnt lgkmcnt(13)\n"
    "v_mfma_f32_16x16x32_bf16 v[192:195], a[192:195], a[80:83], v[192:195]\n"
    "v_mfma_f32_16x16x32_bf16 v[196:199], a[196:199], a[80:83], v[196:199]\n"
    "v_mfma_f32_16x16x32_bf16 v[200:203], a[200:203], a[80:83], v[200:203]\n"
    "v_mfma_f32_16x16x32_bf16 v[204:207], a[204:207], a[80:83], v[204:207]\n"
    "s_waitcnt lgkmcnt(12)\n"
    "v_mfma_f32_16x16x32_bf16 v[220:223], a[204:207], a[84:87], v[220:223]\n"
    "v_mfma_f32_16x16x32_bf16 v[216:219], a[200:203], a[84:87], v[216:219]\n"
    "v_add_u32 v16, 98304, v16\n"
    "v_add_u32 v17, 98304, v17\n"
    "v_mfma_f32_16x16x32_bf16 v[212:215], a[196:199], a[84:87], v[212:215]\n"
    "v_add_u32 v18, 98304, v18\n"
    "v_add_u32 v19, 98304, v19\n"
    "v_mfma_f32_16x16x32_bf16 v[208:211], a[192:195], a[84:87], v[208:211]\n"
    "s_waitcnt vmcnt(0)\n"
    "s_barrier\n"
    "ds_read_b128 a[128:131], v12 offset:4096\n"
    "s_waitcnt lgkmcnt(10)\n"
    "v_mfma_f32_16x16x32_bf16 v[160:163], a[208:211], a[88:91], v[160:163]\n"
    "s_mov_b32 s86, 7\n"
    "ds_read_b64_tr_b16 a[224:225], v16 offset:0\n"
    "s_waitcnt lgkmcnt(9)\n"
    "v_mfma_f32_16x16x32_bf16 v[164:167], a[212:215], a[88:91], v[164:167]\n"
    "ds_read_b64_tr_b16 a[226:227], v16 offset:4096\n"
    "s_waitcnt lgkmcnt(8)\n"
    "v_mfma_f32_16x16x32_bf16 v[168:171], a[216:219], a[88:91], v[168:171]\n"
    "ds_read_b64_tr_b16 a[228:229], v17 offset:0\n"
    "s_waitcnt lgkmcnt(7)\n"
    "v_mfma_f32_16x16x32_bf16 v[172:175], a[220:223], a[88:91], v[172:175]\n"
    "ds_read_b64_tr_b16 a[230:231], v17 offset:4096\n"
    "s_waitcnt lgkmcnt(7)\n"
    "v_mfma_f32_16x16x32_bf16 v[188:191], a[220:223], a[92:95], v[188:191]\n"
    "ds_read_b64_tr_b16 a[232:233], v18 offset:0\n"
    "v_mfma_f32_16x16x32_bf16 v[184:187], a[216:219], a[92:95], v[184:187]\n"
    "ds_read_b64_tr_b16 a[234:235], v18 offset:4096\n"
    "v_mfma_f32_16x16x32_bf16 v[180:183], a[212:215], a[92:95], v[180:183]\n"
    "ds_read_b64_tr_b16 a[236:237], v19 offset:0\n"
    "v_mfma_f32_16x16x32_bf16 v[176:179], a[208:211], a[92:95], v[176:179]\n"
    "ds_read_b64_tr_b16 a[238:239], v19 offset:4096\n"
    "s_waitcnt lgkmcnt(10)\n"
    "v_mfma_f32_16x16x32_bf16 v[192:195], a[208:211], a[120:123], v[192:195]\n"
    "ds_read_b128 a[132:135], v12 offset:5120\n"
    "v_mfma_f32_16x16x32_bf16 v[196:199], a[212:215], a[120:123], v[196:199]\n"
    "ds_read_b64_tr_b16 a[240:241], v16 offset:8192\n"
    "v_mfma_f32_16x16x32_bf16 v[200:203], a[216:219], a[120:123], v[200:203]\n"
    "ds_read_b64_tr_b16 a[242:243], v16 offset:12288\n"
    "v_mfma_f32_16x16x32_bf16 v[204:207], a[220:223], a[120:123], v[204:207]\n"
    "ds_read_b64_tr_b16 a[244:245], v17 offset:8192\n"
    "s_waitcnt lgkmcnt(13)\n"
    "v_mfma_f32_16x16x32_bf16 v[220:223], a[220:223], a[124:127], v[220:223]\n"
    "ds_read_b64_tr_b16 a[246:247], v17 offset:12288\n"
    "v_mfma_f32_16x16x32_bf16 v[216:219], a[216:219], a[124:127], v[216:219]\n"
    "ds_read_b64_tr_b16 a[248:249], v18 offset:8192\n"
    "v_mfma_f32_16x16x32_bf16 v[212:215], a[212:215], a[124:127], v[212:215]\n"
    "ds_read_b64_tr_b16 a[250:251], v18 offset:12288\n"
    "v_mfma_f32_16x16x32_bf16 v[208:211], a[208:211], a[124:127], v[208:211]\n"
    "ds_read_b64_tr_b16 a[252:253], v19 offset:8192\n"
    "s_waitcnt lgkmcnt(14)\n"
    "v_mfma_f32_16x16x32_bf16 v[160:163], a[224:227], a[128:131], v[160:163]\n"
    "ds_read_b64_tr_b16 a[254:255], v19 offset:12288\n"
    "s_waitcnt lgkmcnt(13)\n"
    "v_mfma_f32_16x16x32_bf16 v[164:167], a[228:231], a[128:131], v[164:167]\n"
    "ds_read_b128 a[136:139], v12 offset:6144\n"
    "s_waitcnt lgkmcnt(12)\n"
    "v_mfma_f32_16x16x32_bf16 v[168:171], a[232:235], a[128:131], v[168:171]\n"
    "ds_read_b128 a[140:143], v12 offset:7168\n"
    "s_waitcnt lgkmcnt(11)\n"
    "v_mfma_f32_16x16x32_bf16 v[172:175], a[236:239], a[128:131], v[172:175]\n"
    "ds_read_b128 a[72:75], v12 offset:8192\n"
    "s_waitcnt lgkmcnt(9)\n"
    "v_mfma_f32_16x16x32_bf16 v[160:163], a[240:243], a[132:135], v[160:163]\n"
    "ds_read_b128 a[76:79], v12 offset:9216\n"
    "s_waitcnt lgkmcnt(8)\n"
    "v_mfma_f32_16x16x32_bf16 v[164:167], a[244:247], a[132:135], v[164:167]\n"
    "ds_read_b128 a[80:83], v12 offset:10240\n"
    "s_waitcnt lgkmcnt(7)\n"
    "v_mfma_f32_16x16x32_bf16 v[168:171], a[248:251], a[132:135], v[168:171]\n"
    "ds_read_b128 a[84:87], v12 offset:11264\n"
    "s_waitcnt lgkmcnt(6)\n"
    "v_mfma_f32_16x16x32_bf16 v[172:175], a[252:255], a[132:135], v[172:175]\n"
    "ds_read_b128 a[88:91], v15 offset:61440\n"
    "s_waitcnt lgkmcnt(6)\n"
    "v_mfma_f32_16x16x32_bf16 v[176:179], a[224:227], a[136:139], v[176:179]\n"
    "ds_read_b128 a[92:95], v15 offset:62464\n"
    "v_mfma_f32_16x16x32_bf16 v[180:183], a[228:231], a[136:139], v[180:183]\n"
    "v_cvt_pk_bf16_f32 v160, v160, v161\n"
    "v_cvt_pk_bf16_f32 v161, v162, v163\n"
    "v_mfma_f32_16x16x32_bf16 v[184:187], a[232:235], a[136:139], v[184:187]\n"
    "v_cvt_pk_bf16_f32 v162, v164, v165\n"
    "v_cvt_pk_bf16_f32 v163, v166, v167\n"
    "v_mfma_f32_16x16x32_bf16 v[188:191], a[236:239], a[136:139], v[188:191]\n"
    "buffer_store_dwordx4 v[160:163], v21, s[60:63], s77 offen offset:0 sc1\n"
    "s_waitcnt lgkmcnt(6)\n"
    "v_mfma_f32_16x16x32_bf16 v[176:179], a[240:243], a[140:143], v[176:179]\n"
    "v_mfma_f32_16x16x32_bf16 v[180:183], a[244:247], a[140:143], v[180:183]\n"
    "v_cvt_pk_bf16_f32 v168, v168, v169\n"
    "v_cvt_pk_bf16_f32 v169, v170, v171\n"
    "v_mfma_f32_16x16x32_bf16 v[184:187], a[248:251], a[140:143], v[184:187]\n"
    "v_cvt_pk_bf16_f32 v170, v172, v173\n"
    "v_cvt_pk_bf16_f32 v171, v174, v175\n"
    "v_mfma_f32_16x16x32_bf16 v[188:191], a[252:255], a[140:143], v[188:191]\n"
    "ds_read_b128 a[120:123], v12 offset:12288\n"
    "buffer_store_dwordx4 v[168:171], v21, s[60:63], s77 offen offset:1024 sc1\n"
    "s_waitcnt lgkmcnt(6)\n"
    "v_mfma_f32_16x16x32_bf16 v[192:195], a[224:227], a[72:75], v[192:195]\n"
    "ds_read_b128 a[124:127], v12 offset:13312\n"
    "v_mfma_f32_16x16x32_bf16 v[196:199], a[228:231], a[72:75], v[196:199]\n"
    "v_cvt_pk_bf16_f32 v176, v176, v177\n"
    "v_cvt_pk_bf16_f32 v177, v178, v179\n"
    "v_mfma_f32_16x16x32_bf16 v[200:203], a[232:235], a[72:75], v[200:203]\n"
    "v_cvt_pk_bf16_f32 v178, v180, v181\n"
    "v_cvt_pk_bf16_f32 v179, v182, v183\n"
    "TC12:\n"
    "s_cbranch_execz TC16\n"
    "v_mfma_f32_16x16x32_bf16 v[204:207], a[236:239], a[72:75], v[204:207]\n"
    "buffer_store_dwordx4 v[176:179], v21, s[60:63], s77 offen offset:2048 sc1\n"
    "s_waitcnt lgkmcnt(6)\n"
    "v_mfma_f32_16x16x32_bf16 v[192:195], a[240:243], a[76:79], v[192:195]\n"
    "v_mfma_f32_16x16x32_bf16 v[196:199], a[244:247], a[76:79], v[196:199]\n"
    "v_cvt_pk_bf16_f32 v184, v184, v185\n"
    "v_cvt_pk_bf16_f32 v185, v186, v187\n"
    "v_mfma_f32_16x16x32_bf16 v[200:203], a[248:251], a[76:79], v[200:203]\n"
    "v_cvt_pk_bf16_f32 v186, v188, v189\n"
    "v_cvt_pk_bf16_f32 v187, v190, v191\n"
    "v_mfma_f32_16x16x32_bf16 v[204:207], a[252:255], a[76:79], v[204:207]\n"
    "ds_read_b128 a[128:131], v15 offset:63488\n"
    "buffer_store_dwordx4 v[184:187], v21, s[60:63], s77 offen offset:3072 sc1\n"
    "s_waitcnt lgkmcnt(6)\n"
    "v_mfma_f32_16x16x32_bf16 v[208:211], a[224:227], a[80:83], v[208:211]\n"
    "ds_read_b128 a[132:135], v15 offset:64512\n"
    "v_mfma_f32_16x16x32_bf16 v[212:215], a[228:231], a[80:83], v[212:215]\n"
    "v_cvt_pk_bf16_f32 v192, v192, v193\n"
    "v_cvt_pk_bf16_f32 v193, v194, v195\n"
    "v_mfma_f32_16x16x32_bf16 v[216:219], a[232:235], a[80:83], v[216:219]\n"
    "v_cvt_pk_bf16_f32 v194, v196, v197\n"
    "v_cvt_pk_bf16_f32 v195, v198, v199\n"
    "v_mfma_f32_16x16x32_bf16 v[220:223], a[236:239], a[80:83], v[220:223]\n"
    "buffer_store_dwordx4 v[192:195], v21, s[60:63], s78 offen offset:0 sc1\n"
    "s_waitcnt lgkmcnt(6)\n"
    "v_mfma_f32_16x16x32_bf16 v[208:211], a[240:243], a[84:87], v[208:211]\n"
    "v_mfma_f32_16x16x32_bf16 v[212:215], a[244:247], a[84:87], v[212:215]\n"
    "v_cvt_pk_bf16_f32 v200, v200, v201\n"
    "v_cvt_pk_bf16_f32 v201, v202, v203\n"
    "v_mfma_f32_16x16x32_bf16 v[216:219], a[248:251], a[84:87], v[216:219]\n"
    "v_cvt_pk_bf16_f32 v202, v204, v205\n"
    "v_cvt_pk_bf16_f32 v203, v206, v207\n"
    "v_mfma_f32_16x16x32_bf16 v[220:223], a[252:255], a[84:87], v[220:223]\n"
    "ds_read_b128 a[136:139], v12 offset:14336\n"
    "buffer_store_dwordx4 v[200:203], v21, s[60:63], s78 offen offset:1024 sc1\n"
    "s_waitcnt lgkmcnt(6)\n"
    "v_mfma_f32_16x16x32_bf16 v[224:227], a[192:195], a[88:91], v[224:227]\n"
    "ds_read_b128 a[140:143], v12 offset:15360\n"
    "v_mfma_f32_16x16x32_bf16 v[228:231], a[196:199], a[88:91], v[228:231]\n"
    "v_cvt_pk_bf16_f32 v208, v208, v209\n"
    "v_cvt_pk_bf16_f32 v209, v210, v211\n"
    "v_mfma_f32_16x16x32_bf16 v[232:235], a[200:203], a[88:91], v[232:235]\n"
    "v_cvt_pk_bf16_f32 v210, v212, v213\n"
    "v_cvt_pk_bf16_f32 v211, v214, v215\n"
    "v_mfma_f32_16x16x32_bf16 v[236:239], a[204:207], a[88:91], v[236:239]\n"
    "buffer_store_dwordx4 v[208:211], v21, s[60:63], s78 offen offset:2048 sc1\n"
    "s_waitcnt lgkmcnt(6)\n"
    "v_mfma_f32_16x16x32_bf16 v[224:227], a[208:211], a[92:95], v[224:227]\n"
    "v_mfma_f32_16x16x32_bf16 v[228:231], a[212:215], a[92:95], v[228:231]\n"
    "v_cvt_pk_bf16_f32 v216, v216, v217\n"
    "v_cvt_pk_bf16_f32 v217, v218, v219\n"
    "v_mfma_f32_16x16x32_bf16 v[232:235], a[216:219], a[92:95], v[232:235]\n"
    "v_cvt_pk_bf16_f32 v218, v220, v221\n"
    "v_cvt_pk_bf16_f32 v219, v222, v223\n"
    "v_mfma_f32_16x16x32_bf16 v[236:239], a[220:223], a[92:95], v[236:239]\n"
    "ds_read_b128 a[72:75], v15 offset:0\n"
    "buffer_store_dwordx4 v[216:219], v21, s[60:63], s78 offen offset:3072 sc1\n"
    "s_waitcnt lgkmcnt(6)\n"
    "v_mfma_f32_16x16x32_bf16 v[224:227], a[224:227], a[120:123], v[224:227]\n"
    "ds_read_b128 a[76:79], v15 offset:2048\n"
    "v_mfma_f32_16x16x32_bf16 v[228:231], a[228:231], a[120:123], v[228:231]\n"
    "v_mfma_f32_16x16x32_bf16 v[232:235], a[232:235], a[120:123], v[232:235]\n"
    "v_mfma_f32_16x16x32_bf16 v[236:239], a[236:239], a[120:123], v[236:239]\n"
    "s_waitcnt lgkmcnt(6)\n"
    "v_mfma_f32_16x16x32_bf16 v[224:227], a[240:243], a[124:127], v[224:227]\n"
    "v_mfma_f32_16x16x32_bf16 v[228:231], a[244:247], a[124:127], v[228:231]\n"
    "v_mfma_f32_16x16x32_bf16 v[232:235], a[248:251], a[124:127], v[232:235]\n"
    "v_mfma_f32_16x16x32_bf16 v[236:239], a[252:255], a[124:127], v[236:239]\n"
    "ds_read_b128 a[80:83], v15 offset:1024\n"
    "s_waitcnt lgkmcnt(6)\n"
    "v_mfma_f32_16x16x32_bf16 v[240:243], a[192:195], a[128:131], v[240:243]\n"
    "ds_read_b128 a[84:87], v15 offset:3072\n"
    "v_mfma_f32_16x16x32_bf16 v[244:247], a[196:199], a[128:131], v[244:247]\n"
    "v_cvt_pk_bf16_f32 v224, v224, v225\n"
    "v_cvt_pk_bf16_f32 v225, v226, v227\n"
    "v_mfma_f32_16x16x32_bf16 v[248:251], a[200:203], a[128:131], v[248:251]\n"
    "v_cvt_pk_bf16_f32 v226, v228, v229\n"
    "v_cvt_pk_bf16_f32 v227, v230, v231\n"
    "v_mfma_f32_16x16x32_bf16 v[252:255], a[204:207], a[128:131], v[252:255]\n"
    "buffer_store_dwordx4 v[224:227], v21, s[60:63], s79 offen offset:0 sc1\n"
    "s_waitcnt lgkmcnt(6)\n"
    "v_mfma_f32_16x16x32_bf16 v[240:243], a[208:211], a[132:135], v[240:243]\n"
    "v_mfma_f32_16x16x32_bf16 v[244:247], a[212:215], a[132:135], v[244:247]\n"
    "v_cvt_pk_bf16_f32 v232, v232, v233\n"
    "v_cvt_pk_bf16_f32 v233, v234, v235\n"
    "v_mfma_f32_16x16x32_bf16 v[248:251], a[216:219], a[132:135], v[248:251]\n"
    "v_cvt_pk_bf16_f32 v234, v236, v237\n"
    "v_cvt_pk_bf16_f32 v235, v238, v239\n"
    "v_mfma_f32_16x16x32_bf16 v[252:255], a[220:223], a[132:135], v[252:255]\n"
    "ds_read_b128 a[88:91], v15 offset:49152\n"
    "buffer_store_dwordx4 v[232:235], v21, s[60:63], s79 offen offset:1024 sc1\n"
    "s_waitcnt lgkmcnt(6)\n"
    "v_mfma_f32_16x16x32_bf16 v[240:243], a[224:227], a[136:139], v[240:243]\n"
    "ds_read_b128 a[92:95], v15 offset:51200\n"
    "v_mfma_f32_16x16x32_bf16 v[244:247], a[228:231], a[136:139], v[244:247]\n"
    "v_mfma_f32_16x16x32_bf16 v[248:251], a[232:235], a[136:139], v[248:251]\n"
    "v_mfma_f32_16x16x32_bf16 v[252:255], a[236:239], a[136:139], v[252:255]\n"
    "s_waitcnt lgkmcnt(6)\n"
    "v_mfma_f32_16x16x32_bf16 v[240:243], a[240:243], a[140:143], v[240:243]\n"
    "v_mfma_f32_16x16x32_bf16 v[244:247], a[244:247], a[140:143], v[244:247]\n"
    "v_mfma_f32_16x16x32_bf16 v[248:251], a[248:251], a[140:143], v[248:251]\n"
    "v_mfma_f32_16x16x32_bf16 v[252:255], a[252:255], a[140:143], v[252:255]\n"
    "ds_read_b128 a[120:123], v15 offset:50176\n"
    "s_waitcnt lgkmcnt(6)\n"
    "v_mfma_f32_16x16x32_bf16 v[128:131], a[160:163], a[72:75], v[128:131]\n"
    "ds_read_b128 a[124:127], v15 offset:52224\n"
    "v_mfma_f32_16x16x32_bf16 v[132:135], a[164:167], a[72:75], v[132:135]\n"
    "v_cvt_pk_bf16_f32 v240, v240, v241\n"
    "v_cvt_pk_bf16_f32 v241, v242, v243\n"
    "v_mfma_f32_16x16x32_bf16 v[136:139], a[168:171], a[72:75], v[136:139]\n"
    "v_cvt_pk_bf16_f32 v242, v244, v245\n"
    "v_cvt_pk_bf16_f32 v243, v246, v247\n"
    "v_mfma_f32_16x16x32_bf16 v[140:143], a[172:175], a[72:75], v[140:143]\n"
    "buffer_store_dwordx4 v[240:243], v21, s[60:63], s79 offen offset:2048 sc1\n"
    "s_waitcnt lgkmcnt(6)\n"
    "v_mfma_f32_16x16x32_bf16 v[156:159], a[172:175], a[76:79], v[156:159]\n"
    "v_mfma_f32_16x16x32_bf16 v[152:155], a[168:171], a[76:79], v[152:155]\n"
    "v_cvt_pk_bf16_f32 v248, v248, v249\n"
    "v_cvt_pk_bf16_f32 v249, v250, v251\n"
    "v_mfma_f32_16x16x32_bf16 v[148:151], a[164:167], a[76:79], v[148:151]\n"
    "v_cvt_pk_bf16_f32 v250, v252, v253\n"
    "v_cvt_pk_bf16_f32 v251, v254, v255\n"
    "v_mfma_f32_16x16x32_bf16 v[144:147], a[160:163], a[76:79], v[144:147]\n"
    "ds_read_b128 a[128:131], v12 offset:0\n"
    "buffer_store_dwordx4 v[248:251], v21, s[60:63], s79 offen offset:3072 sc1\n"
    "s_waitcnt lgkmcnt(6)\n"
    "v_mfma_f32_16x16x32_bf16 v[128:131], a[176:179], a[80:83], v[128:131]\n"
    "ds_read_b128 a[132:135], v12 offset:2048\n"
    "v_mfma_f32_16x16x32_bf16 v[132:135], a[180:183], a[80:83], v[132:135]\n"
    "v_mfma_f32_16x16x32_bf16 v[136:139], a[184:187], a[80:83], v[136:139]\n"
    "v_mfma_f32_16x16x32_bf16 v[140:143], a[188:191], a[80:83], v[140:143]\n"
    "s_waitcnt lgkmcnt(6)\n"
    "v_mfma_f32_16x16x32_bf16 v[156:159], a[188:191], a[84:87], v[156:159]\n"
    "v_mfma_f32_16x16x32_bf16 v[152:155], a[184:187], a[84:87], v[152:155]\n"
    "v_mfma_f32_16x16x32_bf16 v[148:151], a[180:183], a[84:87], v[148:151]\n"
    "v_mfma_f32_16x16x32_bf16 v[144:147], a[176:179], a[84:87], v[144:147]\n"
    "ds_read_b128 a[136:139], v12 offset:1024\n"
    "s_waitcnt lgkmcnt(6)\n"
    "v_mfma_f32_16x16x32_bf16 v[128:131], a[192:195], a[88:91], v[128:131]\n"
    "ds_read_b128 a[140:143], v12 offset:3072\n"
    "v_mfma_f32_16x16x32_bf16 v[132:135], a[196:199], a[88:91], v[132:135]\n"
    "v_mfma_f32_16x16x32_bf16 v[136:139], a[200:203], a[88:91], v[136:139]\n"
    "v_mfma_f32_16x16x32_bf16 v[140:143], a[204:207], a[88:91], v[140:143]\n"
    "s_waitcnt lgkmcnt(6)\n"
    "v_mfma_f32_16x16x32_bf16 v[156:159], a[204:207], a[92:95], v[156:159]\n"
    "v_mfma_f32_16x16x32_bf16 v[152:155], a[200:203], a[92:95], v[152:155]\n"
    "v_mfma_f32_16x16x32_bf16 v[148:151], a[196:199], a[92:95], v[148:151]\n"
    "v_mfma_f32_16x16x32_bf16 v[144:147], a[192:195], a[92:95], v[144:147]\n"
    "s_waitcnt lgkmcnt(5)\n"
    "v_mfma_f32_16x16x32_bf16 v[128:131], a[208:211], a[120:123], v[128:131]\n"
    "v_mfma_f32_16x16x32_bf16 v[132:135], a[212:215], a[120:123], v[132:135]\n"
    "s_mov_b64 exec, 7\n"
    "global_load_dwordx2 v[2:3], v7, s[52:53] sc1\n"
    "s_mov_b64 exec, -1\n"
    "v_mfma_f32_16x16x32_bf16 v[136:139], a[216:219], a[120:123], v[136:139]\n"
    "v_mfma_f32_16x16x32_bf16 v[140:143], a[220:223], a[120:123], v[140:143]\n"
    "s_waitcnt lgkmcnt(4)\n"
    "v_mfma_f32_16x16x32_bf16 v[156:159], a[220:223], a[124:127], v[156:159]\n"
    "v_mfma_f32_16x16x32_bf16 v[152:155], a[216:219], a[124:127], v[152:155]\n"
    "v_mfma_f32_16x16x32_bf16 v[148:151], a[212:215], a[124:127], v[148:151]\n"
    "v_mfma_f32_16x16x32_bf16 v[144:147], a[208:211], a[124:127], v[144:147]\n"
    "s_waitcnt lgkmcnt(3)\n"
    "v_mfma_f32_16x16x32_bf16 v[128:131], a[224:227], a[128:131], v[128:131]\n"
    "v_mfma_f32_16x16x32_bf16 v[132:135], a[228:231], a[128:131], v[132:135]\n"
    "v_mfma_f32_16x16x32_bf16 v[136:139], a[232:235], a[128:131], v[136:139]\n"
    "v_mfma_f32_16x16x32_bf16 v[140:143], a[236:239], a[128:131], v[140:143]\n"
    "s_waitcnt lgkmcnt(2)\n"
    "v_mfma_f32_16x16x32_bf16 v[156:159], a[236:239], a[132:135], v[156:159]\n"
    "v_mfma_f32_16x16x32_bf16 v[152:155], a[232:235], a[132:135], v[152:155]\n"
    "v_mfma_f32_16x16x32_bf16 v[148:151], a[228:231], a[132:135], v[148:151]\n"
    "v_mfma_f32_16x16x32_bf16 v[144:147], a[224:227], a[132:135], v[144:147]\n"
    "s_waitcnt vmcnt(1)\n"
    "s_mov_b64 exec, 7\n"
    "global_store_dwordx2 v6, v[4:5], s[52:53] sc1\n"
    "s_mov_b64 exec, -1\n"
    "s_or_b32 s84, s54, s55\n"
    "s_cbranch_scc0 EP_ld\n"
    "P2H_ret:\n"
    "s_waitcnt lgkmcnt(1)\n"
    "v_mfma_f32_16x16x32_bf16 v[128:131], a[240:243], a[136:139], v[128:131]\n"
    "v_mfma_f32_16x16x32_bf16 v[132:135], a[244:247], a[136:139], v[132:135]\n"
    "v_mfma_f32_16x16x32_bf16 v[136:139], a[248:251], a[136:139], v[136:139]\n"
    "v_mfma_f32_16x16x32_bf16 v[140:143], a[252:255], a[136:139], v[140:143]\n"
    "s_waitcnt lgkmcnt(0)\n"
    "v_mfma_f32_16x16x32_bf16 v[156:159], a[252:255], a[140:143], v[156:159]\n"
    "v_mfma_f32_16x16x32_bf16 v[152:155], a[248:251], a[140:143], v[152:155]\n"
    "v_mfma_f32_16x16x32_bf16 v[148:151], a[244:247], a[140:143], v[148:151]\n"
    "v_mfma_f32_16x16x32_bf16 v[144:147], a[240:243], a[140:143], v[144:147]\n"
    "s_waitcnt vmcnt(1)\n"
    "s_mov_b64 exec, 7\n"
    "v_cmp_eq_u64 vcc, s[8:9], v[2:3]\n"
    "s_nop 0\n"
    "s_and_b32 s87, vcc_lo, s86\n"
    "s_cbranch_scc0 XE_n\n"
    "s_andn2_b32 s86, s86, s87\n"
    "s_mov_b64 exec, -1\n"
    "s_bitcmp1_b32 s87, 0\n"
    "s_cbranch_scc0 XE_1\n"
    "buffer_load_dwordx4 v[160:163], v21, s[60:63], s81 offen offset:0 sc1\n"
    "buffer_load_dwordx4 v[164:167], v21, s[60:63], s81 offen offset:1024 sc1\n"
    "buffer_load_dwordx4 v[168:171], v21, s[60:63], s81 offen offset:2048 sc1\n"
    "buffer_load_dwordx4 v[172:175], v21, s[60:63], s81 offen offset:3072 sc1\n"
    "XE_1:\n"
    "s_bitcmp1_b32 s87, 1\n"
    "s_cbranch_scc0 XE_2\n"
    "buffer_load_dwordx4 v[176:179], v21, s[60:63], s82 offen offset:0 sc1\n"
    "buffer_load_dwordx4 v[180:183], v21, s[60:63], s82 offen offset:1024 sc1\n"
    "buffer_load_dwordx4 v[184:187], v21, s[60:63], s82 offen offset:2048 sc1\n"
    "buffer_load_dwordx4 v[188:191], v21, s[60:63], s82 offen offset:3072 sc1\n"
    "XE_2:\n"
    "s_bitcmp1_b32 s87, 2\n"
    "s_cbranch_scc0 XE_3\n"
    "buffer_load_dwordx4 v[192:195], v21, s[60:63], s83 offen offset:0 sc1\n"
    "buffer_load_dwordx4 v[196:199], v21, s[60:63], s83 offen offset:1024 sc1\n"
    "buffer_load_dwordx4 v[200:203], v21, s[60:63], s83 offen offset:2048 sc1\n"
    "buffer_load_dwordx4 v[204:207], v21, s[60:63], s83 offen offset:3072 sc1\n"
    "XE_3:\n"
    "XE_n:\n"
    "s_mov_b64 exec, -1\n"
    "v_mfma_f32_16x16x32_bf16 v[128:131], a[48:51], v[88:91], v[128:131]\n"
    "v_mfma_f32_16x16x32_bf16 v[132:135], a[52:55], v[88:91], v[132:135]\n"
    "v_mfma_f32_16x16x32_bf16 v[136:139], a[56:59], v[88:91], v[136:139]\n"
    "v_mfma_f32_16x16x32_bf16 v[140:143], a[60:63], v[88:91], v[140:143]\n"
    "v_mfma_f32_16x16x32_bf16 v[156:159], a[60:63], v[92:95], v[156:159]\n"
    "v_mfma_f32_16x16x32_bf16 v[152:155], a[56:59], v[92:95], v[152:155]\n"
    "v_mfma_f32_16x16x32_bf16 v[148:151], a[52:55], v[92:95], v[148:151]\n"
    "v_mfma_f32_16x16x32_bf16 v[144:147], a[48:51], v[92:95], v[144:147]\n"
    "v_mfma_f32_16x16x32_bf16 v[128:131], v[64:67], v[80:83], v[128:131]\n"
    "v_mfma_f32_16x16x32_bf16 v[132:135], v[68:71], v[80:83], v[132:135]\n"
    "v_mfma_f32_16x16x32_bf16 v[136:139], v[72:75], v[80:83], v[136:139]\n"
    "v_mfma_f32_16x16x32_bf16 v[140:143], v[76:79], v[80:83], v[140:143]\n"
    "v_mfma_f32_16x16x32_bf16 v[156:159], v[76:79], v[84:87], v[156:159]\n"
    "v_mfma_f32_16x16x32_bf16 v[152:155], v[72:75], v[84:87], v[152:155]\n"
    "v_mfma_f32_16x16x32_bf16 v[148:151], v[68:71], v[84:87], v[148:151]\n"
    "v_mfma_f32_16x16x32_bf16 v[144:147], v[64:67], v[84:87], v[144:147]\n"
    "v_mfma_f32_16x16x32_bf16 v[128:131], v[104:107], v[120:123], v[128:131]\n"
    "v_mfma_f32_16x16x32_bf16 v[132:135], v[108:111], v[120:123], v[132:135]\n"
    "v_mfma_f32_16x16x32_bf16 v[136:139], v[112:115], v[120:123], v[136:139]\n"
    "v_mfma_f32_16x16x32_bf16 v[140:143], v[116:119], v[120:123], v[140:143]\n"
    "v_mfma_f32_16x16x32_bf16 v[156:159], v[116:119], v[124:127], v[156:159]\n"
    "v_mfma_f32_16x16x32_bf16 v[152:155], v[112:115], v[124:127], v[152:155]\n"
    "v_mfma_f32_16x16x32_bf16 v[148:151], v[108:111], v[124:127], v[148:151]\n"
    "v_mfma_f32_16x16x32_bf16 v[144:147], v[104:107], v[124:127], v[144:147]\n"
    "v_mfma_f32_16x16x32_bf16 v[128:131], v[40:43], v[56:59], v[128:131]\n"
    "v_mfma_f32_16x16x32_bf16 v[132:135], v[44:47], v[56:59], v[132:135]\n"
    "v_mfma_f32_16x16x32_bf16 v[136:139], v[48:51], v[56:59], v[136:139]\n"
    "v_mfma_f32_16x16x32_bf16 v[140:143], v[52:55], v[56:59], v[140:143]\n"
    "v_mfma_f32_16x16x32_bf16 v[156:159], v[52:55], v[60:63], v[156:159]\n"
    "v_mfma_f32_16x16x32_bf16 v[152:155], v[48:51], v[60:63], v[152:155]\n"
    "v_mfma_f32_16x16x32_bf16 v[148:151], v[44:47], v[60:63], v[148:151]\n"
    "v_mfma_f32_16x16x32_bf16 v[144:147], v[40:43], v[60:63], v[144:147]\n"
    "v_mfma_f32_16x16x32_bf16 v[128:131], a[24:27], a[40:43], v[128:131]\n"
    "v_mfma_f32_16x16x32_bf16 v[132:135], a[28:31], a[40:43], v[132:135]\n"
    "v_mfma_f32_16x16x32_bf16 v[136:139], a[32:35], a[40:43], v[136:139]\n"
    "v_mfma_f32_16x16x32_bf16 v[140:143], a[36:39], a[40:43], v[140:143]\n"
    "v_mfma_f32_16x16x32_bf16 v[156:159], a[36:39], a[44:47], v[156:159]\n"
    "v_mfma_f32_16x16x32_bf16 v[152:155], a[32:35], a[44:47], v[152:155]\n"
    "v_mfma_f32_16x16x32_bf16 v[148:151], a[28:31], a[44:47], v[148:151]\n"
    "v_mfma_f32_16x16x32_bf16 v[144:147], a[24:27], a[44:47], v[144:147]\n"
    "v_mfma_f32_16x16x32_bf16 v[128:131], a[0:3], a[16:19], v[128:131]\n"
    "v_mfma_f32_16x16x32_bf16 v[132:135], a[4:7], a[16:19], v[132:135]\n"
    "v_mfma_f32_16x16x32_bf16 v[136:139], a[8:11], a[16:19], v[136:139]\n"
    "v_mfma_f32_16x16x32_bf16 v[140:143], a[12:15], a[16:19], v[140:143]\n"
    "v_mfma_f32_16x16x32_bf16 v[156:159], a[12:15], a[20:23], v[156:159]\n"
    "v_mfma_f32_16x16x32_bf16 v[152:155], a[8:11], a[20:23], v[152:155]\n"
    "v_mfma_f32_16x16x32_bf16 v[148:151], a[4:7], a[20:23], v[148:151]\n"
    "v_mfma_f32_16x16x32_bf16 v[144:147], a[0:3], a[20:23], v[144:147]\n"
    "s_mov_b64 exec, 7\n"
    "global_load_dwordx2 v[2:3], v7, s[52:53] sc1\n"
    "s_mov_b64 exec, -1\n"
    "v_mfma_f32_16x16x32_bf16 v[128:131], a[96:99], a[64:67], v[128:131]\n"
    "v_mfma_f32_16x16x32_bf16 v[132:135], a[100:103], a[64:67], v[132:135]\n"
    "v_mfma_f32_16x16x32_bf16 v[136:139], a[104:107], a[64:67], v[136:139]\n"
    "v_mfma_f32_16x16x32_bf16 v[140:143], a[108:111], a[64:67], v[140:143]\n"
    "v_mfma_f32_16x16x32_bf16 v[156:159], a[108:111], a[68:71], v[156:159]\n"
    "v_mfma_f32_16x16x32_bf16 v[152:155], a[104:107], a[68:71], v[152:155]\n"
    "v_mfma_f32_16x16x32_bf16 v[148:151], a[100:103], a[68:71], v[148:151]\n"
    "v_mfma_f32_16x16x32_bf16 v[144:147], a[96:99], a[68:71], v[144:147]\n"
    "v_mfma_f32_16x16x32_bf16 v[128:131], a[144:147], a[112:115], v[128:131]\n"
    "v_mfma_f32_16x16x32_bf16 v[132:135], a[148:151], a[112:115], v[132:135]\n"
    "v_mfma_f32_16x16x32_bf16 v[136:139], a[152:155], a[112:115], v[136:139]\n"
    "v_mfma_f32_16x16x32_bf16 v[140:143], a[156:159], a[112:115], v[140:143]\n"
    "v_mfma_f32_16x16x32_bf16 v[156:159], a[156:159], a[116:119], v[156:159]\n"
    "v_mfma_f32_16x16x32_bf16 v[152:155], a[152:155], a[116:119], v[152:155]\n"
    "v_mfma_f32_16x16x32_bf16 v[148:151], a[148:151], a[116:119], v[148:151]\n"
    "v_mfma_f32_16x16x32_bf16 v[144:147], a[144:147], a[116:119], v[144:147]\n"
    "s_mov_b64 exec, 7\n"
    "s_cmp_eq_u32 s86, 0\n"
    "s_cbranch_scc1 XP_ok\n"
    "s_mov_b32 s68, 0x40000\n"
    "s_branch XP_w\n"
    "XP:\n"
    "global_load_dwordx2 v[2:3], v7, s[52:53] sc1\n"
    "XP_w:\n"
    "s_waitcnt vmcnt(0)\n"
    "v_cmp_eq_u64 vcc, s[8:9], v[2:3]\n"
    "s_nop 0\n"
    "s_and_b32 s87, vcc_lo, s86\n"
    "s_cbranch_scc0 XP_n\n"
    "s_andn2_b32 s86, s86, s87\n"
    "s_mov_b64 exec, -1\n"
    "s_bitcmp1_b32 s87, 0\n"
    "s_cbranch_scc0 XP_1\n"
    "buffer_load_dwordx4 v[160:163], v21, s[60:63], s81 offen offset:0 sc1\n"
    "buffer_load_dwordx4 v[164:167], v21, s[60:63], s81 offen offset:1024 sc1\n"
    "buffer_load_dwordx4 v[168:171], v21, s[60:63], s81 offen offset:2048 sc1\n"
    "buffer_load_dwordx4 v[172:175], v21, s[60:63], s81 offen offset:3072 sc1\n"
    "XP_1:\n"
    "s_bitcmp1_b32 s87, 1\n"
    "s_cbranch_scc0 XP_2\n"
    "buffer_load_dwordx4 v[176:179], v21, s[60:63], s82 offen offset:0 sc1\n"
    "buffer_load_dwordx4 v[180:183], v21, s[60:63], s82 offen offset:1024 sc1\n"
    "buffer_load_dwordx4 v[184:187], v21, s[60:63], s82 offen offset:2048 sc1\n"
    "buffer_load_dwordx4 v[188:191], v21, s[60:63], s82 offen offset:3072 sc1\n"
    "XP_2:\n"
    "s_bitcmp1_b32 s87, 2\n"
    "s_cbranch_scc0 XP_3\n"
    "buffer_load_dwordx4 v[192:195], v21, s[60:63], s83 offen offset:0 sc1\n"
    "buffer_load_dwordx4 v[196:199], v21, s[60:63], s83 offen offset:1024 sc1\n"
    "buffer_load_dwordx4 v[200:203], v21, s[60:63], s83 offen offset:2048 sc1\n"
    "buffer_load_dwordx4 v[204:207], v21, s[60:63], s83 offen offset:3072 sc1\n"
    "XP_3:\n"
    "s_cmp_eq_u32 s86, 0\n"
    "s_cbranch_scc1 XP_ok\n"
    "s_mov_b64 exec, 7\n"
    "XP_n:\n"
    "s_sleep 1\n"
    "s_sub_u32 s68, s68, 1\n"
    "s_cbranch_scc0 XP\n"
    "XP_ok:\n"
    "s_mov_b64 exec, -1\n"
    "s_waitcnt vmcnt(3)\n"
    "v_mfma_f32_16x16x32_bf16 v[128:131], v[24:27], v[160:163], v[128:131]\n"
    "v_mfma_f32_16x16x32_bf16 v[132:135], v[22:25], v[160:163], v[132:135]\n"
    "v_mfma_f32_16x16x32_bf16 v[128:131], v[24:27], v[176:179], v[128:131]\n"
    "v_mfma_f32_16x16x32_bf16 v[132:135], v[22:25], v[176:179], v[132:135]\n"
    "v_mfma_f32_16x16x32_bf16 v[128:131], v[24:27], v[192:195], v[128:131]\n"
    "v_mfma_f32_16x16x32_bf16 v[132:135], v[22:25], v[192:195], v[132:135]\n"
    "s_waitcnt vmcnt(2)\n"
    "v_mfma_f32_16x16x32_bf16 v[136:139], v[24:27], v[164:167], v[136:139]\n"
    "v_mfma_f32_16x16x32_bf16 v[140:143], v[22:25], v[164:167], v[140:143]\n"
    "v_mfma_f32_16x16x32_bf16 v[136:139], v[24:27], v[180:183], v[136:139]\n"
    "v_mfma_f32_16x16x32_bf16 v[140:143], v[22:25], v[180:183], v[140:143]\n"
    "v_cvt_pk_bf16_f32 v128, v128, v129\n"
    "v_cvt_pk_bf16_f32 v129, v130, v131\n"
    "v_cvt_pk_bf16_f32 v130, v132, v133\n"
    "v_cvt_pk_bf16_f32 v131, v134, v135\n"
    "v_mfma_f32_16x16x32_bf16 v[136:139], v[24:27], v[196:199], v[136:139]\n"
    "s_nop 1\n"
    "v_permlane16_swap_b32 v128, v130\n"
    "v_permlane16_swap_b32 v129, v131\n"
    "s_nop 1\n"
    "global_store_dwordx4 v34, v[128:131], s[50:51] offset:0\n"
    "v_mfma_f32_16x16x32_bf16 v[140:143], v[22:25], v[196:199], v[140:143]\n"
    "s_waitcnt vmcnt(2)\n"
    "v_mfma_f32_16x16x32_bf16 v[144:147], v[24:27], v[168:171], v[144:147]\n"
    "v_mfma_f32_16x16x32_bf16 v[148:151], v[22:25], v[168:171], v[148:151]\n"
    "v_mfma_f32_16x16x32_bf16 v[144:147], v[24:27], v[184:187], v[144:147]\n"
    "v_mfma_f32_16x16x32_bf16 v[148:151], v[22:25], v[184:187], v[148:151]\n"
    "v_cvt_pk_bf16_f32 v136, v136, v137\n"
    "v_cvt_pk_bf16_f32 v137, v138, v139\n"
    "v_cvt_pk_bf16_f32 v138, v140, v141\n"
    "v_cvt_pk_bf16_f32 v139, v142, v143\n"
    "v_mfma_f32_16x16x32_bf16 v[144:147], v[24:27], v[200:203], v[144:147]\n"
    "s_nop 1\n"
    "v_permlane16_swap_b32 v136, v138\n"
    "v_permlane16_swap_b32 v137, v139\n"
    "s_nop 1\n"
    "global_store_dwordx4 v34, v[136:139], s[50:51] offset:64\n"
    "v_mfma_f32_16x16x32_bf16 v[148:151], v[22:25], v[200:203], v[148:151]\n"
    "s_waitcnt vmcnt(2)\n"
    "v_mfma_f32_16x16x32_bf16 v[152:155], v[24:27], v[172:175], v[152:155]\n"
    "v_mfma_f32_16x16x32_bf16 v[156:159], v[22:25], v[172:175], v[156:159]\n"
    "v_mfma_f32_16x16x32_bf16 v[152:155], v[24:27], v[188:191], v[152:155]\n"
    "v_mfma_f32_16x16x32_bf16 v[156:159], v[22:25], v[188:191], v[156:159]\n"
    "v_cvt_pk_bf16_f32 v144, v144, v145\n"
    "v_cvt_pk_bf16_f32 v145, v146, v147\n"
    "v_cvt_pk_bf16_f32 v146, v148, v149\n"
    "v_cvt_pk_bf16_f32 v147, v150, v151\n"
    "v_mfma_f32_16x16x32_bf16 v[152:155], v[24:27], v[204:207], v[152:155]\n"
    "s_nop 1\n"
    "v_permlane16_swap_b32 v144, v146\n"
    "v_permlane16_swap_b32 v145, v147\n"
    "s_nop 1\n"
    "global_store_dwordx4 v35, v[144:147], s[50:51] offset:0\n"
    "v_mfma_f32_16x16x32_bf16 v[156:159], v[22:25], v[204:207], v[156:159]\n"
    "s_nop 15\n"
    "v_cvt_pk_bf16_f32 v152, v152, v153\n"
    "v_cvt_pk_bf16_f32 v153, v154, v155\n"
    "v_cvt_pk_bf16_f32 v154, v156, v157\n"
    "v_cvt_pk_bf16_f32 v155, v158, v159\n"
    "s_nop 1\n"
    "v_permlane16_swap_b32 v152, v154\n"
    "v_permlane16_swap_b32 v153, v155\n"
    "s_nop 1\n"
    "global_store_dwordx4 v35, v[152:155], s[50:51] offset:64\n"
    "s_cmp_eq_u32 s84, 0\n"
    "s_cbranch_scc1 EP_chk\n"
    "EP_ret:\n"
    "s_branch K_end\n"
    "BP_slow:\n"
    "s_mov_b32 s68, 0x40000\n"
    "BP:\n"
    "global_load_dwordx2 v[100:101], v20, s[52:53] sc1\n"
    "s_waitcnt vmcnt(0)\n"
    "v_cmp_ne_u64 vcc, s[8:9], v[100:101]\n"
    "s_cbranch_vccz BP_ok\n"
    "s_sleep 1\n"
    "s_sub_u32 s68, s68, 1\n"
    "s_cbranch_scc0 BP\n"
    "BP_ok:\n"
    "s_branch BP_back\n"
    "HP_slow:\n"
    "s_mov_b32 s68, 0x40000\n"
    "HP:\n"
    "global_load_dwordx2 v[100:101], v98, s[52:53] sc1\n"
    "s_waitcnt vmcnt(0)\n"
    "v_cmp_ne_u64 vcc, s[8:9], v[100:101]\n"
    "s_cbranch_vccz HP_ok\n"
    "s_sleep 1\n"
    "s_sub_u32 s68, s68, 1\n"
    "s_cbranch_scc0 HP\n"
    "HP_ok:\n"
    "s_branch HP_back\n"
    "EP_ld:\n"
    "v_lshlrev_b32 v36, 7, v0\n"
    "v_add_u32 v36, 37773312, v36\n"
    "global_load_dwordx4 v[208:211], v36, s[52:53] offset:0 sc1\n"
    "global_load_dwordx4 v[212:215], v36, s[52:53] offset:16 sc1\n"
    "global_load_dwordx4 v[216:219], v36, s[52:53] offset:32 sc1\n"
    "global_load_dwordx4 v[220:223], v36, s[52:53] offset:48 sc1\n"
    "global_load_dwordx4 v[224:227], v36, s[52:53] offset:64 sc1\n"
    "global_load_dwordx4 v[228:231], v36, s[52:53] offset:80 sc1\n"
    "global_load_dwordx4 v[232:235], v36, s[52:53] offset:96 sc1\n"
    "global_load_dwordx4 v[236:239], v36, s[52:53] offset:112 sc1\n"
    "s_branch P2H_ret\n"
    "EP_chk:\n"
    "s_mov_b64 exec, -1\n"
    "s_mov_b32 s68, 0x40000\n"
    "EP:\n"
    "v_cmp_ne_u64 vcc, s[8:9], v[208:209]\n"
    "v_cmp_ne_u64 s[70:71], s[8:9], v[210:211]\n"
    "s_or_b64 vcc, vcc, s[70:71]\n"
    "v_cmp_ne_u64 s[70:71], s[8:9], v[212:213]\n"
    "s_or_b64 vcc, vcc, s[70:71]\n"
    "v_cmp_ne_u64 s[70:71], s[8:9], v[214:215]\n"
    "s_or_b64 vcc, vcc, s[70:71]\n"
    "v_cmp_ne_u64 s[70:71], s[8:9], v[216:217]\n"
    "s_or_b64 vcc, vcc, s[70:71]\n"
    "v_cmp_ne_u64 s[70:71], s[8:9], v[218:219]\n"
    "s_or_b64 vcc, vcc, s[70:71]\n"
    "v_cmp_ne_u64 s[70:71], s[8:9], v[220:221]\n"
    "s_or_b64 vcc, vcc, s[70:71]\n"
    "v_cmp_ne_u64 s[70:71], s[8:9], v[222:223]\n"
    "s_or_b64 vcc, vcc, s[70:71]\n"
    "v_cmp_ne_u64 s[70:71], s[8:9], v[224:225]\n"
    "s_or_b64 vcc, vcc, s[70:71]\n"
    "v_cmp_ne_u64 s[70:71], s[8:9], v[226:227]\n"
    "s_or_b64 vcc, vcc, s[70:71]\n"
    "v_cmp_ne_u64 s[70:71], s[8:9], v[228:229]\n"
    "s_or_b64 vcc, vcc, s[70:71]\n"
    "v_cmp_ne_u64 s[70:71], s[8:9], v[230:231]\n"
    "s_or_b64 vcc, vcc, s[70:71]\n"
    "v_cmp_ne_u64 s[70:71], s[8:9], v[232:233]\n"
    "s_or_b64 vcc, vcc, s[70:71]\n"
    "v_cmp_ne_u64 s[70:71], s[8:9], v[234:235]\n"
    "s_or_b64 vcc, vcc, s[70:71]\n"
    "v_cmp_ne_u64 s[70:71], s[8:9], v[236:237]\n"
    "s_or_b64 vcc, vcc, s[70:71]\n"
    "v_cmp_ne_u64 s[70:71], s[8:9], v[238:239]\n"
    "s_or_b64 vcc, vcc, s[70:71]\n"
    "s_cbranch_vccz EP_ok\n"
    "global_load_dwordx4 v[208:211], v36, s[52:53] offset:0 sc1\n"
    "global_load_dwordx4 v[212:215], v36, s[52:53] offset:16 sc1\n"
    "global_load_dwordx4 v[216:219], v36, s[52:53] offset:32 sc1\n"
    "global_load_dwordx4 v[220:223], v36, s[52:53] offset:48 sc1\n"
    "global_load_dwordx4 v[224:227], v36, s[52:53] offset:64 sc1\n"
    "global_load_dwordx4 v[228:231], v36, s[52:53] offset:80 sc1\n"
    "global_load_dwordx4 v[232:235], v36, s[52:53] offset:96 sc1\n"
    "global_load_dwordx4 v[236:239], v36, s[52:53] offset:112 sc1\n"
    "s_waitcnt vmcnt(0)\n"
    "s_sub_u32 s68, s68, 1\n"
    "s_cbranch_scc0 EP\n"
    "EP_ok:\n"
    "v_mov_b32 v36, 37781504\n"
    "s_mov_b64 exec, 1\n"
    "global_store_dwordx2 v36, v[4:5], s[52:53] sc1\n"
    "s_mov_b64 exec, -1\n"
    "s_branch EP_ret\n"
    "K_end:\n"
    :
    : "s"(__builtin_amdgcn_kernarg_segment_ptr()), "s"(blockIdx.x), "s"(wave), "s"((unsigned)(size_t)smem)
    :
      "v0","v1","v2","v3","v4","v5","v6","v7","v8","v9","v10","v11","v12","v13","v14","v15","v16","v17","v18","v19","v20","v21","v22","v23","v24","v25",
      "v26","v27","v28","v29","v30","v31","v32","v33","v34","v35","v36","v37","v38","v39","v40","v41","v42","v43","v44","v45","v46","v47","v48","v49","v50",
      "v51","v52","v53","v54","v55","v56","v57","v58","v59","v60","v61","v62","v63","v64","v65","v66","v67","v68","v69","v70","v71","v72","v73","v74","v75",
      "v76","v77","v78","v79","v80","v81","v82","v83","v84","v85","v86","v87","v88","v89","v90","v91","v92","v93","v94","v95","v96","v97","v98","v99","v100",
      "v101","v102","v103","v104","v105","v106","v107","v108","v109","v110","v111","v112","v113","v114","v115","v116","v117","v118","v119","v120","v121",
      "v122","v123","v124","v125","v126","v127","v128","v129","v130","v131","v132","v133","v134","v135","v136","v137","v138","v139","v140","v141","v142",
      "v143","v144","v145","v146","v147","v148","v149","v150","v151","v152","v153","v154","v155","v156","v157","v158","v159","v160","v161","v162","v163",
      "v164","v165","v166","v167","v168","v169","v170","v171","v172","v173","v174","v175","v176","v177","v178","v179","v180","v181","v182","v183","v184",
      "v185","v186","v187","v188","v189","v190","v191","v192","v193","v194","v195","v196","v197","v198","v199","v200","v201","v202","v203","v204","v205",
      "v206","v207","v208","v209","v210","v211","v212","v213","v214","v215","v216","v217","v218","v219","v220","v221","v222","v223","v224","v225","v226",
      "v227","v228","v229","v230","v231","v232","v233","v234","v235","v236","v237","v238","v239","v240","v241","v242","v243","v244","v245","v246","v247",
      "v248","v249","v250","v251","v252","v253","v254","v255","a0","a1","a2","a3","a4","a5","a6","a7","a8","a9","a10","a11","a12","a13","a14","a15","a16",
      "a17","a18","a19","a20","a21","a22","a23","a24","a25","a26","a27","a28","a29","a30","a31","a32","a33","a34","a35","a36","a37","a38","a39","a40","a41",
      "a42","a43","a44","a45","a46","a47","a48","a49","a50","a51","a52","a53","a54","a55","a56","a57","a58","a59","a60","a61","a62","a63","a64","a65","a66",
      "a67","a68","a69","a70","a71","a72","a73","a74","a75","a76","a77","a78","a79","a80","a81","a82","a83","a84","a85","a86","a87","a88","a89","a90","a91",
      "a92","a93","a94","a95","a96","a97","a98","a99","a100","a101","a102","a103","a104","a105","a106","a107","a108","a109","a110","a111","a112","a113",
      "a114","a115","a116","a117","a118","a119","a120","a121","a122","a123","a124","a125","a126","a127","a128","a129","a130","a131","a132","a133","a134",
      "a135","a136","a137","a138","a139","a140","a141","a142","a143","a144","a145","a146","a147","a148","a149","a150","a151","a152","a153","a154","a155",
      "a156","a157","a158","a159","a160","a161","a162","a163","a164","a165","a166","a167","a168","a169","a170","a171","a172","a173","a174","a175","a176",
      "a177","a178","a179","a180","a181","a182","a183","a184","a185","a186","a187","a188","a189","a190","a191","a192","a193","a194","a195","a196","a197",
      "a198","a199","a200","a201","a202","a203","a204","a205","a206","a207","a208","a209","a210","a211","a212","a213","a214","a215","a216","a217","a218",
      "a219","a220","a221","a222","a223","a224","a225","a226","a227","a228","a229","a230","a231","a232","a233","a234","a235","a236","a237","a238","a239",
      "a240","a241","a242","a243","a244","a245","a246","a247","a248","a249","a250","a251","a252","a253","a254","a255","s8","s9","s10","s11","s12","s13",
      "s14","s15","s16","s17","s18","s19","s20","s21","s22","s23","s24","s25","s26","s27","s28","s29","s30","s31","s33","s34","s35","s36","s37","s38","s39",
      "s40","s41","s42","s43","s44","s45","s46","s47","s48","s49","s50","s51","s52","s53","s54","s55","s56","s57","s58","s59","s60","s61","s62","s63","s64",
      "s65","s66","s67","s68","s69","s70","s71","s72","s73","s74","s75","s76","s77","s78","s79","s80","s81","s82","s83","s84","s85","s86","s87","s88","s89",
      "s90","s91","s92","s93","s94","s95","s96","s97","s98","s99","vcc","scc","memory");
}
extern "C" void kernel_launch(void* const* d_in, const int* in_sizes, int n_in, void* d_out, int out_size, void* d_ws, size_t ws_size, hipStream_t stream) {
  fnet_geglu<<<256, 256, 0, stream>>>(d_in[0], d_in[1], d_in[2], d_in[3], d_in[4], d_in[6], d_in[7], d_in[8], d_in[9], d_out, d_ws);
}
```
